```python
import torch

def _softmax(x, axis=-1):
    return torch.softmax(x, dim=axis)
def _gelu(x):
    return torch.nn.functional.gelu(x, approximate="tanh")
def _silu(x):
    return torch.nn.functional.silu(x)
def _sigmoid(x):
    return torch.sigmoid(x)
def _layernorm(x, axis=-1, eps=1e-5):
    m = x.mean(dim=axis, keepdim=True)
    v = x.var(dim=axis, keepdim=True, unbiased=False)
    return (x - m) / torch.sqrt(v + eps)
def _scatter_add(tgt, idx, src, axis=0):
    return tgt.clone().index_add_(axis, idx, src)
def _segment_sum(src, seg, n):
    out = torch.zeros((n,) + src.shape[1:], dtype=src.dtype)
    return out.index_add_(0, seg, src)
def _pad(x, pw):
    flat = tuple(v for pair in reversed(pw) for v in pair)
    return torch.nn.functional.pad(x, flat)

kernel_name = 'gated_mix__ssm_conv_h3__dense_activation_silu__prompt'

def setup_inputs():

    _w = (lambda a: (lambda t: t.to(torch.bfloat16) if t.is_floating_point() else t)(torch.as_tensor(a)))
    def _A_setup():
        B, S, D = 1, 2048, 640
        ks = 4
        return {
            "q": _w((lambda *s: torch.randn(*s, dtype=torch.bfloat16) * 0.05)(B, S, D)),
            "k": _w((lambda *s: torch.randn(*s, dtype=torch.bfloat16) * 0.2)(B, S, D)),
            "v": _w((lambda *s: torch.randn(*s, dtype=torch.bfloat16) * 0.2)(B, S, D)),
            "shift_k": _w((lambda *s: torch.randn(*s, dtype=torch.bfloat16) * 0.2)(D, ks) * 0.5),
            "ssm_k": _w((lambda *s: torch.randn(*s, dtype=torch.bfloat16) * 0.4)(D, S) * (1.0 / S ** 0.5)),
            "ssm_D": _w((lambda *s: torch.randn(*s, dtype=torch.bfloat16) * 0.2)(D)),
        }
    _A_in = _A_setup()

    def _B_setup():
        B, S, D, F = 1, 2048, 640, 1536
        return {
            "x": _w((lambda *s: torch.randn(*s, dtype=torch.bfloat16) * 0.05)(B, S, D)),
            "w1": _w((lambda *s: torch.randn(*s, dtype=torch.bfloat16) * 3.2)(D, F) * (D ** -0.5)),
            "w2": _w((lambda *s: torch.randn(*s, dtype=torch.bfloat16) * 0.05)(F, D) * (F ** -0.5)),
        }
    _B_in = _B_setup()

    return {
        "A_q": _A_in["q"],
        "A_k": _A_in["k"],
        "A_v": _A_in["v"],
        "A_shift_k": _A_in["shift_k"],
        "A_ssm_k": _A_in["ssm_k"],
        "A_ssm_D": _A_in["ssm_D"],
        "B_x": _B_in["x"],
        "B_w1": _B_in["w1"],
        "B_w2": _B_in["w2"],
        "gate_w": _w((lambda *s: torch.randn(*s, dtype=torch.bfloat16) * 0.05)(640) * 16.0),
    }

def reference(A_q, A_k, A_v, A_shift_k, A_ssm_k, A_ssm_D, B_x, B_w1, B_w2, gate_w):
    def _A_ref(q, k, v, shift_k, ssm_k, ssm_D):
        B, S, D = q.shape
        ks = shift_k.shape[-1]
        k_pad = _pad(k, ((0, 0), (ks - 1, 0), (0, 0)))
        k_shift = torch.zeros_like(k)
        for t in range(ks):
            k_shift = k_shift + k_pad[:, t:t + S, :] * shift_k[:, ks - 1 - t][None, None, :]
        kv = k_shift * v
        L = 2 * S
        kv_pad = _pad(kv, ((0, 0), (0, S), (0, 0)))
        kf_pad = _pad(ssm_k, ((0, 0), (0, S)))
        kv_f = (lambda x, axes=(-2,-1): torch.fft.rfft2(x.float(), dim=axes))(kv_pad[:, None, :, :], axes=(1, 2))
        kf_f = (lambda x, axes=(-2,-1): torch.fft.rfft2(x.float(), dim=axes))(kf_pad[:, None, :], axes=(1, 2))
        kf_f = (lambda x, *d: x.permute(*d))(kf_f, 1, 2, 0)
        y_f = kv_f * kf_f[None, :, :, :]
        y = (lambda x, s, axes=(-2,-1): torch.fft.irfft2(x, s=s, dim=axes).to(torch.bfloat16))(y_f, (1, L), axes=(1, 2))[:, 0, :S, :]
        y = y + kv * ssm_D[None, None, :]
        return q * y

    def _B_ref(x, w1, w2):
        h = torch.einsum("bsd,df->bsf", x, w1)
        h = _silu(h)
        out = torch.einsum("bsf,fd->bsd", h, w2)
        return out

    x = A_q
    g = _sigmoid((x * gate_w).sum(-1, keepdims=True))
    a = _A_ref(x, A_k, A_v, A_shift_k, A_ssm_k, A_ssm_D)
    b = _B_ref(x, B_w1, B_w2)
    return g * a + (1 - g) * b
```

```cpp
#include <hip/hip_runtime.h>
#include <hip/hip_bf16.h>
#include <stdint.h>

typedef unsigned short u16;
static constexpr int S = 2048, D = 640, F = 1536;

__device__ __forceinline__ float bf2f(u16 x) { return __uint_as_float(((unsigned)x) << 16); }
__device__ __forceinline__ u16 f2bf(float x) { __hip_bfloat16 b = __float2bfloat16(x); return *(u16*)&b; }
__device__ __forceinline__ float rbf(float x) { return bf2f(f2bf(x)); }

__global__ void k_prep(const u16* q, const u16* k, const u16* v, const u16* shk, const u16* gw, u16* kv, float* g) {
    int s = blockIdx.x;
    __shared__ float red[256];
    float acc = 0.f;
    for (int d = threadIdx.x; d < D; d += 256) {
        float ks = 0.f;
        for (int t = 0; t < 4; ++t) {
            int ss = s - 3 + t;
            float kk = ss >= 0 ? bf2f(k[ss * D + d]) : 0.f;
            ks = rbf(ks + rbf(kk * bf2f(shk[d * 4 + 3 - t])));
        }
        kv[s * D + d] = f2bf(ks * bf2f(v[s * D + d]));
        acc += rbf(bf2f(q[s * D + d]) * bf2f(gw[d]));
    }
    red[threadIdx.x] = acc;
    __syncthreads();
    for (int o = 128; o > 0; o >>= 1) { if (threadIdx.x < o) red[threadIdx.x] += red[threadIdx.x + o]; __syncthreads(); }
    if (threadIdx.x == 0) { float z = rbf(red[0]); g[s] = rbf(1.f / (1.f + __expf(-z))); }
}
__global__ void k_conv(const u16* kv, const u16* h, float* y) {
    int d = blockIdx.x; int s = blockIdx.y * 256 + threadIdx.x;
    __shared__ float sh[S], sx[S];
    for (int i = threadIdx.x; i < S; i += 256) { sh[i] = bf2f(h[d * S + i]); sx[i] = bf2f(kv[i * D + d]); }
    __syncthreads();
    float acc = 0.f;
    for (int j = 0; j <= s; ++j) acc += sx[j] * sh[s - j];
    y[s * D + d] = acc;
}
__global__ void k_gemm1(const u16* x, const u16* w1, u16* hh) {
    int f = blockIdx.x * 256 + threadIdx.x; int s = blockIdx.y;
    float acc = 0.f;
    for (int d = 0; d < D; ++d) acc += bf2f(x[s * D + d]) * bf2f(w1[d * F + f]);
    float hv = rbf(acc);
    hh[s * F + f] = f2bf(hv / (1.f + __expf(-hv)));
}
__global__ void k_gemm2(const u16* hh, const u16* w2, const u16* q, const u16* kv, const float* y, const u16* Dv, const float* g, u16* out) {
    int d = blockIdx.x * 128 + threadIdx.x; int s = blockIdx.y;
    float acc = 0.f;
    for (int f = 0; f < F; ++f) acc += bf2f(hh[s * F + f]) * bf2f(w2[f * D + d]);
    float b = rbf(acc);
    float yy = rbf(y[s * D + d]);
    yy = rbf(yy + rbf(bf2f(kv[s * D + d]) * bf2f(Dv[d])));
    float a = rbf(bf2f(q[s * D + d]) * yy);
    float gg = g[s];
    out[s * D + d] = f2bf(rbf(gg * a) + rbf(rbf(1.f - gg) * b));
}

extern "C" void kernel_launch(void* const* d_in, const int* in_sizes, int n_in, void* d_out, int out_size, void* d_ws, size_t ws_size, hipStream_t stream) {
    const u16* q = (const u16*)d_in[0]; const u16* k = (const u16*)d_in[1]; const u16* v = (const u16*)d_in[2];
    const u16* shk = (const u16*)d_in[3]; const u16* h = (const u16*)d_in[4]; const u16* Dv = (const u16*)d_in[5];
    const u16* w1 = (const u16*)d_in[7]; const u16* w2 = (const u16*)d_in[8]; const u16* gw = (const u16*)d_in[9];
    char* ws = (char*)d_ws;
    u16* kv = (u16*)ws; float* g = (float*)(ws + (4 << 20)); float* y = (float*)(ws + (8 << 20)); u16* hh = (u16*)(ws + (16 << 20));
    k_prep<<<S, 256, 0, stream>>>(q, k, v, shk, gw, kv, g);
    k_conv<<<dim3(D, S / 256), 256, 0, stream>>>(kv, h, y);
    k_gemm1<<<dim3(F / 256, S), 256, 0, stream>>>(q, w1, hh);
    k_gemm2<<<dim3(D / 128, S), 128, 0, stream>>>(hh, w2, q, kv, y, Dv, g, (u16*)d_out);
}
```

```cpp
#include <hip/hip_runtime.h>
#include <stdint.h>

typedef unsigned short u16;
typedef unsigned v4u __attribute__((ext_vector_type(4)));
typedef unsigned v2u __attribute__((ext_vector_type(2)));
typedef float f32x4 __attribute__((ext_vector_type(4)));
typedef float f32x2 __attribute__((ext_vector_type(2)));
typedef short bf16x8 __attribute__((ext_vector_type(8)));
typedef short s16x4 __attribute__((ext_vector_type(4)));
typedef __bf16 bf16x2_t __attribute__((ext_vector_type(2)));
#define LAS __attribute__((address_space(3)))
#define GAS __attribute__((address_space(1)))

constexpr int S = 2048, D = 640, F = 1536;
constexpr int NTHR = 512;
constexpr size_t WS_H = 0;
constexpr size_t WS_KV = WS_H + (size_t)S * F * 2;
constexpr size_t WS_Y = WS_KV + (size_t)S * D * 2;
constexpr size_t WS_G = WS_Y + (size_t)S * D * 2;

struct Params {
    const u16 *q, *k, *v, *shk, *ssm, *Dv, *w1, *w2, *gw;
    u16* out;
    char* ws;
};

__device__ __forceinline__ float bf_lo(unsigned u) { return __uint_as_float(u << 16); }
__device__ __forceinline__ float bf_hi(unsigned u) { return __uint_as_float(u & 0xffff0000u); }
__device__ __forceinline__ unsigned pk_bf16(float lo, float hi) { f32x2 v = {lo, hi}; return __builtin_bit_cast(unsigned, __builtin_convertvector(v, bf16x2_t)); }
__device__ __forceinline__ float rbf(float x) { return bf_lo(pk_bf16(x, x)); }
__device__ __forceinline__ f32x4 mfma16(v4u a, v4u b, f32x4 c) { return __builtin_amdgcn_mfma_f32_16x16x32_bf16(__builtin_bit_cast(bf16x8, a), __builtin_bit_cast(bf16x8, b), c, 0, 0, 0); }
__device__ __forceinline__ v2u tr_read(LAS char* p) { return __builtin_bit_cast(v2u, __builtin_amdgcn_ds_read_tr16_b64_v4i16((LAS s16x4*)p)); }
__device__ __forceinline__ v4u lds_rd16(LAS char* p) { return *(LAS v4u*)p; }
__device__ __forceinline__ void glds16(unsigned voff, const void* sbase, unsigned lds_dst) {
    asm volatile("s_mov_b32 m0, %2\n\ts_nop 0\n\tglobal_load_lds_dwordx4 %0, %1" :: "v"(voff), "s"(sbase), "s"(lds_dst) : "memory");
}
#define WAIT_VM(n) asm volatile("s_waitcnt vmcnt(%0)" :: "n"(n) : "memory")
#define WAIT_LGKM0() asm volatile("s_waitcnt lgkmcnt(0)" ::: "memory")
#define BAR() do { asm volatile("" ::: "memory"); __builtin_amdgcn_s_barrier(); asm volatile("" ::: "memory"); } while (0)
#define SCHED() __builtin_amdgcn_sched_barrier(0)

constexpr int G1_NS = 4, G1_SLOT = 32768, G1_NST = D / 64;
__device__ __forceinline__ void g1_loader(const Params& p, int panel, int ct, int lw, int lane) {
    unsigned vx[4], vw[4];
#pragma unroll
    for (int i = 0; i < 4; ++i) {
        const int r = lane >> 3, c = lane & 7, row = 32 * lw + 8 * i + r;
        vx[i] = (unsigned)(row * (D * 2) + ((c ^ ((row >> 1) & 7)) * 16));
        const int r2 = lane >> 4, c2 = lane & 15, k = 16 * lw + 4 * i + r2, phi = (k & 3) | (((k >> 3) & 1) << 2);
        vw[i] = (unsigned)(k * (F * 2) + ((c2 ^ (phi << 1)) * 16));
    }
    const char* xb = (const char*)p.q + (size_t)panel * 128 * D * 2;
    const char* wb = (const char*)p.w1 + (size_t)ct * 256;
    auto issue = [&](int st) {
        const unsigned slot = (unsigned)(st % G1_NS) * G1_SLOT;
        const char* xs = xb + st * 128; const char* wsrc = wb + (size_t)st * 64 * F * 2;
#pragma unroll
        for (int i = 0; i < 4; ++i) glds16(vx[i], xs, slot + (32 * lw + 8 * i) * 128);
#pragma unroll
        for (int i = 0; i < 4; ++i) glds16(vw[i], wsrc, slot + 16384 + (16 * lw + 4 * i) * 256);
    };
#pragma unroll
    for (int st = 0; st < G1_NS - 1; ++st) issue(st);
#pragma unroll
    for (int i = 0; i < G1_NST; ++i) {
        constexpr int dummy = 0; (void)dummy;
        const int younger = (G1_NST - 1 - i) < (G1_NS - 2) ? (G1_NST - 1 - i) : (G1_NS - 2);
        if (younger == 2) WAIT_VM(16); else if (younger == 1) WAIT_VM(8); else WAIT_VM(0);
        BAR();
        if (i + G1_NS - 1 < G1_NST) issue(i + G1_NS - 1);
    }
}
__device__ __forceinline__ void g1_compute(const Params& p, LAS char* L, int panel, int ct, int w, int lane) {
    const int wf = w & 1, wsx = w >> 1;
    const int g = lane >> 4, q = (lane & 15) >> 2, pp = lane & 3, s = lane & 15;
    const int phi = q | ((g & 1) << 2);
    unsigned aw[4], ax[2];
#pragma unroll
    for (int ft = 0; ft < 4; ++ft) aw[ft] = 16384 + (8 * g + q) * 256 + (((4 * wf + ft) ^ phi) * 32) + 8 * pp;
#pragma unroll
    for (int ks = 0; ks < 2; ++ks) ax[ks] = (64 * wsx + s) * 128 + (((4 * ks + g) ^ ((s >> 1) & 7)) * 16);
    f32x4 acc[4][4];
#pragma unroll
    for (int a = 0; a < 4; ++a)
#pragma unroll
        for (int b = 0; b < 4; ++b) acc[a][b] = (f32x4){0.f, 0.f, 0.f, 0.f};
    v4u A[2][4], B[2][4];
    auto rd = [&](int set, int st, int ks) {
        LAS char* base = L + (st % G1_NS) * G1_SLOT;
#pragma unroll
        for (int ft = 0; ft < 4; ++ft) {
            const v2u lo = tr_read(base + aw[ft] + ks * 8192), hi = tr_read(base + aw[ft] + ks * 8192 + 1024);
            A[set][ft] = (v4u){lo.x, lo.y, hi.x, hi.y};
        }
#pragma unroll
        for (int sx = 0; sx < 4; ++sx) B[set][sx] = lds_rd16(base + ax[ks] + sx * 2048);
    };
    auto mm = [&](int set) {
#pragma unroll
        for (int ft = 0; ft < 4; ++ft)
#pragma unroll
            for (int sx = 0; sx < 4; ++sx) acc[ft][sx] = mfma16(A[set][ft], B[set][sx], acc[ft][sx]);
    };
#pragma unroll
    for (int i = 0; i < G1_NST; ++i) {
        BAR();
        rd(0, i, 0);
        SCHED();
        if (i > 0) mm(1);
        SCHED();
        rd(1, i, 1);
        SCHED();
        mm(0);
        SCHED();
        WAIT_LGKM0();
    }
    mm(1);
    char* hb = p.ws + WS_H;
#pragma unroll
    for (int sx = 0; sx < 4; ++sx) {
        const int stg = panel * 8 + wsx * 4 + sx;
#pragma unroll
        for (int u = 0; u < 2; ++u) {
            const int ksg = ct * 4 + wf * 2 + u;
            unsigned o[4];
#pragma unroll
            for (int t = 0; t < 2; ++t) {
                const f32x4 a = acc[2 * u + t][sx];
                float r[4];
#pragma unroll
                for (int e = 0; e < 4; e += 2) {
                    const unsigned hb2 = pk_bf16(a[e], a[e + 1]);
                    const float h0 = bf_lo(hb2), h1 = bf_hi(hb2);
                    r[e] = h0 * __builtin_amdgcn_rcpf(1.f + __builtin_amdgcn_exp2f(h0 * -1.44269504f));
                    r[e + 1] = h1 * __builtin_amdgcn_rcpf(1.f + __builtin_amdgcn_exp2f(h1 * -1.44269504f));
                }
                o[2 * t] = pk_bf16(r[0], r[1]); o[2 * t + 1] = pk_bf16(r[2], r[3]);
            }
            *(GAS v4u*)(hb + ((size_t)(stg * 48 + ksg) * 64 + lane) * 16) = (v4u){o[0], o[1], o[2], o[3]};
        }
    }
}

__device__ __forceinline__ void prepass(const Params& p, LAS char* L, int wg, int tid) {
    const int s0 = 64 * (wg >> 1), c0 = 320 * (wg & 1);
    const int lane = tid & 63, w = tid >> 6;
    {
        const int row = s0 + 32 * (wg & 1) + 4 * w + (lane >> 4), i = lane & 15;
        float acc = 0.f;
#pragma unroll
        for (int j = 0; j < 5; ++j) {
            const v4u a = *(const GAS v4u*)((const char*)p.q + (size_t)row * (D * 2) + (i + 16 * j) * 16);
            const v4u b = *(const GAS v4u*)((const char*)p.gw + (i + 16 * j) * 16);
#pragma unroll
            for (int e = 0; e < 4; ++e) {
                const unsigned pr = pk_bf16(bf_lo(a[e]) * bf_lo(b[e]), bf_hi(a[e]) * bf_hi(b[e]));
                acc += bf_lo(pr) + bf_hi(pr);
            }
        }
#pragma unroll
        for (int m = 8; m >= 1; m >>= 1) acc += __shfl_xor(acc, m);
        if (i == 0) {
            const float z = rbf(acc);
            ((GAS float*)(p.ws + WS_G))[row] = rbf(__builtin_amdgcn_rcpf(1.f + __builtin_amdgcn_exp2f(z * -1.44269504f)));
        }
    }
    if (tid < 320) {
        const int cc = tid % 40, rg = tid / 40;
        const int ch = c0 + 8 * cc, r0 = s0 + 8 * rg;
        v4u kk[11], vv[8];
#pragma unroll
        for (int j = 0; j < 11; ++j) {
            const int row = r0 - 3 + j;
            kk[j] = row >= 0 ? *(const GAS v4u*)((const char*)p.k + (size_t)row * (D * 2) + ch * 2) : (v4u){0u, 0u, 0u, 0u};
        }
#pragma unroll
        for (int j = 0; j < 8; ++j) vv[j] = *(const GAS v4u*)((const char*)p.v + (size_t)(r0 + j) * (D * 2) + ch * 2);
        v4u sw[4];
#pragma unroll
        for (int j = 0; j < 4; ++j) sw[j] = *(const GAS v4u*)((const char*)p.shk + (size_t)ch * 8 + j * 16);
#pragma unroll
        for (int c = 0; c < 8; ++c) {
            const unsigned w01 = sw[c >> 1][(c & 1) * 2], w23 = sw[c >> 1][(c & 1) * 2 + 1];
            const float w0 = bf_lo(w01), w1 = bf_hi(w01), w2 = bf_lo(w23), w3 = bf_hi(w23);
            float kf[11];
#pragma unroll
            for (int j = 0; j < 11; ++j) kf[j] = (c & 1) ? bf_hi(kk[j][c >> 1]) : bf_lo(kk[j][c >> 1]);
            float o[8];
#pragma unroll
            for (int j = 0; j < 8; ++j) {
                const float ksh = rbf(kf[j] * w3 + kf[j + 1] * w2 + kf[j + 2] * w1 + kf[j + 3] * w0);
                const float vf = (c & 1) ? bf_hi(vv[j][c >> 1]) : bf_lo(vv[j][c >> 1]);
                o[j] = ksh * vf;
            }
            const v4u pk = {pk_bf16(o[0], o[1]), pk_bf16(o[2], o[3]), pk_bf16(o[4], o[5]), pk_bf16(o[6], o[7])};
            *(LAS v4u*)(L + (8 * cc + c) * 128 + ((rg ^ (cc & 7)) * 16)) = pk;
        }
    }
    __syncthreads();
#pragma unroll
    for (int j = 0; j < 5; ++j) {
        const int n = tid + NTHR * j, ch = n >> 3, pc = n & 7;
        const v4u val = *(LAS v4u*)(L + ch * 128 + ((pc ^ ((ch >> 3) & 7)) * 16));
        *(GAS v4u*)(p.ws + WS_KV + (size_t)(c0 + ch) * (S * 2) + s0 * 2 + pc * 16) = val;
    }
}

constexpr int CV_COPY = 4160, CV_X = 4 * CV_COPY, CV_WAVE = CV_X + 4608;
__device__ __forceinline__ void conv_wave(const Params& p, LAS char* Lw, unsigned ldsoff, int ch, int lane) {
    const char* hsrc = (const char*)p.ssm + (size_t)ch * (S * 2);
    const char* xsrc = p.ws + WS_KV + (size_t)ch * (S * 2);
#pragma unroll
    for (int j = 0; j < 4; ++j) glds16(lane * 16, xsrc + j * 1024, ldsoff + CV_X + 480 + j * 1024);
#pragma unroll
    for (int qq = 0; qq < 4; ++qq) {
#pragma unroll
        for (int j = 0; j < 4; ++j) {
            if (j == 0 && qq > 0) { if (lane > 0) glds16(lane * 16 - 2 * qq, hsrc, ldsoff + qq * CV_COPY + 32); }
            else glds16(lane * 16, hsrc + j * 1024 - 2 * qq, ldsoff + qq * CV_COPY + 32 + j * 1024);
        }
    }
    const v4u h0 = *(const GAS v4u*)hsrc;
    const float Dd = bf_lo((unsigned)p.Dv[ch]);
    if (lane < 30) *(LAS v4u*)(Lw + CV_X + lane * 16) = (v4u){0u, 0u, 0u, 0u};
    if (lane < 8) *(LAS v4u*)(Lw + (lane >> 1) * CV_COPY + (lane & 1) * 16) = (v4u){0u, 0u, 0u, 0u};
    if (lane >= 1 && lane < 4) {
        v4u v;
        if (lane == 1) v = (v4u){h0.x << 16, (h0.x >> 16) | (h0.y << 16), (h0.y >> 16) | (h0.z << 16), (h0.z >> 16) | (h0.w << 16)};
        else if (lane == 2) v = (v4u){0u, h0.x, h0.y, h0.z};
        else v = (v4u){0u, h0.x << 16, (h0.x >> 16) | (h0.y << 16), (h0.y >> 16) | (h0.z << 16)};
        *(LAS v4u*)(Lw + lane * CV_COPY + 32) = v;
    }
    WAIT_VM(0);
    WAIT_LGKM0();
    const int g = lane >> 4, q = (lane & 15) >> 2, pp = lane & 3, n = lane & 15;
    LAS char* la = Lw + q * CV_COPY + 32 - 16 * g + 8 * pp + 24;
    LAS char* lb = Lw + CV_X + 32 * n + 16 * g;
    f32x4 acc[8];
#pragma unroll
    for (int t = 0; t < 8; ++t) acc[t] = (f32x4){0.f, 0.f, 0.f, 0.f};
    v4u A[2][8], B[2][8];
    auto rd = [&](int set, int r) {
#pragma unroll
        for (int i = 0; i < 8; ++i) {
            const v2u lo = tr_read(la + 32 * (r - 1) + 512 * i + 8), hi = tr_read(la + 32 * (r - 1) + 512 * i);
            A[set][i] = (v4u){lo.x, lo.y, hi.x, hi.y};
            B[set][i] = lds_rd16(lb + 480 - 32 * r + 512 * i);
        }
    };
    auto mm = [&](int set) {
#pragma unroll
        for (int l = 0; l < 8; ++l)
#pragma unroll
            for (int i = 0; i + l < 8; ++i) acc[i + l] = mfma16(A[set][i], B[set][l], acc[i + l]);
    };
    rd(0, 1);
#pragma unroll
    for (int rr = 0; rr < 8; ++rr) {
        SCHED();
        if (rr < 7) rd((rr + 1) & 1, 2 * rr + 3);
        SCHED();
        mm(rr & 1);
    }
    WAIT_LGKM0();
#pragma unroll
    for (int t = 0; t < 8; ++t) {
        const int sidx = 256 * t + 16 * n + 4 * g;
        const v2u xv = *(LAS v2u*)(Lw + CV_X + 480 + 2 * sidx);
        const unsigned y01 = pk_bf16(acc[t][0], acc[t][1]), y23 = pk_bf16(acc[t][2], acc[t][3]);
        const unsigned d01 = pk_bf16(bf_lo(xv.x) * Dd, bf_hi(xv.x) * Dd), d23 = pk_bf16(bf_lo(xv.y) * Dd, bf_hi(xv.y) * Dd);
        const v2u o = {pk_bf16(bf_lo(y01) + bf_lo(d01), bf_hi(y01) + bf_hi(d01)), pk_bf16(bf_lo(y23) + bf_lo(d23), bf_hi(y23) + bf_hi(d23))};
        *(LAS v2u*)(Lw + 2 * sidx) = o;
    }
    WAIT_LGKM0();
    char* ydst = p.ws + WS_Y + (size_t)ch * (S * 2);
#pragma unroll
    for (int j = 0; j < 4; ++j) {
        const v4u val = *(LAS v4u*)(Lw + j * 1024 + lane * 16);
        *(GAS v4u*)(ydst + j * 1024 + lane * 16) = val;
    }
}

constexpr int G2_NS = 8, G2_SLOT = 8192, G2_NST = F / 64, G2_Q = G2_NS * G2_SLOT, G2_Y = G2_Q + 16384, G2_G = G2_Y + 16384, G2_HD = 4;
__device__ __forceinline__ void g2_loader(const Params& p, int panel, int ct, int lw, int lane) {
    unsigned vw[2];
#pragma unroll
    for (int i = 0; i < 2; ++i) {
        const int r = lane >> 3, c = lane & 7, k = 16 * lw + 8 * i + r;
        vw[i] = (unsigned)(k * (D * 2) + ((c ^ (((k >> 1) & 3) << 1)) * 16));
    }
    const char* wb = (const char*)p.w2 + ct * 128;
    auto issue = [&](int st) {
        const unsigned slot = (unsigned)(st % G2_NS) * G2_SLOT;
        const char* src = wb + (size_t)st * 64 * D * 2;
#pragma unroll
        for (int i = 0; i < 2; ++i) glds16(vw[i], src, slot + (16 * lw + 8 * i) * 128);
    };
#pragma unroll
    for (int st = 0; st < G2_NS - 1; ++st) issue(st);
    auto issue_qy = [&]() {
#pragma unroll
        for (int i = 0; i < 4; ++i) {
            const int r = lane >> 3, c = lane & 7, row = 32 * lw + 8 * i + r;
            glds16((unsigned)(row * (D * 2) + ((c ^ ((row >> 1) & 7)) * 16)), (const char*)p.q + (size_t)panel * 128 * D * 2 + ct * 128, G2_Q + (32 * lw + 8 * i) * 128);
        }
#pragma unroll
        for (int i = 0; i < 4; ++i) {
            const int r = lane >> 4, c = lane & 15, d = 16 * lw + 4 * i + r;
            glds16((unsigned)(d * (S * 2) + ((c ^ ((d & 7) << 1)) * 16)), p.ws + WS_Y + (size_t)ct * 64 * S * 2 + panel * 256, G2_Y + (16 * lw + 4 * i) * 256);
        }
        if (lane < 8) glds16(lane * 16, p.ws + WS_G + panel * 512 + lw * 128, G2_G + lw * 128);
    };
#pragma unroll
    for (int i = 0; i < G2_NST; ++i) {
        const int younger = (G2_NST - 1 - i) < (G2_NS - 2) ? (G2_NST - 1 - i) : (G2_NS - 2);
        if (i <= 17) WAIT_VM(12);
        else if (i == 18) WAIT_VM(19);
        else if (i == 19) WAIT_VM(17);
        else if (i == 20) WAIT_VM(15);
        else if (i == 21) WAIT_VM(13);
        else if (i == 22) WAIT_VM(11);
        else WAIT_VM(9);
        (void)younger;
        BAR();
        if (i + G2_NS - 1 < G2_NST) issue(i + G2_NS - 1);
        if (i == 17) issue_qy();
    }
    WAIT_VM(0);
    BAR();
}
__device__ __forceinline__ void g2_compute(const Params& p, LAS char* L, int panel, int ct, int w, int lane) {
    const int g = lane >> 4, q = (lane & 15) >> 2, pp = lane & 3, s = lane & 15;
    const int sw = ((g & 1) << 1) | (q >> 1);
    unsigned aw[4];
#pragma unroll
    for (int dt = 0; dt < 4; ++dt) aw[dt] = (4 * g + q) * 128 + ((dt ^ sw) * 32) + 8 * pp;
    const GAS v4u* hb = (const GAS v4u*)(p.ws + WS_H) + (size_t)(panel * 8 + 2 * w) * 48 * 64 + lane;
    f32x4 acc[4][2];
#pragma unroll
    for (int a = 0; a < 4; ++a) { acc[a][0] = (f32x4){0.f, 0.f, 0.f, 0.f}; acc[a][1] = (f32x4){0.f, 0.f, 0.f, 0.f}; }
    v4u H[G2_HD][2][2];
    auto ldh = [&](int st) {
#pragma unroll
        for (int ks = 0; ks < 2; ++ks)
#pragma unroll
            for (int sx = 0; sx < 2; ++sx) H[st % G2_HD][ks][sx] = hb[(sx * 48 + 2 * st + ks) * 64];
    };
#pragma unroll
    for (int st = 0; st < G2_HD; ++st) ldh(st);
    SCHED();
    v4u A[2][4];
    auto rd = [&](int set, int st, int ks) {
        LAS char* base = L + (st % G2_NS) * G2_SLOT + ks * 4096;
#pragma unroll
        for (int dt = 0; dt < 4; ++dt) {
            const v2u lo = tr_read(base + aw[dt]), hi = tr_read(base + aw[dt] + 2048);
            A[set][dt] = (v4u){lo.x, lo.y, hi.x, hi.y};
        }
    };
    auto mm = [&](int set, int st, int ks) {
#pragma unroll
        for (int dt = 0; dt < 4; ++dt)
#pragma unroll
            for (int sx = 0; sx < 2; ++sx) acc[dt][sx] = mfma16(A[set][dt], H[st % G2_HD][ks][sx], acc[dt][sx]);
    };
#pragma unroll
    for (int i = 0; i < G2_NST; ++i) {
        BAR();
        rd(0, i, 0);
        SCHED();
        if (i > 0) { mm(1, i - 1, 1); SCHED(); if (i - 1 + G2_HD < G2_NST) ldh(i - 1 + G2_HD); }
        SCHED();
        rd(1, i, 1);
        SCHED();
        mm(0, i, 0);
        SCHED();
        WAIT_LGKM0();
    }
    mm(1, G2_NST - 1, 1);
    BAR();
#pragma unroll
    for (int sx = 0; sx < 2; ++sx) {
        const int row = 32 * w + 16 * sx + s;
        const float gg = *(LAS float*)(L + G2_G + row * 4);
        const float omg = rbf(1.f - gg);
#pragma unroll
        for (int dt = 0; dt < 4; ++dt) {
            const int d = 16 * dt + 4 * g + q;
            const v2u yv = tr_read(L + G2_Y + d * 256 + (((2 * w + sx) ^ (d & 7)) * 32) + 8 * pp);
            LAS v2u* qp = (LAS v2u*)(L + G2_Q + row * 128 + (((2 * dt + (g >> 1)) ^ ((row >> 1) & 7)) * 16) + 8 * (g & 1));
            const v2u qv = *qp;
            const f32x4 a = acc[dt][sx];
            const unsigned b01 = pk_bf16(a[0], a[1]), b23 = pk_bf16(a[2], a[3]);
            const unsigned a01 = pk_bf16(bf_lo(qv.x) * bf_lo(yv.x), bf_hi(qv.x) * bf_hi(yv.x)), a23 = pk_bf16(bf_lo(qv.y) * bf_lo(yv.y), bf_hi(qv.y) * bf_hi(yv.y));
            const unsigned ga01 = pk_bf16(gg * bf_lo(a01), gg * bf_hi(a01)), ga23 = pk_bf16(gg * bf_lo(a23), gg * bf_hi(a23));
            const unsigned gb01 = pk_bf16(omg * bf_lo(b01), omg * bf_hi(b01)), gb23 = pk_bf16(omg * bf_lo(b23), omg * bf_hi(b23));
            *qp = (v2u){pk_bf16(bf_lo(ga01) + bf_lo(gb01), bf_hi(ga01) + bf_hi(gb01)), pk_bf16(bf_lo(ga23) + bf_lo(gb23), bf_hi(ga23) + bf_hi(gb23))};
        }
    }
    WAIT_LGKM0();
#pragma unroll
    for (int i = 0; i < 4; ++i) {
        const int row = 32 * w + 8 * i + (lane >> 3), c = lane & 7;
        const v4u val = *(LAS v4u*)(L + G2_Q + row * 128 + c * 16);
        *(GAS v4u*)((char*)p.out + (size_t)(panel * 128 + row) * (D * 2) + ct * 128 + ((c ^ ((row >> 1) & 7)) * 16)) = val;
    }
}

__global__ void __launch_bounds__(NTHR, 2) k_stage1(Params p) {
    extern __shared__ __attribute__((aligned(16))) char smem[];
    LAS char* L = (LAS char*)smem;
    const int tid = threadIdx.x, lane = tid & 63, w = __builtin_amdgcn_readfirstlane(tid >> 6);
    const int x = blockIdx.x & 7, idx = blockIdx.x >> 3;
    if (idx < 24) {
        const int panel = 2 * x + idx / 12, ct = idx % 12;
        if (w >= 4) g1_loader(p, panel, ct, w - 4, lane);
        else g1_compute(p, L, panel, ct, w, lane);
    } else {
        prepass(p, L, x * 8 + (idx - 24), tid);
    }
}
__global__ void __launch_bounds__(NTHR, 2) k_conv(Params p) {
    extern __shared__ __attribute__((aligned(16))) char smem[];
    LAS char* L = (LAS char*)smem;
    const int tid = threadIdx.x, lane = tid & 63, w = __builtin_amdgcn_readfirstlane(tid >> 6);
    const int cw = blockIdx.x;
    const int ch0 = (20 * cw) / 3, ch1 = (20 * (cw + 1)) / 3;
    if (ch0 + w < ch1) conv_wave(p, L + w * CV_WAVE, (unsigned)(w * CV_WAVE), ch0 + w, lane);
}
__global__ void __launch_bounds__(NTHR, 2) k_gemm2(Params p) {
    extern __shared__ __attribute__((aligned(16))) char smem[];
    LAS char* L = (LAS char*)smem;
    const int tid = threadIdx.x, lane = tid & 63, w = __builtin_amdgcn_readfirstlane(tid >> 6);
    const int x = blockIdx.x & 7, idx = blockIdx.x >> 3;
    const int panel = 2 * x + idx / 10, ct = idx % 10;
    if (w >= 4) g2_loader(p, panel, ct, w - 4, lane);
    else g2_compute(p, L, panel, ct, w, lane);
}

extern "C" void kernel_launch(void* const* d_in, const int* in_sizes, int n_in, void* d_out, int out_size, void* d_ws, size_t ws_size, hipStream_t stream) {
    Params p;
    p.q = (const u16*)d_in[0]; p.k = (const u16*)d_in[1]; p.v = (const u16*)d_in[2]; p.shk = (const u16*)d_in[3];
    p.ssm = (const u16*)d_in[4]; p.Dv = (const u16*)d_in[5]; p.w1 = (const u16*)d_in[7]; p.w2 = (const u16*)d_in[8]; p.gw = (const u16*)d_in[9];
    p.out = (u16*)d_out; p.ws = (char*)d_ws;
    constexpr int LDS1 = G1_NS * G1_SLOT, LDSC = 7 * CV_WAVE, LDS2 = G2_G + 512;
    (void)hipFuncSetAttribute((const void*)k_stage1, hipFuncAttributeMaxDynamicSharedMemorySize, LDS1);
    (void)hipFuncSetAttribute((const void*)k_conv, hipFuncAttributeMaxDynamicSharedMemorySize, LDSC);
    (void)hipFuncSetAttribute((const void*)k_gemm2, hipFuncAttributeMaxDynamicSharedMemorySize, LDS2);
    k_stage1<<<256, NTHR, LDS1, stream>>>(p);
    k_conv<<<96, NTHR, LDSC, stream>>>(p);
    k_gemm2<<<160, NTHR, LDS2, stream>>>(p);
}
```

```cpp
#include <hip/hip_runtime.h>
#include <stdint.h>

typedef unsigned short u16;
typedef unsigned v4u __attribute__((ext_vector_type(4)));
typedef unsigned v2u __attribute__((ext_vector_type(2)));
typedef float f32x4 __attribute__((ext_vector_type(4)));
typedef float f32x2 __attribute__((ext_vector_type(2)));
typedef short bf16x8 __attribute__((ext_vector_type(8)));
typedef short s16x4 __attribute__((ext_vector_type(4)));
typedef __bf16 bf16x2_t __attribute__((ext_vector_type(2)));
#define LAS __attribute__((address_space(3)))
#define GAS __attribute__((address_space(1)))
typedef GAS unsigned gu32;
#define RLX_AGENT __ATOMIC_RELAXED, __HIP_MEMORY_SCOPE_AGENT

constexpr int S = 2048, D = 640, F = 1536;
constexpr int NTHR = 512;
constexpr unsigned WS_H = 0;
constexpr unsigned WS_KV = WS_H + S * F * 2;
constexpr unsigned WS_Y = WS_KV + S * D * 2;
constexpr unsigned WS_G = WS_Y + S * D * 2;
constexpr unsigned WS_CTL = WS_G + S * 4;
constexpr unsigned WS_END = WS_CTL + 8192;
constexpr int W_EPOCH = 0, W_H = 64, W_KV = 320, W_Y = 384;
constexpr int AUX_SC1 = 16;
constexpr unsigned kSpinCap = 1u << 18;

struct Params {
    const u16 *q, *k, *v, *shk, *ssm, *Dv, *w1, *w2, *gw;
    u16* out;
    char* ws;
};

__device__ __forceinline__ float bf_lo(unsigned u) { return __uint_as_float(u << 16); }
__device__ __forceinline__ float bf_hi(unsigned u) { return __uint_as_float(u & 0xffff0000u); }
__device__ __forceinline__ f32x2 bf_2(unsigned u) { return (f32x2){bf_lo(u), bf_hi(u)}; }
__device__ __forceinline__ unsigned pk2(f32x2 v) { return __builtin_bit_cast(unsigned, __builtin_convertvector(v, bf16x2_t)); }
__device__ __forceinline__ unsigned pk_bf16(float lo, float hi) { return pk2((f32x2){lo, hi}); }
__device__ __forceinline__ float rbf(float x) { return bf_lo(pk_bf16(x, x)); }
__device__ __forceinline__ f32x4 mfma16(v4u a, v4u b, f32x4 c) { return __builtin_amdgcn_mfma_f32_16x16x32_bf16(__builtin_bit_cast(bf16x8, a), __builtin_bit_cast(bf16x8, b), c, 0, 0, 0); }
__device__ __forceinline__ v2u tr_read(LAS char* p) { return __builtin_bit_cast(v2u, __builtin_amdgcn_ds_read_tr16_b64_v4i16((LAS s16x4*)p)); }
__device__ __forceinline__ v4u lds_rd16(LAS char* p) { return *(LAS v4u*)p; }
__device__ __forceinline__ void glds16(unsigned voff, const void* sbase, unsigned lds_dst) {
    asm volatile("s_mov_b32 m0, %2\n\ts_nop 0\n\tglobal_load_lds_dwordx4 %0, %1" :: "v"(voff), "s"(sbase), "s"(lds_dst) : "memory");
}
#define WAIT_VM(n) asm volatile("s_waitcnt vmcnt(%0)" :: "n"(n) : "memory")
#define WAIT_LGKM0() asm volatile("s_waitcnt lgkmcnt(0)" ::: "memory")
#define BAR() do { asm volatile("" ::: "memory"); __builtin_amdgcn_s_barrier(); asm volatile("" ::: "memory"); } while (0)
#define SCHED() __builtin_amdgcn_sched_barrier(0)
#define STAMP(id) do {} while (0)
#define STAMP_ARG
#define STAMP_PASS
#define STAMP_FLUSH() do {} while (0)

__device__ __forceinline__ void st16_sc1(v4u v, __amdgpu_buffer_rsrc_t rs, unsigned off) { __builtin_amdgcn_raw_buffer_store_b128(v, rs, off, 0, AUX_SC1); }
__device__ __forceinline__ void poll_flags(gu32* f, int n, unsigned tag, int lane) {
#pragma unroll 1
    for (unsigned spins = 0; spins < kSpinCap; ++spins) {
        const unsigned v = lane < n ? __hip_atomic_load(f + lane, RLX_AGENT) : tag;
        if (__all(v == tag)) break;
        __builtin_amdgcn_s_sleep(2);
    }
    __builtin_amdgcn_fence(__ATOMIC_ACQUIRE, "wavefront");
}
__device__ __forceinline__ unsigned silu2(unsigned hb) {
    const f32x2 h = bf_2(hb);
    const f32x2 t = h * -1.44269504f;
    const f32x2 den = (f32x2){__builtin_amdgcn_exp2f(t.x), __builtin_amdgcn_exp2f(t.y)} + 1.f;
    const f32x2 r = (f32x2){__builtin_amdgcn_rcpf(den.x), __builtin_amdgcn_rcpf(den.y)};
    return pk2(h * r);
}

constexpr int G1_NS = 4, G1_SLOT = 32768, G1_NST = D / 64, G1_STG = G1_NS * G1_SLOT;
constexpr int G2_NST = F / 64, G2_HD = 4;
constexpr int G2_Q = 0, G2_Y = 16384, G2_G = 32768;
__device__ __forceinline__ unsigned g2_slot(int u) { return (unsigned)((((u >> 2) + 2) & 3) * 32768 + (u & 3) * 8192); }

struct W2Src { unsigned vw[2]; const char* base; int kst; };
__device__ __forceinline__ void w2_issue(W2Src& s, int lw, unsigned slot) {
    const char* src = s.base + (size_t)s.kst * (64 * D * 2);
#pragma unroll
    for (int i = 0; i < 2; ++i) glds16(s.vw[i], src, slot + (16 * lw + 8 * i) * 128);
    s.kst = s.kst + 1 == G2_NST ? 0 : s.kst + 1;
}

template <bool TYPE_A>
__device__ __forceinline__ void g1_loader(const Params& p, int panel, int ct, int off1, W2Src& w2s, int lw, int lane STAMP_ARG) {
    unsigned vx[4], vw[4];
#pragma unroll
    for (int i = 0; i < 4; ++i) {
        const int r = lane >> 3, c = lane & 7, row = 32 * lw + 8 * i + r;
        vx[i] = (unsigned)(row * (D * 2) + ((c ^ ((row >> 1) & 7)) * 16));
        const int r2 = lane >> 4, c2 = lane & 15, k = 16 * lw + 4 * i + r2, phi = (k & 3) | (((k >> 3) & 1) << 2);
        vw[i] = (unsigned)(k * (F * 2) + ((c2 ^ (phi << 1)) * 16));
    }
    const char* xb = (const char*)p.q + (size_t)panel * 128 * D * 2;
    const char* wb = (const char*)p.w1 + (size_t)ct * 256;
    int kst = off1;
    auto issue = [&](int slot_i) {
        const unsigned slot = (unsigned)slot_i * G1_SLOT;
        const char* xs = xb + kst * 128; const char* wsrc = wb + (size_t)kst * (64 * F * 2);
#pragma unroll
        for (int i = 0; i < 4; ++i) glds16(vx[i], xs, slot + (32 * lw + 8 * i) * 128);
#pragma unroll
        for (int i = 0; i < 4; ++i) glds16(vw[i], wsrc, slot + 16384 + (16 * lw + 4 * i) * 256);
        kst = kst + 1 == G1_NST ? 0 : kst + 1;
    };
#pragma unroll
    for (int st = 0; st < G1_NS - 1; ++st) issue(st);
#pragma unroll 1
    for (int i = 0; i < G1_NST - 3; ++i) { WAIT_VM(16); BAR(); issue((i + 3) & 3); }
    STAMP(1);
    if (TYPE_A) {
#pragma unroll
        for (int i = 0; i < 3; ++i) {
            WAIT_VM(16); BAR();
#pragma unroll
            for (int u = 0; u < 4; ++u) w2_issue(w2s, lw, g2_slot(4 * i + u));
        }
    } else {
        WAIT_VM(16); BAR(); WAIT_VM(8); BAR(); WAIT_VM(0); BAR();
    }
}
__device__ __forceinline__ void g1_store(const Params& p, __amdgpu_buffer_rsrc_t rs, int stg, int ksg, int lane, v4u hb) {
    const v4u o = {silu2(hb.x), silu2(hb.y), silu2(hb.z), silu2(hb.w)};
    st16_sc1(o, rs, WS_H + (stg * 48 + ksg) * 1024 + lane * 16);
}
__device__ __forceinline__ void g1_compute(const Params& p, LAS char* L, __amdgpu_buffer_rsrc_t rs, int panel, int ct, int w, int lane STAMP_ARG) {
    const int wf = w & 1, wsx = w >> 1;
    const int g = lane >> 4, q = (lane & 15) >> 2, pp = lane & 3, s = lane & 15;
    const int phi = q | ((g & 1) << 2);
    unsigned aw[4], ax[2];
#pragma unroll
    for (int ft = 0; ft < 4; ++ft) aw[ft] = 16384 + (8 * g + q) * 256 + (((4 * wf + ft) ^ phi) * 32) + 8 * pp;
#pragma unroll
    for (int ks = 0; ks < 2; ++ks) ax[ks] = (64 * wsx + s) * 128 + (((4 * ks + g) ^ ((s >> 1) & 7)) * 16);
    f32x4 acc[4][4];
#pragma unroll
    for (int a = 0; a < 4; ++a)
#pragma unroll
        for (int b = 0; b < 4; ++b) acc[a][b] = (f32x4){0.f, 0.f, 0.f, 0.f};
    v4u A[2][4], B[2][4];
#pragma unroll
    for (int a = 0; a < 4; ++a) { A[1][a] = (v4u){0u, 0u, 0u, 0u}; B[1][a] = (v4u){0u, 0u, 0u, 0u}; }
    auto rd = [&](int set, LAS char* base, int ks) {
#pragma unroll
        for (int ft = 0; ft < 4; ++ft) {
            const v2u lo = tr_read(base + aw[ft] + ks * 8192), hi = tr_read(base + aw[ft] + ks * 8192 + 1024);
            A[set][ft] = (v4u){lo.x, lo.y, hi.x, hi.y};
        }
#pragma unroll
        for (int sx = 0; sx < 4; ++sx) B[set][sx] = lds_rd16(base + ax[ks] + sx * 2048);
    };
    auto mm = [&](int set) {
#pragma unroll
        for (int ft = 0; ft < 4; ++ft)
#pragma unroll
            for (int sx = 0; sx < 4; ++sx) acc[ft][sx] = mfma16(A[set][ft], B[set][sx], acc[ft][sx]);
    };
#pragma unroll 1
    for (int i = 0; i < G1_NST; ++i) {
        LAS char* base = L + (i & 3) * G1_SLOT;
        BAR();
        rd(0, base, 0);
        SCHED();
        mm(1);
        SCHED();
        rd(1, base, 1);
        SCHED();
        mm(0);
        SCHED();
        WAIT_LGKM0();
    }
    mm(1);
    STAMP(2);
    v4u hb[4][2];
#pragma unroll
    for (int sx = 0; sx < 4; ++sx)
#pragma unroll
        for (int u = 0; u < 2; ++u) {
            const f32x4 a = acc[2 * u][sx], b = acc[2 * u + 1][sx];
            hb[sx][u] = (v4u){pk_bf16(a[0], a[1]), pk_bf16(a[2], a[3]), pk_bf16(b[0], b[1]), pk_bf16(b[2], b[3])};
        }
#pragma unroll
    for (int j = 0; j < 4; ++j) *(LAS v4u*)(L + G1_STG + w * 4096 + j * 1024 + lane * 16) = hb[2 + (j >> 1)][j & 1];
    WAIT_LGKM0();
    BAR();
#pragma unroll
    for (int j = 0; j < 4; ++j) g1_store(p, rs, panel * 8 + wsx * 4 + (j >> 1), ct * 4 + wf * 2 + (j & 1), lane, hb[j >> 1][j & 1]);
}
__device__ __forceinline__ void g1_loader_epilogue(const Params& p, LAS char* L, __amdgpu_buffer_rsrc_t rs, int panel, int ct, int lw, int lane) {
    const int wf = lw & 1, wsx = lw >> 1;
    v4u hb[4];
#pragma unroll
    for (int j = 0; j < 4; ++j) hb[j] = *(LAS v4u*)(L + G1_STG + lw * 4096 + j * 1024 + lane * 16);
#pragma unroll
    for (int j = 0; j < 4; ++j) g1_store(p, rs, panel * 8 + wsx * 4 + 2 + (j >> 1), ct * 4 + wf * 2 + (j & 1), lane, hb[j]);
}

__device__ __forceinline__ void prepass(const Params& p, LAS char* L, __amdgpu_buffer_rsrc_t rs, int wg, int tid STAMP_ARG) {
    const int s0 = 64 * (wg >> 1), c0 = 320 * (wg & 1);
    const int lane = tid & 63, w = __builtin_amdgcn_readfirstlane(tid >> 6);
    if (w < 5) {
        const int cc = tid % 40, rg = tid / 40;
        const int ch = c0 + 8 * cc, r0 = s0 + 8 * rg;
        v4u kk[11], vv[8], sw[4];
#pragma unroll
        for (int j = 0; j < 11; ++j) {
            const int row = r0 - 3 + j;
            kk[j] = *(const GAS v4u*)((const char*)p.k + (size_t)(row < 0 ? 0 : row) * (D * 2) + ch * 2);
        }
#pragma unroll
        for (int j = 0; j < 8; ++j) vv[j] = *(const GAS v4u*)((const char*)p.v + (size_t)(r0 + j) * (D * 2) + ch * 2);
#pragma unroll
        for (int j = 0; j < 4; ++j) sw[j] = *(const GAS v4u*)((const char*)p.shk + (size_t)ch * 8 + j * 16);
        if (r0 == 0) { kk[0] = (v4u){0u, 0u, 0u, 0u}; kk[1] = kk[0]; kk[2] = kk[0]; }
#pragma unroll
        for (int e = 0; e < 4; ++e) {
            const f32x2 W0 = {bf_lo(sw[e].x), bf_lo(sw[e].z)}, W1 = {bf_hi(sw[e].x), bf_hi(sw[e].z)};
            const f32x2 W2 = {bf_lo(sw[e].y), bf_lo(sw[e].w)}, W3 = {bf_hi(sw[e].y), bf_hi(sw[e].w)};
            f32x2 kf[11], o[8];
#pragma unroll
            for (int j = 0; j < 11; ++j) kf[j] = bf_2(kk[j][e]);
#pragma unroll
            for (int j = 0; j < 8; ++j) {
                const f32x2 ksh = kf[j] * W3 + kf[j + 1] * W2 + kf[j + 2] * W1 + kf[j + 3] * W0;
                o[j] = bf_2(pk2(ksh)) * bf_2(vv[j][e]);
            }
            const v4u p0 = {pk_bf16(o[0].x, o[1].x), pk_bf16(o[2].x, o[3].x), pk_bf16(o[4].x, o[5].x), pk_bf16(o[6].x, o[7].x)};
            const v4u p1 = {pk_bf16(o[0].y, o[1].y), pk_bf16(o[2].y, o[3].y), pk_bf16(o[4].y, o[5].y), pk_bf16(o[6].y, o[7].y)};
            *(LAS v4u*)(L + (8 * cc + 2 * e) * 128 + ((rg ^ (cc & 7)) * 16)) = p0;
            *(LAS v4u*)(L + (8 * cc + 2 * e + 1) * 128 + ((rg ^ (cc & 7)) * 16)) = p1;
        }
    } else {
        const int i = lane & 15;
        v4u gwv[5], qv[3][5];
#pragma unroll
        for (int j = 0; j < 5; ++j) gwv[j] = *(const GAS v4u*)((const char*)p.gw + (i + 16 * j) * 16);
#pragma unroll
        for (int t = 0; t < 3; ++t) {
            const int grp = (w - 5) + 3 * t < 8 ? (w - 5) + 3 * t : 7;
            const int row = s0 + 32 * (wg & 1) + 4 * grp + (lane >> 4);
#pragma unroll
            for (int j = 0; j < 5; ++j) qv[t][j] = *(const GAS v4u*)((const char*)p.q + (size_t)row * (D * 2) + (i + 16 * j) * 16);
        }
#pragma unroll
        for (int t = 0; t < 3; ++t) {
            const int grp = (w - 5) + 3 * t;
            const int row = s0 + 32 * (wg & 1) + 4 * grp + (lane >> 4);
            f32x2 a2 = {0.f, 0.f};
#pragma unroll
            for (int j = 0; j < 5; ++j)
#pragma unroll
                for (int e = 0; e < 4; ++e) a2 += bf_2(pk2(bf_2(qv[t][j][e]) * bf_2(gwv[j][e])));
            float acc = a2.x + a2.y;
#pragma unroll
            for (int m = 8; m >= 1; m >>= 1) acc += __shfl_xor(acc, m);
            if (i == 0 && grp < 8) {
                const float z = rbf(acc);
                const float gv = rbf(__builtin_amdgcn_rcpf(1.f + __builtin_amdgcn_exp2f(z * -1.44269504f)));
                __builtin_amdgcn_raw_buffer_store_b32(__float_as_uint(gv), rs, row * 4, WS_G, AUX_SC1);
            }
        }
    }
    STAMP(1);
    WAIT_LGKM0();
    BAR();
#pragma unroll
    for (int j = 0; j < 5; ++j) {
        const int n = tid + NTHR * j, ch = n >> 3, pc = n & 7;
        const v4u val = *(LAS v4u*)(L + ch * 128 + ((pc ^ ((ch >> 3) & 7)) * 16));
        st16_sc1(val, rs, WS_KV + (c0 + ch) * (S * 2) + s0 * 2 + pc * 16);
    }
}

constexpr int CV_COPY = 4160, CV_X = 4 * CV_COPY, CV_WAVE = CV_X + 4608;
struct ConvIn { v4u v[4], vp[4], x[4]; unsigned vq[4]; float Dd; };
__device__ __forceinline__ void conv_load_h(const Params& p, ConvIn& c, int ch, int lane) {
    const char* hsrc = (const char*)p.ssm + (size_t)ch * (S * 2);
#pragma unroll
    for (int j = 0; j < 4; ++j) {
        const int o = j * 1024 + lane * 16;
        c.v[j] = *(const GAS v4u*)(hsrc + o);
        c.vp[j] = *(const GAS v4u*)(hsrc + (o < 4 ? 0 : o - 4));
        c.vq[j] = *(const GAS unsigned*)(hsrc + (o < 8 ? 0 : o - 8));
    }
    c.Dd = bf_lo((unsigned)p.Dv[ch]);
}
__device__ __forceinline__ void conv_load_x(ConvIn& c, __amdgpu_buffer_rsrc_t rs, int ch, int lane) {
#pragma unroll
    for (int j = 0; j < 4; ++j) c.x[j] = __builtin_amdgcn_raw_buffer_load_b128(rs, j * 1024 + lane * 16, WS_KV + ch * (S * 2), AUX_SC1);
}
__device__ __forceinline__ void conv_fill(ConvIn& c, LAS char* Lw, int lane) {
    if (lane == 0) { c.vp[0] = (v4u){0u, c.v[0].x, c.v[0].y, c.v[0].z}; c.vq[0] = 0u; }
#pragma unroll
    for (int j = 0; j < 4; ++j) {
        const v4u v = c.v[j], vp = c.vp[j];
        const unsigned a0 = __builtin_amdgcn_alignbit(v.x, vp.x, 16), a1 = __builtin_amdgcn_alignbit(v.y, v.x, 16), a2 = __builtin_amdgcn_alignbit(v.z, v.y, 16), a3 = __builtin_amdgcn_alignbit(v.w, v.z, 16);
        const unsigned am = __builtin_amdgcn_alignbit(vp.x, c.vq[j], 16);
        LAS char* d = Lw + 32 + j * 1024 + lane * 16;
        *(LAS v4u*)(d) = v;
        *(LAS v4u*)(d + CV_COPY) = (v4u){a0, a1, a2, a3};
        *(LAS v4u*)(d + 2 * CV_COPY) = vp;
        *(LAS v4u*)(d + 3 * CV_COPY) = (v4u){am, a0, a1, a2};
        *(LAS v4u*)(Lw + CV_X + 480 + j * 1024 + lane * 16) = c.x[j];
    }
}
__device__ __forceinline__ void conv_waves(const Params& p, LAS char* Lw, __amdgpu_buffer_rsrc_t rs, unsigned tag, ConvIn& c, int ch, int nch, int chstep, int lane STAMP_ARG) {
    gu32* ctl = (gu32*)(p.ws + WS_CTL);
    if (lane < 30) *(LAS v4u*)(Lw + CV_X + lane * 16) = (v4u){0u, 0u, 0u, 0u};
    if (lane < 8) *(LAS v4u*)(Lw + (lane >> 1) * CV_COPY + (lane & 1) * 16) = (v4u){0u, 0u, 0u, 0u};
    const int g = lane >> 4, q = (lane & 15) >> 2, pp = lane & 3, n = lane & 15;
#pragma unroll 1
    for (int it = 0; it < nch; ++it) {
        conv_fill(c, Lw, lane);
        const float Dd = c.Dd;
        WAIT_LGKM0();
        if (it == 0) STAMP(8); else STAMP(12);
        if (it + 1 < nch) { conv_load_h(p, c, ch + chstep, lane); conv_load_x(c, rs, ch + chstep, lane); }
        LAS char* la = Lw + q * CV_COPY + 32 - 16 * g + 8 * pp + 24;
        LAS char* lb = Lw + CV_X + 32 * n + 16 * g;
        f32x4 acc[8];
#pragma unroll
        for (int t = 0; t < 8; ++t) acc[t] = (f32x4){0.f, 0.f, 0.f, 0.f};
        v4u A[2][8], B[2][8];
        auto rd = [&](int set, int ro) {
#pragma unroll
            for (int i = 0; i < 8; ++i) {
                const v2u lo = tr_read(la + ro + 512 * i + 8), hi = tr_read(la + ro + 512 * i);
                A[set][i] = (v4u){lo.x, lo.y, hi.x, hi.y};
                B[set][i] = lds_rd16(lb + 448 - ro + 512 * i);
            }
        };
        auto mm = [&](int set) {
#pragma unroll
            for (int l = 0; l < 8; ++l)
#pragma unroll
                for (int i = 0; i + l < 8; ++i) acc[i + l] = mfma16(A[set][i], B[set][l], acc[i + l]);
        };
        rd(0, 0);
#pragma unroll 1
        for (int rr = 0; rr < 4; ++rr) {
            SCHED();
            rd(1, 64);
            SCHED();
            mm(0);
            SCHED();
            la += 128; lb -= 128;
            if (rr < 3) rd(0, 0);
            SCHED();
            mm(1);
            if (rr == 1 && it > 0) {
                WAIT_VM(0);
                if (lane == 0) __hip_atomic_store(ctl + W_Y + ch - chstep, tag, RLX_AGENT);
            }
        }
        if (it == 0) STAMP(9); else STAMP(13);
        WAIT_LGKM0();
#pragma unroll
        for (int t = 0; t < 8; ++t) {
            const int sidx = 256 * t + 16 * n + 4 * g;
            const v2u xv = *(LAS v2u*)(Lw + CV_X + 480 + 2 * sidx);
            const unsigned y01 = pk_bf16(acc[t][0], acc[t][1]), y23 = pk_bf16(acc[t][2], acc[t][3]);
            const v2u o = {pk2(bf_2(y01) + bf_2(pk2(bf_2(xv.x) * Dd))), pk2(bf_2(y23) + bf_2(pk2(bf_2(xv.y) * Dd)))};
            *(LAS v2u*)(Lw + 32 + 2 * sidx) = o;
        }
        WAIT_LGKM0();
#pragma unroll
        for (int j = 0; j < 4; ++j) {
            const v4u val = *(LAS v4u*)(Lw + 32 + j * 1024 + lane * 16);
            st16_sc1(val, rs, WS_Y + ch * (S * 2) + j * 1024 + lane * 16);
        }
        WAIT_LGKM0();
        if (it == 0) STAMP(10); else STAMP(14);
        ch += chstep;
    }
    WAIT_VM(0);
    if (lane == 0) __hip_atomic_store(ctl + W_Y + ch - chstep, tag, RLX_AGENT);
    STAMP(16);
}

__device__ __forceinline__ void g2_loader(const Params& p, LAS char* L, __amdgpu_buffer_rsrc_t rs, unsigned tag, W2Src& w2s, int panel, int ct, int lw, int lane STAMP_ARG) {
    gu32* ctl = (gu32*)(p.ws + WS_CTL);
    if (lw == 0) poll_flags(ctl + W_H + panel * 16, 12, tag, lane);
    STAMP(5);
    BAR();
#pragma unroll 1
    for (int j = 1; j <= 8; ++j) { BAR(); w2_issue(w2s, lw, g2_slot((j - 1) & 15)); }
#pragma unroll 1
    for (int j = 9; j <= 13; ++j) BAR();
#pragma unroll
    for (int i = 0; i < 4; ++i) {
        const int r = lane >> 3, c = lane & 7, row = 32 * lw + 8 * i + r;
        glds16((unsigned)(row * (D * 2) + ((c ^ ((row >> 1) & 7)) * 16)), (const char*)p.q + (size_t)panel * 128 * D * 2 + ct * 128, G2_Q + (32 * lw + 8 * i) * 128);
    }
    BAR(); BAR();
    WAIT_VM(4);
    BAR();
    poll_flags(ctl + W_Y + ct * 64, 64, tag, lane);
    STAMP(6);
    v4u yv[4];
#pragma unroll
    for (int i = 0; i < 4; ++i) {
        const int r = lane >> 4, c = lane & 15, d = 16 * lw + 4 * i + r;
        yv[i] = __builtin_amdgcn_raw_buffer_load_b128(rs, d * (S * 2) + c * 16, WS_Y + ct * 64 * S * 2 + panel * 256, AUX_SC1);
    }
    const unsigned gv = __builtin_amdgcn_raw_buffer_load_b32(rs, (lw * 32 + (lane & 31)) * 4, WS_G + panel * 512, AUX_SC1);
#pragma unroll
    for (int i = 0; i < 4; ++i) {
        const int r = lane >> 4, c = lane & 15, d = 16 * lw + 4 * i + r;
        *(LAS v4u*)(L + G2_Y + d * 256 + ((c ^ ((d & 7) << 1)) * 16)) = yv[i];
    }
    if (lane < 32) *(LAS unsigned*)(L + G2_G + (lw * 32 + lane) * 4) = gv;
    WAIT_VM(0);
    WAIT_LGKM0();
    BAR();
}
__device__ __forceinline__ void g2_compute(const Params& p, LAS char* L, __amdgpu_buffer_rsrc_t rs, int panel, int ct, int off2, int w, int lane STAMP_ARG) {
    const int g = lane >> 4, q = (lane & 15) >> 2, pp = lane & 3, s = lane & 15;
    const int sw = ((g & 1) << 1) | (q >> 1);
    unsigned aw[4];
#pragma unroll
    for (int dt = 0; dt < 4; ++dt) aw[dt] = (4 * g + q) * 128 + ((dt ^ sw) * 32) + 8 * pp;
    const unsigned hbase = WS_H + (panel * 8 + 2 * w) * (48 * 1024);
    int kld = off2;
    f32x4 acc[4][2];
#pragma unroll
    for (int a = 0; a < 4; ++a) { acc[a][0] = (f32x4){0.f, 0.f, 0.f, 0.f}; acc[a][1] = (f32x4){0.f, 0.f, 0.f, 0.f}; }
    v4u H[G2_HD][2][2];
    auto ldh = [&](int slot) {
#pragma unroll
        for (int ks = 0; ks < 2; ++ks)
#pragma unroll
            for (int sx = 0; sx < 2; ++sx) H[slot][ks][sx] = __builtin_amdgcn_raw_buffer_load_b128(rs, lane * 16 + sx * (48 * 1024) + ks * 1024, hbase + kld * 2048, AUX_SC1);
        kld = kld + 1 == G2_NST ? 0 : kld + 1;
    };
    BAR();
    STAMP(5);
#pragma unroll
    for (int st = 0; st < G2_HD - 1; ++st) ldh(st);
#pragma unroll
    for (int a = 0; a < 4; ++a) H[G2_HD - 1][a >> 1][a & 1] = (v4u){0u, 0u, 0u, 0u};
    SCHED();
    v4u A[2][4];
#pragma unroll
    for (int a = 0; a < 4; ++a) A[1][a] = (v4u){0u, 0u, 0u, 0u};
    auto rd = [&](int set, LAS char* base) {
#pragma unroll
        for (int dt = 0; dt < 4; ++dt) {
            const v2u lo = tr_read(base + aw[dt]), hi = tr_read(base + aw[dt] + 2048);
            A[set][dt] = (v4u){lo.x, lo.y, hi.x, hi.y};
        }
    };
    auto mm = [&](int set, int slot, int ks) {
#pragma unroll
        for (int dt = 0; dt < 4; ++dt)
#pragma unroll
            for (int sx = 0; sx < 2; ++sx) acc[dt][sx] = mfma16(A[set][dt], H[slot][ks][sx], acc[dt][sx]);
    };
#pragma unroll 1
    for (int jj = 0; jj < 5; ++jj) {
        LAS char* base = L + ((jj + 2) & 3) * 32768;
#pragma unroll
        for (int u = 0; u < 4; ++u) {
            const int j = 4 * jj + u;
            if (j > 0 && j <= 16) BAR();
            rd(0, base + u * 8192);
            SCHED();
            mm(1, (u + 3) & 3, 1);
            SCHED();
            ldh((u + 3) & 3);
            SCHED();
            rd(1, base + u * 8192 + 4096);
            SCHED();
            mm(0, u, 0);
            SCHED();
            WAIT_LGKM0();
        }
    }
    {
        LAS char* base = L + ((5 + 2) & 3) * 32768;
#pragma unroll
        for (int u = 0; u < 4; ++u) {
            rd(0, base + u * 8192);
            SCHED();
            mm(1, (u + 3) & 3, 1);
            SCHED();
            if (u == 0) ldh(3);
            SCHED();
            rd(1, base + u * 8192 + 4096);
            SCHED();
            mm(0, u, 0);
            SCHED();
        }
        mm(1, 3, 1);
    }
    STAMP(6);
    BAR();
    STAMP(7);
#pragma unroll
    for (int sx = 0; sx < 2; ++sx) {
        const int row = 32 * w + 16 * sx + s;
        const float gg = *(LAS float*)(L + G2_G + row * 4);
        const float omg = rbf(1.f - gg);
#pragma unroll
        for (int dt = 0; dt < 4; ++dt) {
            const int d = 16 * dt + 4 * g + q;
            const v2u yv = tr_read(L + G2_Y + d * 256 + (((2 * w + sx) ^ (d & 7)) * 32) + 8 * pp);
            LAS v2u* qp = (LAS v2u*)(L + G2_Q + row * 128 + (((2 * dt + (g >> 1)) ^ ((row >> 1) & 7)) * 16) + 8 * (g & 1));
            const v2u qv = *qp;
            const f32x4 a = acc[dt][sx];
            const unsigned b01 = pk_bf16(a[0], a[1]), b23 = pk_bf16(a[2], a[3]);
            const unsigned a01 = pk2(bf_2(qv.x) * bf_2(yv.x)), a23 = pk2(bf_2(qv.y) * bf_2(yv.y));
            const unsigned ga01 = pk2(bf_2(a01) * gg), ga23 = pk2(bf_2(a23) * gg);
            const unsigned gb01 = pk2(bf_2(b01) * omg), gb23 = pk2(bf_2(b23) * omg);
            *qp = (v2u){pk2(bf_2(ga01) + bf_2(gb01)), pk2(bf_2(ga23) + bf_2(gb23))};
        }
    }
    WAIT_LGKM0();
#pragma unroll
    for (int i = 0; i < 4; ++i) {
        const int row = 32 * w + 8 * i + (lane >> 3), c = lane & 7;
        const v4u val = *(LAS v4u*)(L + G2_Q + row * 128 + c * 16);
        *(GAS v4u*)((char*)p.out + (size_t)(panel * 128 + row) * (D * 2) + ct * 128 + ((c ^ ((row >> 1) & 7)) * 16)) = val;
    }
    STAMP(8);
}

constexpr int LDS_BYTES = G1_STG + 16384;
static_assert(4 * CV_WAVE <= LDS_BYTES, "conv regions");
__global__ void __launch_bounds__(NTHR, 2) k_fused(Params p) {
    extern __shared__ __attribute__((aligned(16))) char smem[];
    LAS char* L = (LAS char*)smem;
    const int tid = threadIdx.x, lane = tid & 63, w = __builtin_amdgcn_readfirstlane(tid >> 6);
    const int x = blockIdx.x & 7, idx = blockIdx.x >> 3, xr = x >> 1, xc = x & 1;
    STAMP(0);
    gu32* ctl = (gu32*)(p.ws + WS_CTL);
    unsigned epoch_v = __hip_atomic_load(ctl + W_EPOCH, RLX_AGENT);
#define GET_TAG() asm volatile("" : "+v"(epoch_v)); const unsigned tag = __builtin_amdgcn_readfirstlane(epoch_v) + 1u
    const __amdgpu_buffer_rsrc_t rs = __builtin_amdgcn_make_buffer_rsrc(p.ws, 0, WS_END, 0x00020000);
    if (idx < 24) {
        const int panel = 4 * xr + idx / 6, ct = 6 * xc + idx % 6;
        const int off1 = ((idx % 6) * 5 / 3 + (idx / 6) * 5 / 2) % 10;
        const bool typeA = idx < 20;
        const int panel2 = 4 * xr + idx / 5, ct2 = 5 * xc + idx % 5;
        const int off2 = ((idx % 5) * 24 / 5 + (idx / 5) * 6) % 24;
        if (w < 4) {
            g1_compute(p, L, rs, panel, ct, w, lane STAMP_PASS);
            STAMP(3);
            WAIT_VM(0);
            GET_TAG();
            STAMP(4);
            BAR();
            if (tid == 0) __hip_atomic_store(ctl + W_H + panel * 16 + ct, tag, RLX_AGENT);
            if (typeA) g2_compute(p, L, rs, panel2, ct2, off2, w, lane STAMP_PASS);
        } else {
            const int lw = w - 4;
            if (typeA) {
                W2Src w2s;
#pragma unroll
                for (int i = 0; i < 2; ++i) {
                    const int r = lane >> 3, c = lane & 7, k = 16 * lw + 8 * i + r;
                    w2s.vw[i] = (unsigned)(k * (D * 2) + ((c ^ (((k >> 1) & 3) << 1)) * 16));
                }
                w2s.base = (const char*)p.w2 + ct2 * 128; w2s.kst = off2;
                g1_loader<true>(p, panel, ct, off1, w2s, lw, lane STAMP_PASS);
                BAR();
#pragma unroll
                for (int u = 0; u < 4; ++u) w2_issue(w2s, lw, g2_slot(12 + u));
                g1_loader_epilogue(p, L, rs, panel, ct, lw, lane);
                WAIT_VM(0);
                GET_TAG();
                BAR();
                g2_loader(p, L, rs, tag, w2s, panel2, ct2, lw, lane STAMP_PASS);
            } else {
                W2Src w2s; w2s.vw[0] = 0; w2s.vw[1] = 0; w2s.base = nullptr; w2s.kst = 0;
                g1_loader<false>(p, panel, ct, off1, w2s, lw, lane STAMP_PASS);
                const int ch = 512 + 4 * (x * 4 + idx - 20) + lw;
                ConvIn c;
                BAR();
                g1_loader_epilogue(p, L, rs, panel, ct, lw, lane);
                conv_load_h(p, c, ch, lane);
                WAIT_VM(0);
                GET_TAG();
                BAR();
                poll_flags(ctl + W_KV, 64, tag, lane);
                conv_load_x(c, rs, ch, lane);
                conv_waves(p, L + lw * CV_WAVE, rs, tag, c, ch, 1, 0, lane STAMP_PASS);
            }
        }
    } else {
        const int cw = x * 8 + (idx - 24);
        prepass(p, L, rs, cw, tid STAMP_PASS);
        STAMP(2);
        ConvIn c;
        const int ch = 8 * cw + w;
        if (w < 4) conv_load_h(p, c, ch, lane);
        WAIT_VM(0);
        GET_TAG();
        BAR();
        if (tid == 0) __hip_atomic_store(ctl + W_KV + cw, tag, RLX_AGENT);
        STAMP(3);
        if (w < 4) {
            poll_flags(ctl + W_KV, 64, tag, lane);
            STAMP(4);
            conv_load_x(c, rs, ch, lane);
            conv_waves(p, L + w * CV_WAVE, rs, tag, c, ch, 2, 4, lane STAMP_PASS);
        } else if (w == 7 && blockIdx.x == 255) {
            poll_flags(ctl + W_KV, 64, tag, lane);
#pragma unroll 1
            for (int pn = 0; pn < 16; ++pn) poll_flags(ctl + W_H + pn * 16, 12, tag, lane);
            if (lane == 0) __hip_atomic_store(ctl + W_EPOCH, tag, RLX_AGENT);
        }
    }
    STAMP_FLUSH();
}

extern "C" void kernel_launch(void* const* d_in, const int* in_sizes, int n_in, void* d_out, int out_size, void* d_ws, size_t ws_size, hipStream_t stream) {
    Params p;
    p.q = (const u16*)d_in[0]; p.k = (const u16*)d_in[1]; p.v = (const u16*)d_in[2]; p.shk = (const u16*)d_in[3];
    p.ssm = (const u16*)d_in[4]; p.Dv = (const u16*)d_in[5]; p.w1 = (const u16*)d_in[7]; p.w2 = (const u16*)d_in[8]; p.gw = (const u16*)d_in[9];
    p.out = (u16*)d_out; p.ws = (char*)d_ws;
    (void)hipFuncSetAttribute((const void*)k_fused, hipFuncAttributeMaxDynamicSharedMemorySize, LDS_BYTES);
    k_fused<<<256, NTHR, LDS_BYTES, stream>>>(p);
}
```

```cpp
#include <hip/hip_runtime.h>
#include <stdint.h>
#include <type_traits>

typedef unsigned short u16;
typedef unsigned v4u __attribute__((ext_vector_type(4)));
typedef unsigned v2u __attribute__((ext_vector_type(2)));
typedef float f32x4 __attribute__((ext_vector_type(4)));
typedef float f32x2 __attribute__((ext_vector_type(2)));
typedef short bf16x8 __attribute__((ext_vector_type(8)));
typedef short s16x4 __attribute__((ext_vector_type(4)));
typedef __bf16 bf16x2_t __attribute__((ext_vector_type(2)));
#define LAS __attribute__((address_space(3)))
#define GAS __attribute__((address_space(1)))
typedef GAS unsigned gu32;
typedef GAS unsigned long long gu64;
#define RLX_AGENT __ATOMIC_RELAXED, __HIP_MEMORY_SCOPE_AGENT

constexpr int S = 2048, D = 640, F = 1536;
constexpr int NTHR = 512;
constexpr unsigned WS_H = 0;
constexpr unsigned WS_KV = WS_H + S * F * 2;
constexpr unsigned WS_Y = WS_KV + S * D * 2;
constexpr unsigned WS_G = WS_Y + S * D * 2;
constexpr unsigned WS_CTL = WS_G + S * 4;
constexpr int FL = 32;
constexpr int W_EPOCH = 0, W_H = 1, W_KV = W_H + 192, W_G = W_KV + 64, W_Y = W_G + 128, W_LINES = W_Y + 96;
constexpr unsigned WS_END = WS_CTL + W_LINES * 128;
constexpr int AUX_SC1 = 16;
constexpr int H_AUX = 16;
constexpr unsigned kSpinCap = 1u << 18;

struct Params {
    const u16 *q, *k, *v, *shk, *ssm, *Dv, *w1, *w2, *gw;
    u16* out;
    char* ws;
};

__device__ __forceinline__ float bf_lo(unsigned u) { return __uint_as_float(u << 16); }
__device__ __forceinline__ float bf_hi(unsigned u) { return __uint_as_float(u & 0xffff0000u); }
__device__ __forceinline__ f32x2 bf_2(unsigned u) { return (f32x2){bf_lo(u), bf_hi(u)}; }
__device__ __forceinline__ unsigned pk2(f32x2 v) { return __builtin_bit_cast(unsigned, __builtin_convertvector(v, bf16x2_t)); }
__device__ __forceinline__ unsigned pk_bf16(float lo, float hi) { return pk2((f32x2){lo, hi}); }
__device__ __forceinline__ float rbf(float x) { return bf_lo(pk_bf16(x, x)); }
__device__ __forceinline__ f32x4 mfma16(v4u a, v4u b, f32x4 c) { return __builtin_amdgcn_mfma_f32_16x16x32_bf16(__builtin_bit_cast(bf16x8, a), __builtin_bit_cast(bf16x8, b), c, 0, 0, 0); }
__device__ __forceinline__ v2u tr_read(LAS char* p) { return __builtin_bit_cast(v2u, __builtin_amdgcn_ds_read_tr16_b64_v4i16((LAS s16x4*)p)); }
__device__ __forceinline__ v4u lds_rd16(LAS char* p) { return *(LAS v4u*)p; }
__device__ __forceinline__ void glds16(unsigned voff, const void* sbase, unsigned lds_dst) {
    asm volatile("s_mov_b32 m0, %2\n\ts_nop 0\n\tglobal_load_lds_dwordx4 %0, %1" :: "v"(voff), "s"(sbase), "s"(lds_dst) : "memory");
}
#define WAIT_VM(n) asm volatile("s_waitcnt vmcnt(%0)" :: "n"(n) : "memory")
#define WAIT_LGKM0() asm volatile("s_waitcnt lgkmcnt(0)" ::: "memory")
#define BAR() do { asm volatile("" ::: "memory"); __builtin_amdgcn_s_barrier(); asm volatile("" ::: "memory"); } while (0)
#define SCHED() __builtin_amdgcn_sched_barrier(0)
#define A_MFMA(acc, a, b) asm volatile("v_mfma_f32_16x16x32_bf16 %0, %1, %2, %0" : "+v"(acc) : "v"(a), "v"(b))
#define A_RD128(dst, addr, off) asm volatile("ds_read_b128 %0, %1 offset:%2" : "=v"(dst) : "v"(addr), "n"(off))
#define A_RDTR(dst, addr, off) asm volatile("ds_read_b64_tr_b16 %0, %1 offset:%2" : "=v"(dst) : "v"(addr), "n"(off))
#define A_LGKM(n) asm volatile("s_waitcnt lgkmcnt(%0)" :: "n"(n))
#define A_MFMA_DRAIN() asm volatile("s_nop 15\n\ts_nop 7" ::: "memory")
template <int NM, int NR, int PER> __device__ __forceinline__ void interleave() {
#pragma unroll
    for (int i = 0; i < NM; ++i) {
        __builtin_amdgcn_sched_group_barrier(0x8, 1, 0);
        if (i * PER < NR) __builtin_amdgcn_sched_group_barrier(0x100, PER, 0);
    }
}
#define STAMP(id) do {} while (0)
#define STAMP_ARG
#define STAMP_PASS
#define STAMP_FLUSH() do {} while (0)

__device__ __forceinline__ void st16_sc1(v4u v, __amdgpu_buffer_rsrc_t rs, unsigned off) { __builtin_amdgcn_raw_buffer_store_b128(v, rs, off, 0, AUX_SC1); }
__device__ __forceinline__ void poll_flags(gu32* ctl, int line, int n, unsigned tag, int lane) {
    gu32* f = ctl + (line + (lane < n ? lane : 0)) * FL;
#pragma unroll 1
    for (unsigned spins = 0; spins < kSpinCap; ++spins) {
        const unsigned v = __hip_atomic_load(f, RLX_AGENT);
        if (__all(v == tag)) break;
        __builtin_amdgcn_s_sleep(4);
    }
    __builtin_amdgcn_fence(__ATOMIC_ACQUIRE, "wavefront");
}
__device__ __forceinline__ void set_flag(gu32* ctl, int line, unsigned tag) { __hip_atomic_store(ctl + line * FL, tag, RLX_AGENT); }
__device__ __forceinline__ unsigned silu2f(f32x2 h) {
    const f32x2 t = h * -1.44269504f;
    const f32x2 den = (f32x2){__builtin_amdgcn_exp2f(t.x), __builtin_amdgcn_exp2f(t.y)} + 1.f;
    const f32x2 r = (f32x2){__builtin_amdgcn_rcpf(den.x), __builtin_amdgcn_rcpf(den.y)};
    return pk2(h * r);
}
__device__ __forceinline__ unsigned silu2(unsigned hb) { return silu2f(bf_2(hb)); }
__device__ __forceinline__ float row_sum16(float v) {
    v += __builtin_bit_cast(float, __builtin_amdgcn_update_dpp(0, __builtin_bit_cast(int, v), 0x128, 0xf, 0xf, false));
    v += __builtin_bit_cast(float, __builtin_amdgcn_update_dpp(0, __builtin_bit_cast(int, v), 0x124, 0xf, 0xf, false));
    v += __builtin_bit_cast(float, __builtin_amdgcn_update_dpp(0, __builtin_bit_cast(int, v), 0x122, 0xf, 0xf, false));
    v += __builtin_bit_cast(float, __builtin_amdgcn_update_dpp(0, __builtin_bit_cast(int, v), 0x121, 0xf, 0xf, false));
    return v;
}

constexpr int G1_SLOT = 32768, G1_NS = 5, G1_NST = D / 64, G1_STG = 3 * G1_SLOT;
constexpr int G2_NST = F / 64, G2_RING = 40 * 4096, G2_HD = 8;
constexpr int G2_Q = 8 * 4096, G2_Y = 12 * 4096, G2_G = 16 * 4096;
constexpr int CV_COPY = 4160, CV_X = 4 * CV_COPY, CV_PAIR = CV_X + 4608;
constexpr int CV_EX = 4 * CV_PAIR;
constexpr int LDS_BYTES = 163840;
static_assert(CV_EX + 4 * 8192 <= LDS_BYTES, "LDS map");

struct W2Src { unsigned vw[4]; const char* base; int kb0; unsigned aw[4]; };
__device__ __forceinline__ void w2_init(W2Src& s, const Params& p, int ct, int off2, int lane) {
#pragma unroll
    for (int i = 0; i < 4; ++i) {
        const int r = lane >> 3, c = lane & 7, k = 8 * i + r;
        s.vw[i] = (unsigned)(k * (D * 2) + ((c ^ (((k >> 1) & 3) << 1)) * 16));
    }
    const int g = lane >> 4, q = (lane & 15) >> 2, pp = lane & 3, sw = ((g & 1) << 1) | (q >> 1);
#pragma unroll
    for (int dt = 0; dt < 4; ++dt) s.aw[dt] = (4 * g + q) * 128 + ((dt ^ sw) * 32) + 8 * pp;
    s.base = (const char*)p.w2 + ct * 128; s.kb0 = 2 * off2;
}
__device__ __forceinline__ void w2_issue(const W2Src& s, int b) {
    const int kb = s.kb0 + b >= 48 ? s.kb0 + b - 48 : s.kb0 + b;
    const char* src = s.base + (size_t)kb * (32 * D * 2);
    const unsigned slot = (unsigned)(b >= 40 ? b - 40 : b) * 4096;
#pragma unroll
    for (int i = 0; i < 4; ++i) glds16(s.vw[i], src, slot + i * 1024);
}
__device__ __forceinline__ void w2_repack(const W2Src& s, LAS char* L, int b, int lane) {
    LAS char* blk = L + (b >= 40 ? b - 40 : b) * 4096;
    v4u f[4];
#pragma unroll
    for (int dt = 0; dt < 4; ++dt) { const v2u lo = tr_read(blk + s.aw[dt]), hi = tr_read(blk + s.aw[dt] + 2048); f[dt] = (v4u){lo.x, lo.y, hi.x, hi.y}; }
    WAIT_LGKM0();
#pragma unroll
    for (int dt = 0; dt < 4; ++dt) *(LAS v4u*)(blk + dt * 1024 + lane * 16) = f[dt];
    WAIT_LGKM0();
}

template <bool TYPE_A>
__device__ __forceinline__ void g1_loader(const Params& p, int panel, int ct, int off1, const W2Src& w2s, int lw, int lane STAMP_ARG) {
    unsigned vx[4], vw[4];
#pragma unroll
    for (int i = 0; i < 4; ++i) {
        const int r = lane >> 3, c = lane & 7, row = 32 * lw + 8 * i + r;
        vx[i] = (unsigned)(row * (D * 2) + ((c ^ ((row >> 1) & 7)) * 16));
        const int r2 = lane >> 4, c2 = lane & 15, k = 16 * lw + 4 * i + r2, phi = (k & 3) | (((k >> 3) & 1) << 2);
        vw[i] = (unsigned)(k * (F * 2) + ((c2 ^ (phi << 1)) * 16));
    }
    const char* xb = (const char*)p.q + (size_t)panel * 128 * D * 2;
    const char* wb = (const char*)p.w1 + (size_t)ct * 256;
    int kst = off1;
    auto issue = [&](int slot_i) {
        const unsigned slot = (unsigned)slot_i * G1_SLOT;
        const char* xs = xb + kst * 128; const char* wsrc = wb + (size_t)kst * (64 * F * 2);
#pragma unroll
        for (int i = 0; i < 4; ++i) glds16(vx[i], xs, slot + (32 * lw + 8 * i) * 128);
#pragma unroll
        for (int i = 0; i < 4; ++i) glds16(vw[i], wsrc, slot + 16384 + (16 * lw + 4 * i) * 256);
        kst = kst + 1 == G1_NST ? 0 : kst + 1;
    };
#pragma unroll
    for (int st = 0; st < 3; ++st) issue(st);
    WAIT_VM(16); BAR(); issue(3); issue(4);
#pragma unroll 1
    for (int i = 1; i < 6; ++i) { WAIT_VM(24); BAR(); issue(i - 1); }
    STAMP(1);
    if (TYPE_A) {
        WAIT_VM(24); BAR(); w2_issue(w2s, 0 + lw); w2_issue(w2s, 4 + lw);
        WAIT_VM(24); BAR(); w2_issue(w2s, 8 + lw); w2_issue(w2s, 12 + lw);
        WAIT_VM(24); BAR(); w2_issue(w2s, 16 + lw); w2_issue(w2s, 20 + lw);
        WAIT_VM(24); BAR(); w2_issue(w2s, 28 + lw);
    } else {
        WAIT_VM(24); BAR(); WAIT_VM(16); BAR(); WAIT_VM(8); BAR(); WAIT_VM(0); BAR();
    }
}
__device__ __forceinline__ void g1_store(__amdgpu_buffer_rsrc_t rs, int stg, int ksg, int lane, v4u hb) {
    const v4u o = {silu2(hb.x), silu2(hb.y), silu2(hb.z), silu2(hb.w)};
    st16_sc1(o, rs, WS_H + (stg * 48 + ksg) * 1024 + lane * 16);
}
struct ConvIn { v4u v[2], vp[2], x[2]; unsigned vq[2]; float Dd; };
__device__ __forceinline__ void conv_load_h(const Params& p, ConvIn& c, int ch, int hf, int lane) {
    const char* hsrc = (const char*)p.ssm + (size_t)ch * (S * 2);
#pragma unroll
    for (int j = 0; j < 2; ++j) {
        const int o = (2 * hf + j) * 1024 + lane * 16;
        c.v[j] = *(const GAS v4u*)(hsrc + o);
        c.vp[j] = *(const GAS v4u*)(hsrc + (o < 4 ? 0 : o - 4));
        c.vq[j] = *(const GAS unsigned*)(hsrc + (o < 8 ? 0 : o - 8));
    }
    c.Dd = bf_lo((unsigned)p.Dv[ch]);
}
__device__ __forceinline__ void conv_load_x(ConvIn& c, __amdgpu_buffer_rsrc_t rs, int ch, int hf, int lane) {
#pragma unroll
    for (int j = 0; j < 2; ++j) c.x[j] = __builtin_amdgcn_raw_buffer_load_b128(rs, (2 * hf + j) * 1024 + lane * 16, WS_KV + ch * (S * 2), AUX_SC1);
}
constexpr int CV_SYNC = 4128;
template <bool TYPE_A>
__device__ __forceinline__ void g1_compute(const Params& p, LAS char* L, __amdgpu_buffer_rsrc_t rs, int panel, int ct, int w, int lane, ConvIn& cin, int ch STAMP_ARG) {
    const int wf = w & 1, wsx = w >> 1;
    const int g = lane >> 4, q = (lane & 15) >> 2, pp = lane & 3, s = lane & 15;
    const int phi = q | ((g & 1) << 2);
    unsigned aw[4], ax[2];
#pragma unroll
    for (int ft = 0; ft < 4; ++ft) aw[ft] = 16384 + (8 * g + q) * 256 + (((4 * wf + ft) ^ phi) * 32) + 8 * pp;
#pragma unroll
    for (int ks = 0; ks < 2; ++ks) ax[ks] = (64 * wsx + s) * 128 + (((4 * ks + g) ^ ((s >> 1) & 7)) * 16);
    f32x4 acc[4][4];
#pragma unroll
    for (int a = 0; a < 4; ++a)
#pragma unroll
        for (int b = 0; b < 4; ++b) acc[a][b] = (f32x4){0.f, 0.f, 0.f, 0.f};
    v4u A[2][4], B[2][4];
#pragma unroll
    for (int a = 0; a < 4; ++a) { A[1][a] = (v4u){0u, 0u, 0u, 0u}; B[1][a] = (v4u){0u, 0u, 0u, 0u}; }
    auto rd = [&](int set, LAS char* base, int ks) {
#pragma unroll
        for (int ft = 0; ft < 4; ++ft) {
            const v2u lo = tr_read(base + aw[ft] + ks * 8192), hi = tr_read(base + aw[ft] + ks * 8192 + 1024);
            A[set][ft] = (v4u){lo.x, lo.y, hi.x, hi.y};
            if (ft == 0) {
#pragma unroll
                for (int sx = 0; sx < 4; ++sx) B[set][sx] = lds_rd16(base + ax[ks] + sx * 2048);
            }
        }
    };
    auto mm = [&](int set) {
#pragma unroll
        for (int ft = 0; ft < 4; ++ft)
#pragma unroll
            for (int sx = 0; sx < 4; ++sx) acc[ft][sx] = mfma16(A[set][ft], B[set][sx], acc[ft][sx]);
    };
    unsigned slot = 0;
#define X_G1REP 1
#pragma unroll 1
    for (int i = 0; i < G1_NST * X_G1REP; ++i) {
        LAS char* base = L + slot;
        slot = slot + G1_SLOT == G1_NS * G1_SLOT ? 0 : slot + G1_SLOT;
        BAR();
        if (i == 0) STAMP(1);
        rd(0, base, 0);
        mm(1);
        interleave<16, 12, 1>();
        SCHED();
        rd(1, base, 1);
        mm(0);
        interleave<16, 12, 1>();
        SCHED();
        WAIT_LGKM0();
    }
    mm(1);
    STAMP(2);
    if (!TYPE_A) conv_load_h(p, cin, ch, w & 1, lane);
    SCHED();
#pragma unroll
    for (int j = 0; j < 4; ++j) {
        const f32x4 a = acc[2 * (j & 1)][2 + (j >> 1)], b = acc[2 * (j & 1) + 1][2 + (j >> 1)];
        *(LAS v4u*)(L + G1_STG + w * 4096 + j * 1024 + lane * 16) = (v4u){pk_bf16(a[0], a[1]), pk_bf16(a[2], a[3]), pk_bf16(b[0], b[1]), pk_bf16(b[2], b[3])};
    }
    if (!TYPE_A && lane < 8) *(LAS unsigned*)(L + w * CV_PAIR + CV_SYNC + 4 * lane) = 0u;
    WAIT_LGKM0();
    BAR();
    if (!TYPE_A) conv_load_x(cin, rs, ch, w & 1, lane);
    SCHED();
#pragma unroll
    for (int j = 0; j < 4; ++j) {
        const f32x4 a = acc[2 * (j & 1)][j >> 1], b = acc[2 * (j & 1) + 1][j >> 1];
        const v4u o = {silu2f((f32x2){a[0], a[1]}), silu2f((f32x2){a[2], a[3]}), silu2f((f32x2){b[0], b[1]}), silu2f((f32x2){b[2], b[3]})};
        st16_sc1(o, rs, WS_H + ((panel * 8 + wsx * 4 + (j >> 1)) * 48 + ct * 4 + wf * 2 + (j & 1)) * 1024 + lane * 16);
    }
}
__device__ __forceinline__ void g1_loader_epilogue(LAS char* L, __amdgpu_buffer_rsrc_t rs, int panel, int ct, int lw, int lane) {
    const int wf = lw & 1, wsx = lw >> 1;
    v4u hb[4];
#pragma unroll
    for (int j = 0; j < 4; ++j) hb[j] = *(LAS v4u*)(L + G1_STG + lw * 4096 + j * 1024 + lane * 16);
#pragma unroll
    for (int j = 0; j < 4; ++j) g1_store(rs, panel * 8 + wsx * 4 + 2 + (j >> 1), ct * 4 + wf * 2 + (j & 1), lane, hb[j]);
}

__device__ __forceinline__ void prepass(const Params& p, LAS char* L, __amdgpu_buffer_rsrc_t rs, int wg, int tid STAMP_ARG) {
    const int s0 = 64 * (wg >> 1), c0 = 320 * (wg & 1);
    {
        const int w = __builtin_amdgcn_readfirstlane(tid >> 6);
        const int item = w < 4 ? tid : 256 + (tid & 63);
        const int cc = item % 40, rg = item / 40;
        const int ch = c0 + 8 * cc, r0 = s0 + 8 * rg;
        v4u kk[11], vv[8], sw[4];
#pragma unroll
        for (int j = 0; j < 4; ++j) sw[j] = *(const GAS v4u*)((const char*)p.shk + (size_t)ch * 8 + j * 16);
#pragma unroll
        for (int j = 0; j < 11; ++j) {
            const int row = r0 - 3 + j;
            kk[j] = *(const GAS v4u*)((const char*)p.k + (size_t)(row < 0 ? 0 : row) * (D * 2) + ch * 2);
        }
#pragma unroll
        for (int j = 0; j < 8; ++j) vv[j] = *(const GAS v4u*)((const char*)p.v + (size_t)(r0 + j) * (D * 2) + ch * 2);
        if (r0 == 0) { kk[0] = (v4u){0u, 0u, 0u, 0u}; kk[1] = kk[0]; kk[2] = kk[0]; }
#pragma unroll
        for (int e = 0; e < 4; ++e) {
            if (w >= 4 && w - 4 != e) continue;
            const f32x2 W0 = {bf_lo(sw[e].x), bf_lo(sw[e].z)}, W1 = {bf_hi(sw[e].x), bf_hi(sw[e].z)};
            const f32x2 W2 = {bf_lo(sw[e].y), bf_lo(sw[e].w)}, W3 = {bf_hi(sw[e].y), bf_hi(sw[e].w)};
            f32x2 kf[11], o[8];
#pragma unroll
            for (int j = 0; j < 11; ++j) kf[j] = bf_2(kk[j][e]);
#pragma unroll
            for (int j = 0; j < 8; ++j) {
                const f32x2 ksh = kf[j] * W3 + kf[j + 1] * W2 + kf[j + 2] * W1 + kf[j + 3] * W0;
                o[j] = ksh * bf_2(vv[j][e]);
            }
            const v4u p0 = {pk_bf16(o[0].x, o[1].x), pk_bf16(o[2].x, o[3].x), pk_bf16(o[4].x, o[5].x), pk_bf16(o[6].x, o[7].x)};
            const v4u p1 = {pk_bf16(o[0].y, o[1].y), pk_bf16(o[2].y, o[3].y), pk_bf16(o[4].y, o[5].y), pk_bf16(o[6].y, o[7].y)};
            *(LAS v4u*)(L + (8 * cc + 2 * e) * 128 + ((rg ^ (cc & 7)) * 16)) = p0;
            *(LAS v4u*)(L + (8 * cc + 2 * e + 1) * 128 + ((rg ^ (cc & 7)) * 16)) = p1;
        }
    }
    STAMP(1);
    WAIT_LGKM0();
    BAR();
#pragma unroll
    for (int j = 0; j < 5; ++j) {
        const int n = tid + NTHR * j, ch = n >> 3, pc = n & 7;
        const v4u val = *(LAS v4u*)(L + ch * 128 + ((pc ^ ((ch >> 3) & 7)) * 16));
        st16_sc1(val, rs, WS_KV + (c0 + ch) * (S * 2) + s0 * 2 + pc * 16);
    }
}
struct GateIn { v4u gw[5], q[5]; };
__device__ __forceinline__ void gate_load(const Params& p, GateIn& gi, int row0, int lane) {
    const int i = lane & 15, row = row0 + (lane >> 4);
#pragma unroll
    for (int j = 0; j < 5; ++j) {
        gi.gw[j] = *(const GAS v4u*)((const char*)p.gw + (i + 16 * j) * 16);
        gi.q[j] = *(const GAS v4u*)((const char*)p.q + (size_t)row * (D * 2) + (i + 16 * j) * 16);
    }
}
__device__ __forceinline__ void gate_finish(const GateIn& gi, __amdgpu_buffer_rsrc_t rs, int row0, int lane) {
    f32x2 a2 = {0.f, 0.f};
#pragma unroll
    for (int j = 0; j < 5; ++j)
#pragma unroll
        for (int e = 0; e < 4; ++e) a2 += bf_2(gi.q[j][e]) * bf_2(gi.gw[j][e]);
    const float z = rbf(row_sum16(a2.x + a2.y));
    const float gv = rbf(__builtin_amdgcn_rcpf(1.f + __builtin_amdgcn_exp2f(z * -1.44269504f)));
    if ((lane & 15) == 0) __builtin_amdgcn_raw_buffer_store_b32(__float_as_uint(gv), rs, WS_G + (row0 + (lane >> 4)) * 4, 0, AUX_SC1);
}

__device__ __forceinline__ void pair_sync(LAS char* words, int hf) {
    WAIT_LGKM0();
    *(volatile LAS unsigned*)(words + 4 * hf) = 1u;
#pragma unroll 1
    for (unsigned spins = 0; spins < (1u << 22); ++spins) { if (__builtin_amdgcn_readfirstlane(*(volatile LAS unsigned*)(words + 4 * (1 - hf))) != 0u) break; }
    asm volatile("" ::: "memory");
}
template <int hf>
__device__ __forceinline__ void conv_half(const Params& p, LAS char* Lw, LAS char* Lx, __amdgpu_buffer_rsrc_t rs, ConvIn& c, int ch, int chn, int round, int lane STAMP_ARG) {
    const int g = lane >> 4, q = (lane & 15) >> 2, pp = lane & 3, n = lane & 15;
    if (hf == 0) {
        if (lane < 30) *(LAS v4u*)(Lw + CV_X + lane * 16) = (v4u){0u, 0u, 0u, 0u};
        if (lane < 8) *(LAS v4u*)(Lw + (lane >> 1) * CV_COPY + (lane & 1) * 16) = (v4u){0u, 0u, 0u, 0u};
        if (lane == 0) { c.vp[0] = (v4u){0u, c.v[0].x, c.v[0].y, c.v[0].z}; c.vq[0] = 0u; }
    }
#pragma unroll
    for (int j = 0; j < 2; ++j) {
        const v4u v = c.v[j], vp = c.vp[j];
        const unsigned a0 = __builtin_amdgcn_alignbit(v.x, vp.x, 16), a1 = __builtin_amdgcn_alignbit(v.y, v.x, 16), a2 = __builtin_amdgcn_alignbit(v.z, v.y, 16), a3 = __builtin_amdgcn_alignbit(v.w, v.z, 16);
        const unsigned am = __builtin_amdgcn_alignbit(vp.x, c.vq[j], 16);
        LAS char* d = Lw + 32 + (2 * hf + j) * 1024 + lane * 16;
        *(LAS v4u*)(d) = v;
        *(LAS v4u*)(d + CV_COPY) = (v4u){a0, a1, a2, a3};
        *(LAS v4u*)(d + 2 * CV_COPY) = vp;
        *(LAS v4u*)(d + 3 * CV_COPY) = (v4u){am, a0, a1, a2};
        *(LAS v4u*)(Lw + CV_X + 480 + (2 * hf + j) * 1024 + lane * 16) = c.x[j];
    }
    const float Dd = c.Dd;
    pair_sync(Lw + CV_SYNC + 16 * round, hf);
    if (chn >= 0) { conv_load_h(p, c, chn, hf, lane); conv_load_x(c, rs, chn, hf, lane); asm volatile("" ::: "memory"); }
    if (round == 0) STAMP(8); else STAMP(12);
    const unsigned la = (unsigned)(size_t)Lw + q * CV_COPY + 32 - 16 * g + 8 * pp + 24 + 256 * hf;
    const unsigned lb = (unsigned)(size_t)Lw + CV_X + 32 * n + 16 * g + 448 - 256 * hf - 192;
    f32x4 acc[8];
#pragma unroll
    for (int t = 0; t < 8; ++t) acc[t] = (f32x4){0.f, 0.f, 0.f, 0.f};
    v2u Alo[2][8], Ahi[2][8];
    v4u B[2][8];
#define CV_RD(set, rs_, k) do { \
        if ((k) % 3 == 0) A_RDTR(Alo[set][(k) / 3], la, 64 * (rs_) + 512 * ((k) / 3) + 8); \
        else if ((k) % 3 == 1) A_RDTR(Ahi[set][(k) / 3], la, 64 * (rs_) + 512 * ((k) / 3)); \
        else A_RD128(B[set][(k) / 3], lb, 192 - 64 * (rs_) + 512 * ((k) / 3)); } while (0)
    auto step = [&](auto SET, auto NEXT) {
        constexpr int set = decltype(SET)::value, nx = decltype(NEXT)::value;
        int k = 0;
#pragma unroll
        for (int l = 0; l < 8; ++l)
#pragma unroll
            for (int i = 0; i + l < 8; ++i) {
                const v4u a = {Alo[set][i].x, Alo[set][i].y, Ahi[set][i].x, Ahi[set][i].y};
                A_MFMA(acc[i + l], a, B[set][l]);
                if (nx >= 0 && k < 24) { CV_RD(1 - set, (nx >= 0 ? nx : 0), k); }
                ++k;
            }
        if (nx >= 0) A_LGKM(0);
    };
#pragma unroll
    for (int k = 0; k < 24; ++k) CV_RD(0, 0, k);
    A_LGKM(0);
    step(std::integral_constant<int, 0>{}, std::integral_constant<int, 1>{});
    step(std::integral_constant<int, 1>{}, std::integral_constant<int, 2>{});
    step(std::integral_constant<int, 0>{}, std::integral_constant<int, 3>{});
    step(std::integral_constant<int, 1>{}, std::integral_constant<int, -1>{});
#undef CV_RD
    A_MFMA_DRAIN();
    if (round == 0) STAMP(9); else STAMP(13);
#pragma unroll
    for (int t = 0; t < 4; ++t) *(LAS f32x4*)(Lx + hf * 4096 + t * 1024 + lane * 16) = acc[4 * (1 - hf) + t];
    v2u xv[4];
#pragma unroll
    for (int t = 0; t < 4; ++t) xv[t] = *(LAS v2u*)(Lw + CV_X + 480 + 2 * (1024 * hf + 256 * t + 16 * n + 4 * g));
    pair_sync(Lw + CV_SYNC + 16 * round + 8, hf);
    f32x4 o[4];
#pragma unroll
    for (int t = 0; t < 4; ++t) o[t] = *(LAS f32x4*)(Lx + (1 - hf) * 4096 + t * 1024 + lane * 16);
    v2u ov[4];
#pragma unroll
    for (int t = 0; t < 4; ++t) {
        const f32x4 y = acc[4 * hf + t] + o[t];
        ov[t] = (v2u){pk2(bf_2(xv[t].x) * Dd + (f32x2){y[0], y[1]}), pk2(bf_2(xv[t].y) * Dd + (f32x2){y[2], y[3]})};
    }
    LAS char* Ly = Lx + (1 - hf) * 4096;
#pragma unroll
    for (int t = 0; t < 4; ++t) *(LAS v2u*)(Ly + 2 * (256 * t + 16 * n + 4 * g)) = ov[t];
    WAIT_LGKM0();
#pragma unroll
    for (int j = 0; j < 2; ++j) {
        const v4u val = *(LAS v4u*)(Ly + j * 1024 + lane * 16);
        st16_sc1(val, rs, WS_Y + ch * (S * 2) + hf * 2048 + j * 1024 + lane * 16);
    }
    if (round == 0) STAMP(10); else STAMP(14);
}

__device__ __forceinline__ void conv_rounds(const Params& p, LAS char* L, __amdgpu_buffer_rsrc_t rs, ConvIn& c, int ch, int chn, int pr, int hf, int lane STAMP_ARG) {
    LAS char* Lw = L + pr * CV_PAIR; LAS char* Lx = L + CV_EX + pr * 8192;
#pragma unroll 1
    for (int round = 0; round < 2; ++round) {
        if (hf) conv_half<1>(p, Lw, Lx, rs, c, ch, chn, round, lane STAMP_PASS); else conv_half<0>(p, Lw, Lx, rs, c, ch, chn, round, lane STAMP_PASS);
        if (chn < 0) break;
        ch = chn; chn = -1;
    }
}

__device__ __forceinline__ void g2_loader(const Params& p, LAS char* L, __amdgpu_buffer_rsrc_t rs, unsigned tag, const W2Src& w2s, int panel, int ct, int lw, int lane STAMP_ARG) {
    gu32* ctl = (gu32*)(p.ws + WS_CTL);
    const int grow = panel * 128 + (ct & 7) * 16 + 4 * lw;
    if (lw == 0) poll_flags(ctl, W_H + panel * 12, 12, tag, lane);
    w2_issue(w2s, 24 + lw);
    GateIn gi;
    gate_load(p, gi, grow, lane);
    asm volatile("" ::: "memory");
    w2_repack(w2s, L, 0 + lw, lane); w2_repack(w2s, L, 4 + lw, lane);
    STAMP(5);
    BAR();
    w2_repack(w2s, L, 8 + lw, lane); w2_repack(w2s, L, 12 + lw, lane);
    if (ct < 8) gate_finish(gi, rs, grow, lane);
    else WAIT_VM(0);
    BAR();
    w2_issue(w2s, 40 + lw); w2_issue(w2s, 44 + lw);
    w2_repack(w2s, L, 16 + lw, lane); w2_repack(w2s, L, 20 + lw, lane);
    WAIT_VM(8);
    BAR();
    if (ct < 8 && lw == 0 && lane == 0) set_flag(ctl, W_G + panel * 8 + ct, tag);
#pragma unroll
    for (int i = 0; i < 4; ++i) {
        const int r = lane >> 3, c = lane & 7, row = 32 * lw + 8 * i + r;
        glds16((unsigned)(row * (D * 2) + ((c ^ ((row >> 1) & 7)) * 16)), (const char*)p.q + (size_t)panel * 128 * D * 2 + ct * 128, G2_Q + (32 * lw + 8 * i) * 128);
    }
#pragma unroll 1
    for (int b = 24; b < 40; b += 4) w2_repack(w2s, L, b + lw, lane);
    BAR();
    WAIT_VM(4);
    w2_repack(w2s, L, 40 + lw, lane); w2_repack(w2s, L, 44 + lw, lane);
    BAR();
    const int y0 = ct < 7 ? (64 * ct) / 7 : 64 + (64 * ct - 448) / 6, y1 = ct < 7 ? (64 * ct + 63) / 7 : 64 + (64 * ct + 63 - 448) / 6;
    poll_flags(ctl, W_Y + y0, y1 - y0 + 1, tag, lane);
    poll_flags(ctl, W_G + panel * 8, 8, tag, lane);
    STAMP(6);
    v4u yv[4];
#pragma unroll
    for (int i = 0; i < 4; ++i) {
        const int r = lane >> 4, c = lane & 15, d = 16 * lw + 4 * i + r;
        yv[i] = __builtin_amdgcn_raw_buffer_load_b128(rs, d * (S * 2) + c * 16, WS_Y + ct * 64 * S * 2 + panel * 256, AUX_SC1);
    }
    const unsigned gv = __builtin_amdgcn_raw_buffer_load_b32(rs, (lw * 32 + (lane & 31)) * 4, WS_G + panel * 512, AUX_SC1);
#pragma unroll
    for (int i = 0; i < 4; ++i) {
        const int r = lane >> 4, c = lane & 15, d = 16 * lw + 4 * i + r;
        *(LAS v4u*)(L + G2_Y + d * 256 + ((c ^ ((d & 7) << 1)) * 16)) = yv[i];
    }
    if (lane < 32) *(LAS unsigned*)(L + G2_G + (lw * 32 + lane) * 4) = gv;
    WAIT_VM(0);
    WAIT_LGKM0();
    BAR();
}
__device__ __forceinline__ void g2_compute(const Params& p, LAS char* L, __amdgpu_buffer_rsrc_t rs, int panel, int ct, int off2, int w, int lane STAMP_ARG) {
    const int g = lane >> 4, q = (lane & 15) >> 2, pp = lane & 3, s = lane & 15;
    const unsigned hbase = WS_H + (panel * 8 + 2 * w) * (48 * 1024);
    int kld = off2;
    f32x4 acc[4][2];
#pragma unroll
    for (int a = 0; a < 4; ++a) { acc[a][0] = (f32x4){0.f, 0.f, 0.f, 0.f}; acc[a][1] = (f32x4){0.f, 0.f, 0.f, 0.f}; }
    v4u H[G2_HD][2][2];
    auto ldh = [&](int slot) {
#pragma unroll
        for (int ks = 0; ks < 2; ++ks)
#pragma unroll
            for (int sx = 0; sx < 2; ++sx) H[slot][ks][sx] = __builtin_amdgcn_raw_buffer_load_b128(rs, lane * 16 + sx * (48 * 1024) + ks * 1024, hbase + kld * 2048, H_AUX);
        kld = kld + 1 == G2_NST ? 0 : kld + 1;
    };
    BAR();
    STAMP(5);
#pragma unroll
    for (int st = 0; st < G2_HD - 1; ++st) ldh(st);
#pragma unroll
    for (int a = 0; a < 4; ++a) H[G2_HD - 1][a >> 1][a & 1] = (v4u){0u, 0u, 0u, 0u};
    SCHED();
    v4u A[2][4];
#pragma unroll
    for (int a = 0; a < 4; ++a) A[1][a] = (v4u){0u, 0u, 0u, 0u};
    auto half = [&](int set, int slot, int ks, unsigned addr, int off) {
        A_LGKM(3);
        A_MFMA(acc[0][0], A[set][0], H[slot][ks][0]); A_RD128(A[1 - set][0], addr, off);
        A_MFMA(acc[0][1], A[set][0], H[slot][ks][1]); A_RD128(A[1 - set][1], addr, off + 1024);
        A_LGKM(4);
        A_MFMA(acc[1][0], A[set][1], H[slot][ks][0]); A_RD128(A[1 - set][2], addr, off + 2048);
        A_MFMA(acc[1][1], A[set][1], H[slot][ks][1]); A_RD128(A[1 - set][3], addr, off + 3072);
        A_LGKM(5);
        A_MFMA(acc[2][0], A[set][2], H[slot][ks][0]);
        A_MFMA(acc[2][1], A[set][2], H[slot][ks][1]);
        A_LGKM(4);
        A_MFMA(acc[3][0], A[set][3], H[slot][ks][0]);
        A_MFMA(acc[3][1], A[set][3], H[slot][ks][1]);
    };
    const unsigned lbase = (unsigned)(size_t)L + lane * 16;
    unsigned sb = 0;
    auto stage = [&](int u, bool load, bool drain) {
        const unsigned addr = lbase + sb;
        half(1, (u + G2_HD - 1) % G2_HD, 1, addr, 0);
        if (load) { ldh((u + G2_HD - 1) % G2_HD); asm volatile("" ::: "memory"); }
        half(0, u, 0, addr, 4096);
        if (drain) WAIT_LGKM0();
        sb = sb + 8192 == G2_RING ? 0 : sb + 8192;
    };
#pragma unroll 1
    for (int jj = 0; jj < 2; ++jj) {
#pragma unroll
        for (int u = 0; u < 8; ++u) {
            if (u == 4 || (u == 0 && jj == 1)) BAR();
            if (u == 0) { if (jj == 0) STAMP(14); else STAMP(12); }
            stage(u, true, (u & 3) == 3);
        }
    }
    BAR();
    STAMP(13);
#pragma unroll
    for (int u = 0; u < 8; ++u) stage(u, u == 0, false);
    WAIT_LGKM0();
#pragma unroll
    for (int dt = 0; dt < 4; ++dt) { A_MFMA(acc[dt][0], A[1][dt], H[G2_HD - 1][1][0]); A_MFMA(acc[dt][1], A[1][dt], H[G2_HD - 1][1][1]); }
    A_MFMA_DRAIN();
    STAMP(6);
    BAR();
    STAMP(7);
    {
        float gg[2]; v2u yv[2][4], qv[2][4];
#pragma unroll
        for (int sx = 0; sx < 2; ++sx) {
            const int row = 32 * w + 16 * sx + s;
            gg[sx] = *(LAS float*)(L + G2_G + row * 4);
#pragma unroll
            for (int dt = 0; dt < 4; ++dt) {
                const int d = 16 * dt + 4 * g + q;
                yv[sx][dt] = tr_read(L + G2_Y + d * 256 + (((2 * w + sx) ^ (d & 7)) * 32) + 8 * pp);
                qv[sx][dt] = *(LAS v2u*)(L + G2_Q + row * 128 + (((2 * dt + (g >> 1)) ^ ((row >> 1) & 7)) * 16) + 8 * (g & 1));
            }
        }
        SCHED();
#pragma unroll
        for (int sx = 0; sx < 2; ++sx) {
            const int row = 32 * w + 16 * sx + s;
            const float omg = 1.f - gg[sx];
#pragma unroll
            for (int dt = 0; dt < 4; ++dt) {
                const f32x4 a = acc[dt][sx];
                const f32x2 o01 = bf_2(qv[sx][dt].x) * bf_2(yv[sx][dt].x) * gg[sx] + (f32x2){a[0], a[1]} * omg;
                const f32x2 o23 = bf_2(qv[sx][dt].y) * bf_2(yv[sx][dt].y) * gg[sx] + (f32x2){a[2], a[3]} * omg;
                *(LAS v2u*)(L + G2_Q + row * 128 + (((2 * dt + (g >> 1)) ^ ((row >> 1) & 7)) * 16) + 8 * (g & 1)) = (v2u){pk2(o01), pk2(o23)};
            }
        }
    }
    WAIT_LGKM0();
#pragma unroll
    for (int i = 0; i < 4; ++i) {
        const int row = 32 * w + 8 * i + (lane >> 3), c = lane & 7;
        const v4u val = *(LAS v4u*)(L + G2_Q + row * 128 + c * 16);
        *(GAS v4u*)((char*)p.out + (size_t)(panel * 128 + row) * (D * 2) + ct * 128 + ((c ^ ((row >> 1) & 7)) * 16)) = val;
    }
    STAMP(11);
}

__global__ void __launch_bounds__(NTHR, 2) k_fused(Params p) {
    extern __shared__ __attribute__((aligned(16))) char smem[];
    LAS char* L = (LAS char*)smem;
    const int tid = threadIdx.x, lane = tid & 63, w = __builtin_amdgcn_readfirstlane(tid >> 6);
    const int x = blockIdx.x & 7, idx = blockIdx.x >> 3, xr = x >> 1, xc = x & 1;
    STAMP(0);
    gu32* ctl = (gu32*)(p.ws + WS_CTL);
    unsigned epoch_v = __hip_atomic_load(ctl + W_EPOCH * FL, RLX_AGENT);
#define GET_TAG() asm volatile("" : "+v"(epoch_v)); const unsigned tag = __builtin_amdgcn_readfirstlane(epoch_v) + 1u
    const __amdgpu_buffer_rsrc_t rs = __builtin_amdgcn_make_buffer_rsrc(p.ws, 0, WS_END, 0x00020000);
    if (idx < 24) {
        const int panel = 4 * xr + idx / 6, ct = 6 * xc + idx % 6;
        const int off1 = 5 * ((idx / 6 + idx % 6) & 1);
        const bool typeA = idx < 20;
        const int panel2 = 4 * xr + idx / 5, ct2 = 5 * xc + idx % 5;
        const int off2 = 12 * ((idx / 5 + idx % 5) & 1);
        if (typeA) {
            if (w < 4) {
                ConvIn cin;
                g1_compute<true>(p, L, rs, panel, ct, w, lane, cin, 0 STAMP_PASS);
                STAMP(3);
                WAIT_VM(0);
                GET_TAG();
                STAMP(4);
                BAR();
                if (tid == 0) set_flag(ctl, W_H + panel * 12 + ct, tag);
                g2_compute(p, L, rs, panel2, ct2, off2, w, lane STAMP_PASS);
            } else {
                const int lw = w - 4;
                W2Src w2s;
                w2_init(w2s, p, ct2, off2, lane);
                g1_loader<true>(p, panel, ct, off1, w2s, lw, lane STAMP_PASS);
                BAR();
                g1_loader_epilogue(L, rs, panel, ct, lw, lane);
                w2_issue(w2s, 32 + lw); w2_issue(w2s, 36 + lw);
                WAIT_VM(8);
                GET_TAG();
                BAR();
                g2_loader(p, L, rs, tag, w2s, panel2, ct2, lw, lane STAMP_PASS);
            }
        } else {
            const int bw = x * 4 + idx - 20, pr = w >> 1, hf = w & 1;
            const int ch = 448 + 6 * bw + pr, chn = pr < 2 ? ch + 4 : -1;
            ConvIn cin;
            if (w < 4) {
                g1_compute<false>(p, L, rs, panel, ct, w, lane, cin, ch STAMP_PASS);
                STAMP(3);
            } else {
                W2Src w2s;
                g1_loader<false>(p, panel, ct, off1, w2s, w - 4, lane STAMP_PASS);
                conv_load_h(p, cin, ch, hf, lane);
                { GET_TAG(); if (w == 7) poll_flags(ctl, W_KV, 64, tag, lane); }
                BAR();
                conv_load_x(cin, rs, ch, hf, lane);
                g1_loader_epilogue(L, rs, panel, ct, w - 4, lane);
            }
            WAIT_VM(0);
            GET_TAG();
            STAMP(4);
            BAR();
            if (tid == 0) set_flag(ctl, W_H + panel * 12 + ct, tag);
            conv_rounds(p, L, rs, cin, ch, chn, pr, hf, lane STAMP_PASS);
            WAIT_VM(0);
            BAR();
            if (tid == 0) set_flag(ctl, W_Y + 64 + bw, tag);
            STAMP(16);
        }
    } else {
        const int cw = x * 8 + (idx - 24), pr = w >> 1, hf = w & 1;
        const int ch = 7 * cw + pr, chn = pr < 3 ? ch + 4 : -1;
        ConvIn cin;
        conv_load_h(p, cin, ch, hf, lane);
        prepass(p, L, rs, cw, tid STAMP_PASS);
        STAMP(2);
        WAIT_VM(0);
        GET_TAG();
        BAR();
        if (tid == 0) set_flag(ctl, W_KV + cw, tag);
        if (hf == 0 && lane < 8) *(LAS unsigned*)(L + pr * CV_PAIR + CV_SYNC + 4 * lane) = 0u;
        STAMP(3);
        if (w == 1) poll_flags(ctl, W_KV, 64, tag, lane);
        WAIT_LGKM0();
        BAR();
        STAMP(4);
        conv_load_x(cin, rs, ch, hf, lane);
        conv_rounds(p, L, rs, cin, ch, chn, pr, hf, lane STAMP_PASS);
        WAIT_VM(0);
        BAR();
        if (tid == 0) set_flag(ctl, W_Y + cw, tag);
        STAMP(16);
        if (w == 7 && blockIdx.x == 255) {
#pragma unroll 1
            for (int pn = 0; pn < 3; ++pn) poll_flags(ctl, W_H + pn * 64, 64, tag, lane);
            if (lane == 0) set_flag(ctl, W_EPOCH, tag);
        }
    }
    STAMP_FLUSH();
}

extern "C" void kernel_launch(void* const* d_in, const int* in_sizes, int n_in, void* d_out, int out_size, void* d_ws, size_t ws_size, hipStream_t stream) {
    Params p;
    p.q = (const u16*)d_in[0]; p.k = (const u16*)d_in[1]; p.v = (const u16*)d_in[2]; p.shk = (const u16*)d_in[3];
    p.ssm = (const u16*)d_in[4]; p.Dv = (const u16*)d_in[5]; p.w1 = (const u16*)d_in[7]; p.w2 = (const u16*)d_in[8]; p.gw = (const u16*)d_in[9];
    p.out = (u16*)d_out; p.ws = (char*)d_ws;
    (void)hipFuncSetAttribute((const void*)k_fused, hipFuncAttributeMaxDynamicSharedMemorySize, LDS_BYTES);
    k_fused<<<256, NTHR, LDS_BYTES, stream>>>(p);
}
```

```cpp
#include <hip/hip_runtime.h>
#include <stdint.h>
#include <type_traits>

typedef unsigned short u16;
typedef unsigned v4u __attribute__((ext_vector_type(4)));
typedef unsigned v2u __attribute__((ext_vector_type(2)));
typedef float f32x4 __attribute__((ext_vector_type(4)));
typedef float f32x2 __attribute__((ext_vector_type(2)));
typedef short bf16x8 __attribute__((ext_vector_type(8)));
typedef short s16x4 __attribute__((ext_vector_type(4)));
typedef __bf16 bf16x2_t __attribute__((ext_vector_type(2)));
#define LAS __attribute__((address_space(3)))
#define GAS __attribute__((address_space(1)))
typedef GAS unsigned gu32;
typedef GAS unsigned long long gu64;
#define RLX_AGENT __ATOMIC_RELAXED, __HIP_MEMORY_SCOPE_AGENT

constexpr int S = 2048, D = 640, F = 1536;
constexpr int NTHR = 512;
constexpr unsigned WS_H = 0;
constexpr unsigned WS_KV = WS_H + S * F * 2;
constexpr unsigned WS_Y = WS_KV + S * D * 2;
constexpr unsigned WS_G = WS_Y + S * D * 2;
constexpr unsigned WS_CTL = WS_G + S * 4;
constexpr int FL = 32;
constexpr int W_EPOCH = 0, W_H = 1, W_KV = W_H + 192, W_G = W_KV + 64, W_Y = W_G + 128, W_LINES = W_Y + 96;
constexpr unsigned WS_END = WS_CTL + W_LINES * 128;
constexpr int AUX_SC1 = 16;
constexpr int H_AUX = 16;
constexpr unsigned kSpinCap = 1u << 18;

struct Params {
    const u16 *q, *k, *v, *shk, *ssm, *Dv, *w1, *w2, *gw;
    u16* out;
    char* ws;
};

__device__ __forceinline__ float bf_lo(unsigned u) { return __uint_as_float(u << 16); }
__device__ __forceinline__ float bf_hi(unsigned u) { return __uint_as_float(u & 0xffff0000u); }
__device__ __forceinline__ f32x2 bf_2(unsigned u) { return (f32x2){bf_lo(u), bf_hi(u)}; }
__device__ __forceinline__ unsigned pk2(f32x2 v) { return __builtin_bit_cast(unsigned, __builtin_convertvector(v, bf16x2_t)); }
__device__ __forceinline__ unsigned pk_bf16(float lo, float hi) { return pk2((f32x2){lo, hi}); }
__device__ __forceinline__ float rbf(float x) { return bf_lo(pk_bf16(x, x)); }
__device__ __forceinline__ f32x4 mfma16(v4u a, v4u b, f32x4 c) { return __builtin_amdgcn_mfma_f32_16x16x32_bf16(__builtin_bit_cast(bf16x8, a), __builtin_bit_cast(bf16x8, b), c, 0, 0, 0); }
__device__ __forceinline__ v2u tr_read(LAS char* p) { return __builtin_bit_cast(v2u, __builtin_amdgcn_ds_read_tr16_b64_v4i16((LAS s16x4*)p)); }
__device__ __forceinline__ v4u lds_rd16(LAS char* p) { return *(LAS v4u*)p; }
__device__ __forceinline__ void glds16(unsigned voff, const void* sbase, unsigned lds_dst) {
    asm volatile("s_mov_b32 m0, %2\n\ts_nop 0\n\tglobal_load_lds_dwordx4 %0, %1" :: "v"(voff), "s"(sbase), "s"(lds_dst) : "memory");
}
template <bool NT> __device__ __forceinline__ void glds16t(unsigned voff, const void* sbase, unsigned lds_dst) {
    if (NT) asm volatile("s_mov_b32 m0, %2\n\ts_nop 0\n\tglobal_load_lds_dwordx4 %0, %1 nt" :: "v"(voff), "s"(sbase), "s"(lds_dst) : "memory");
    else glds16(voff, sbase, lds_dst);
}
#define NT_X 0
#define NT_W1 0
#define NT_W2 0
#define NT_KV 0
#define NT_SSM 0
template <bool NT> __device__ __forceinline__ v4u gld16(const void* ptr) {
    if (NT) return __builtin_nontemporal_load((const GAS v4u*)ptr);
    return *(const GAS v4u*)ptr;
}
#define WAIT_VM(n) asm volatile("s_waitcnt vmcnt(%0)" :: "n"(n) : "memory")
#define WAIT_LGKM0() asm volatile("s_waitcnt lgkmcnt(0)" ::: "memory")
#define BAR() do { asm volatile("" ::: "memory"); __builtin_amdgcn_s_barrier(); asm volatile("" ::: "memory"); } while (0)
#define SCHED() __builtin_amdgcn_sched_barrier(0)
#define A_MFMA(acc, a, b) asm volatile("v_mfma_f32_16x16x32_bf16 %0, %1, %2, %0" : "+v"(acc) : "v"(a), "v"(b))
#define A_RD128(dst, addr, off) asm volatile("ds_read_b128 %0, %1 offset:%2" : "=v"(dst) : "v"(addr), "n"(off))
#define A_RDTR(dst, addr, off) asm volatile("ds_read_b64_tr_b16 %0, %1 offset:%2" : "=v"(dst) : "v"(addr), "n"(off))
#define A_LGKM(n) asm volatile("s_waitcnt lgkmcnt(%0)" :: "n"(n))
#define A_MFMA_DRAIN() asm volatile("s_nop 15\n\ts_nop 7" ::: "memory")
template <int NM, int NR, int PER> __device__ __forceinline__ void interleave() {
#pragma unroll
    for (int i = 0; i < NM; ++i) {
        __builtin_amdgcn_sched_group_barrier(0x8, 1, 0);
        if (i * PER < NR) __builtin_amdgcn_sched_group_barrier(0x100, PER, 0);
    }
}
#define STAMP(id) do {} while (0)
#define STAMP_ARG
#define STAMP_PASS
#define STAMP_FLUSH() do {} while (0)

__device__ __forceinline__ void st16_sc1(v4u v, __amdgpu_buffer_rsrc_t rs, unsigned off) { __builtin_amdgcn_raw_buffer_store_b128(v, rs, off, 0, AUX_SC1); }
__device__ __forceinline__ void poll_flags(gu32* ctl, int line, int n, unsigned tag, int lane) {
    gu32* f = ctl + (line + (lane < n ? lane : 0)) * FL;
#pragma unroll 1
    for (unsigned spins = 0; spins < kSpinCap; ++spins) {
        const unsigned v = __hip_atomic_load(f, RLX_AGENT);
        if (__all(v == tag)) break;
        __builtin_amdgcn_s_sleep(4);
    }
    __builtin_amdgcn_fence(__ATOMIC_ACQUIRE, "wavefront");
}
__device__ __forceinline__ void set_flag(gu32* ctl, int line, unsigned tag) { __hip_atomic_store(ctl + line * FL, tag, RLX_AGENT); }
__device__ __forceinline__ unsigned silu2f(f32x2 h) {
    const f32x2 t = h * -1.44269504f;
    const f32x2 den = (f32x2){__builtin_amdgcn_exp2f(t.x), __builtin_amdgcn_exp2f(t.y)} + 1.f;
    const f32x2 r = (f32x2){__builtin_amdgcn_rcpf(den.x), __builtin_amdgcn_rcpf(den.y)};
    return pk2(h * r);
}
__device__ __forceinline__ unsigned silu2(unsigned hb) { return silu2f(bf_2(hb)); }
__device__ __forceinline__ float row_sum16(float v) {
    v += __builtin_bit_cast(float, __builtin_amdgcn_update_dpp(0, __builtin_bit_cast(int, v), 0x128, 0xf, 0xf, false));
    v += __builtin_bit_cast(float, __builtin_amdgcn_update_dpp(0, __builtin_bit_cast(int, v), 0x124, 0xf, 0xf, false));
    v += __builtin_bit_cast(float, __builtin_amdgcn_update_dpp(0, __builtin_bit_cast(int, v), 0x122, 0xf, 0xf, false));
    v += __builtin_bit_cast(float, __builtin_amdgcn_update_dpp(0, __builtin_bit_cast(int, v), 0x121, 0xf, 0xf, false));
    return v;
}

constexpr int G1_SLOT = 32768, G1_NS = 5, G1_NST = D / 64, G1_STG = 3 * G1_SLOT;
constexpr int G2_NST = F / 64, G2_RING = 40 * 4096, G2_HD = 8;
constexpr int G2_Q = 8 * 4096, G2_Y = 12 * 4096, G2_G = 16 * 4096;
constexpr int CV_COPY = 4160, CV_X = 4 * CV_COPY, CV_PAIR = CV_X + 4608;
constexpr int CV_EX = 4 * CV_PAIR;
constexpr int LDS_BYTES = 163840;
static_assert(CV_EX + 4 * 8192 <= LDS_BYTES, "LDS map");

struct W2Src { unsigned vw[4]; const char* base; int kb0; unsigned aw[4]; };
__device__ __forceinline__ void w2_init(W2Src& s, const Params& p, int ct, int off2, int lane) {
#pragma unroll
    for (int i = 0; i < 4; ++i) {
        const int r = lane >> 3, c = lane & 7, k = 8 * i + r;
        s.vw[i] = (unsigned)(k * (D * 2) + ((c ^ (((k >> 1) & 3) << 1)) * 16));
    }
    const int g = lane >> 4, q = (lane & 15) >> 2, pp = lane & 3, sw = ((g & 1) << 1) | (q >> 1);
#pragma unroll
    for (int dt = 0; dt < 4; ++dt) s.aw[dt] = (4 * g + q) * 128 + ((dt ^ sw) * 32) + 8 * pp;
    s.base = (const char*)p.w2 + ct * 128; s.kb0 = 2 * off2;
}
__device__ __forceinline__ void w2_issue(const W2Src& s, int b) {
    const int kb = s.kb0 + b >= 48 ? s.kb0 + b - 48 : s.kb0 + b;
    const char* src = s.base + (size_t)kb * (32 * D * 2);
    const unsigned slot = (unsigned)(b >= 40 ? b - 40 : b) * 4096;
#pragma unroll
    for (int i = 0; i < 4; ++i) glds16t<NT_W2>(s.vw[i], src, slot + i * 1024);
}
__device__ __forceinline__ void w2_repack(const W2Src& s, LAS char* L, int b, int lane) {
    LAS char* blk = L + (b >= 40 ? b - 40 : b) * 4096;
    v4u f[4];
#pragma unroll
    for (int dt = 0; dt < 4; ++dt) { const v2u lo = tr_read(blk + s.aw[dt]), hi = tr_read(blk + s.aw[dt] + 2048); f[dt] = (v4u){lo.x, lo.y, hi.x, hi.y}; }
    WAIT_LGKM0();
#pragma unroll
    for (int dt = 0; dt < 4; ++dt) *(LAS v4u*)(blk + dt * 1024 + lane * 16) = f[dt];
    WAIT_LGKM0();
}

template <bool TYPE_A>
__device__ __forceinline__ void g1_loader(const Params& p, int panel, int ct, int off1, const W2Src& w2s, int lw, int lane STAMP_ARG) {
    unsigned vx[4], vw[4];
#pragma unroll
    for (int i = 0; i < 4; ++i) {
        const int r = lane >> 3, c = lane & 7, row = 32 * lw + 8 * i + r;
        vx[i] = (unsigned)(row * (D * 2) + ((c ^ ((row >> 1) & 7)) * 16));
        const int r2 = lane >> 4, c2 = lane & 15, k = 16 * lw + 4 * i + r2, phi = (k & 3) | (((k >> 3) & 1) << 2);
        vw[i] = (unsigned)(k * (F * 2) + ((c2 ^ (phi << 1)) * 16));
    }
    const char* xb = (const char*)p.q + (size_t)panel * 128 * D * 2;
    const char* wb = (const char*)p.w1 + (size_t)ct * 256;
    int kst = off1;
    auto issue = [&](int slot_i) {
        const unsigned slot = (unsigned)slot_i * G1_SLOT;
        const char* xs = xb + kst * 128; const char* wsrc = wb + (size_t)kst * (64 * F * 2);
#pragma unroll
        for (int i = 0; i < 4; ++i) glds16t<NT_X>(vx[i], xs, slot + (32 * lw + 8 * i) * 128);
#pragma unroll
        for (int i = 0; i < 4; ++i) glds16t<NT_W1>(vw[i], wsrc, slot + 16384 + (16 * lw + 4 * i) * 256);
        kst = kst + 1 == G1_NST ? 0 : kst + 1;
    };
#pragma unroll
    for (int st = 0; st < 3; ++st) issue(st);
    WAIT_VM(16); BAR(); issue(3); issue(4);
#pragma unroll 1
    for (int i = 1; i < 6; ++i) { WAIT_VM(24); BAR(); issue(i - 1); }
    STAMP(1);
    if (TYPE_A) {
        WAIT_VM(24); BAR(); w2_issue(w2s, 0 + lw); w2_issue(w2s, 4 + lw);
        WAIT_VM(24); BAR(); w2_issue(w2s, 8 + lw); w2_issue(w2s, 12 + lw);
        WAIT_VM(24); BAR(); w2_issue(w2s, 16 + lw); w2_issue(w2s, 20 + lw);
        WAIT_VM(24); BAR(); w2_issue(w2s, 28 + lw);
    } else {
        WAIT_VM(24); BAR(); WAIT_VM(16); BAR(); WAIT_VM(8); BAR(); WAIT_VM(0); BAR();
    }
}
__device__ __forceinline__ void g1_store(__amdgpu_buffer_rsrc_t rs, int stg, int ksg, int lane, v4u hb) {
    const v4u o = {silu2(hb.x), silu2(hb.y), silu2(hb.z), silu2(hb.w)};
    st16_sc1(o, rs, WS_H + (stg * 48 + ksg) * 1024 + lane * 16);
}
struct ConvIn { v4u v[2], vp[2], x[2]; unsigned vq[2]; float Dd; };
__device__ __forceinline__ void conv_load_h(const Params& p, ConvIn& c, int ch, int hf, int lane) {
    const char* hsrc = (const char*)p.ssm + (size_t)ch * (S * 2);
#pragma unroll
    for (int j = 0; j < 2; ++j) {
        const int o = (2 * hf + j) * 1024 + lane * 16;
        c.v[j] = gld16<NT_SSM>(hsrc + o);
        c.vp[j] = gld16<NT_SSM>(hsrc + (o < 4 ? 0 : o - 4));
        c.vq[j] = *(const GAS unsigned*)(hsrc + (o < 8 ? 0 : o - 8));
    }
    c.Dd = bf_lo((unsigned)p.Dv[ch]);
}
__device__ __forceinline__ void conv_load_x(ConvIn& c, __amdgpu_buffer_rsrc_t rs, int ch, int hf, int lane) {
#pragma unroll
    for (int j = 0; j < 2; ++j) c.x[j] = __builtin_amdgcn_raw_buffer_load_b128(rs, (2 * hf + j) * 1024 + lane * 16, WS_KV + ch * (S * 2), AUX_SC1);
}
constexpr int CV_SYNC = 4128;
template <bool TYPE_A>
__device__ __forceinline__ void g1_compute(const Params& p, LAS char* L, __amdgpu_buffer_rsrc_t rs, int panel, int ct, int w, int lane, ConvIn& cin, int ch STAMP_ARG) {
    const int wf = w & 1, wsx = w >> 1;
    const int g = lane >> 4, q = (lane & 15) >> 2, pp = lane & 3, s = lane & 15;
    const int phi = q | ((g & 1) << 2);
    unsigned aw[4], ax[2];
#pragma unroll
    for (int ft = 0; ft < 4; ++ft) aw[ft] = 16384 + (8 * g + q) * 256 + (((4 * wf + ft) ^ phi) * 32) + 8 * pp;
#pragma unroll
    for (int ks = 0; ks < 2; ++ks) ax[ks] = (64 * wsx + s) * 128 + (((4 * ks + g) ^ ((s >> 1) & 7)) * 16);
    f32x4 acc[4][4];
#pragma unroll
    for (int a = 0; a < 4; ++a)
#pragma unroll
        for (int b = 0; b < 4; ++b) acc[a][b] = (f32x4){0.f, 0.f, 0.f, 0.f};
    v4u A[2][4], B[2][4];
#pragma unroll
    for (int a = 0; a < 4; ++a) { A[1][a] = (v4u){0u, 0u, 0u, 0u}; B[1][a] = (v4u){0u, 0u, 0u, 0u}; }
    auto rd = [&](int set, LAS char* base, int ks) {
#pragma unroll
        for (int ft = 0; ft < 4; ++ft) {
            const v2u lo = tr_read(base + aw[ft] + ks * 8192), hi = tr_read(base + aw[ft] + ks * 8192 + 1024);
            A[set][ft] = (v4u){lo.x, lo.y, hi.x, hi.y};
            if (ft == 0) {
#pragma unroll
                for (int sx = 0; sx < 4; ++sx) B[set][sx] = lds_rd16(base + ax[ks] + sx * 2048);
            }
        }
    };
    auto mm = [&](int set) {
#pragma unroll
        for (int ft = 0; ft < 4; ++ft)
#pragma unroll
            for (int sx = 0; sx < 4; ++sx) acc[ft][sx] = mfma16(A[set][ft], B[set][sx], acc[ft][sx]);
    };
    unsigned slot = 0;
#define X_G1REP 1
#pragma unroll 1
    for (int i = 0; i < G1_NST * X_G1REP; ++i) {
        LAS char* base = L + slot;
        slot = slot + G1_SLOT == G1_NS * G1_SLOT ? 0 : slot + G1_SLOT;
        BAR();
        if (i == 0) STAMP(1);
        rd(0, base, 0);
        mm(1);
        interleave<16, 12, 1>();
        SCHED();
        rd(1, base, 1);
        mm(0);
        interleave<16, 12, 1>();
        SCHED();
        WAIT_LGKM0();
    }
    mm(1);
    STAMP(2);
    if (!TYPE_A) conv_load_h(p, cin, ch, w & 1, lane);
    SCHED();
#pragma unroll
    for (int j = 0; j < 4; ++j) {
        const f32x4 a = acc[2 * (j & 1)][2 + (j >> 1)], b = acc[2 * (j & 1) + 1][2 + (j >> 1)];
        *(LAS v4u*)(L + G1_STG + w * 4096 + j * 1024 + lane * 16) = (v4u){pk_bf16(a[0], a[1]), pk_bf16(a[2], a[3]), pk_bf16(b[0], b[1]), pk_bf16(b[2], b[3])};
    }
    if (!TYPE_A && lane < 8) *(LAS unsigned*)(L + w * CV_PAIR + CV_SYNC + 4 * lane) = 0u;
    WAIT_LGKM0();
    BAR();
    if (!TYPE_A) conv_load_x(cin, rs, ch, w & 1, lane);
    SCHED();
#pragma unroll
    for (int j = 0; j < 4; ++j) {
        const f32x4 a = acc[2 * (j & 1)][j >> 1], b = acc[2 * (j & 1) + 1][j >> 1];
        const v4u o = {silu2f((f32x2){a[0], a[1]}), silu2f((f32x2){a[2], a[3]}), silu2f((f32x2){b[0], b[1]}), silu2f((f32x2){b[2], b[3]})};
        st16_sc1(o, rs, WS_H + ((panel * 8 + wsx * 4 + (j >> 1)) * 48 + ct * 4 + wf * 2 + (j & 1)) * 1024 + lane * 16);
    }
}
__device__ __forceinline__ void g1_loader_epilogue(LAS char* L, __amdgpu_buffer_rsrc_t rs, int panel, int ct, int lw, int lane) {
    const int wf = lw & 1, wsx = lw >> 1;
    v4u hb[4];
#pragma unroll
    for (int j = 0; j < 4; ++j) hb[j] = *(LAS v4u*)(L + G1_STG + lw * 4096 + j * 1024 + lane * 16);
#pragma unroll
    for (int j = 0; j < 4; ++j) g1_store(rs, panel * 8 + wsx * 4 + 2 + (j >> 1), ct * 4 + wf * 2 + (j & 1), lane, hb[j]);
}

__device__ __forceinline__ void prepass(const Params& p, LAS char* L, __amdgpu_buffer_rsrc_t rs, int wg, int tid STAMP_ARG) {
    const int s0 = 64 * (wg >> 1), c0 = 320 * (wg & 1);
    {
        const int w = __builtin_amdgcn_readfirstlane(tid >> 6);
        const int item = w < 4 ? tid : 256 + (tid & 63);
        const int cc = item % 40, rg = item / 40;
        const int ch = c0 + 8 * cc, r0 = s0 + 8 * rg;
        v4u kk[11], vv[8], sw[4];
#pragma unroll
        for (int j = 0; j < 4; ++j) sw[j] = *(const GAS v4u*)((const char*)p.shk + (size_t)ch * 8 + j * 16);
#pragma unroll
        for (int j = 0; j < 11; ++j) {
            const int row = r0 - 3 + j;
            kk[j] = gld16<NT_KV>((const char*)p.k + (size_t)(row < 0 ? 0 : row) * (D * 2) + ch * 2);
        }
#pragma unroll
        for (int j = 0; j < 8; ++j) vv[j] = gld16<NT_KV>((const char*)p.v + (size_t)(r0 + j) * (D * 2) + ch * 2);
        if (r0 == 0) { kk[0] = (v4u){0u, 0u, 0u, 0u}; kk[1] = kk[0]; kk[2] = kk[0]; }
#pragma unroll
        for (int e = 0; e < 4; ++e) {
            if (w >= 4 && w - 4 != e) continue;
            const f32x2 W0 = {bf_lo(sw[e].x), bf_lo(sw[e].z)}, W1 = {bf_hi(sw[e].x), bf_hi(sw[e].z)};
            const f32x2 W2 = {bf_lo(sw[e].y), bf_lo(sw[e].w)}, W3 = {bf_hi(sw[e].y), bf_hi(sw[e].w)};
            f32x2 kf[11], o[8];
#pragma unroll
            for (int j = 0; j < 11; ++j) kf[j] = bf_2(kk[j][e]);
#pragma unroll
            for (int j = 0; j < 8; ++j) {
                const f32x2 ksh = kf[j] * W3 + kf[j + 1] * W2 + kf[j + 2] * W1 + kf[j + 3] * W0;
                o[j] = ksh * bf_2(vv[j][e]);
            }
            const v4u p0 = {pk_bf16(o[0].x, o[1].x), pk_bf16(o[2].x, o[3].x), pk_bf16(o[4].x, o[5].x), pk_bf16(o[6].x, o[7].x)};
            const v4u p1 = {pk_bf16(o[0].y, o[1].y), pk_bf16(o[2].y, o[3].y), pk_bf16(o[4].y, o[5].y), pk_bf16(o[6].y, o[7].y)};
            *(LAS v4u*)(L + (8 * cc + 2 * e) * 128 + ((rg ^ (cc & 7)) * 16)) = p0;
            *(LAS v4u*)(L + (8 * cc + 2 * e + 1) * 128 + ((rg ^ (cc & 7)) * 16)) = p1;
        }
    }
    STAMP(1);
    WAIT_LGKM0();
    BAR();
#pragma unroll
    for (int j = 0; j < 5; ++j) {
        const int n = tid + NTHR * j, ch = n >> 3, pc = n & 7;
        const v4u val = *(LAS v4u*)(L + ch * 128 + ((pc ^ ((ch >> 3) & 7)) * 16));
        st16_sc1(val, rs, WS_KV + (c0 + ch) * (S * 2) + s0 * 2 + pc * 16);
    }
}
struct GateIn { v4u gw[5], q[5]; };
__device__ __forceinline__ void gate_load(const Params& p, GateIn& gi, int row0, int lane) {
    const int i = lane & 15, row = row0 + (lane >> 4);
#pragma unroll
    for (int j = 0; j < 5; ++j) {
        gi.gw[j] = *(const GAS v4u*)((const char*)p.gw + (i + 16 * j) * 16);
        gi.q[j] = *(const GAS v4u*)((const char*)p.q + (size_t)row * (D * 2) + (i + 16 * j) * 16);
    }
}
__device__ __forceinline__ void gate_finish(const GateIn& gi, __amdgpu_buffer_rsrc_t rs, int row0, int lane) {
    f32x2 a2 = {0.f, 0.f};
#pragma unroll
    for (int j = 0; j < 5; ++j)
#pragma unroll
        for (int e = 0; e < 4; ++e) a2 += bf_2(gi.q[j][e]) * bf_2(gi.gw[j][e]);
    const float z = rbf(row_sum16(a2.x + a2.y));
    const float gv = rbf(__builtin_amdgcn_rcpf(1.f + __builtin_amdgcn_exp2f(z * -1.44269504f)));
    if ((lane & 15) == 0) __builtin_amdgcn_raw_buffer_store_b32(__float_as_uint(gv), rs, WS_G + (row0 + (lane >> 4)) * 4, 0, AUX_SC1);
}

__device__ __forceinline__ void pair_sync(LAS char* words, int hf) {
    WAIT_LGKM0();
    *(volatile LAS unsigned*)(words + 4 * hf) = 1u;
#pragma unroll 1
    for (unsigned spins = 0; spins < (1u << 22); ++spins) { if (__builtin_amdgcn_readfirstlane(*(volatile LAS unsigned*)(words + 4 * (1 - hf))) != 0u) break; }
    asm volatile("" ::: "memory");
}
template <int hf>
__device__ __forceinline__ void conv_half(const Params& p, LAS char* Lw, LAS char* Lx, __amdgpu_buffer_rsrc_t rs, ConvIn& c, int ch, int chn, int round, int lane STAMP_ARG) {
    const int g = lane >> 4, q = (lane & 15) >> 2, pp = lane & 3, n = lane & 15;
    if (hf == 0) {
        if (lane < 30) *(LAS v4u*)(Lw + CV_X + lane * 16) = (v4u){0u, 0u, 0u, 0u};
        if (lane < 8) *(LAS v4u*)(Lw + (lane >> 1) * CV_COPY + (lane & 1) * 16) = (v4u){0u, 0u, 0u, 0u};
        if (lane == 0) { c.vp[0] = (v4u){0u, c.v[0].x, c.v[0].y, c.v[0].z}; c.vq[0] = 0u; }
    }
#pragma unroll
    for (int j = 0; j < 2; ++j) {
        const v4u v = c.v[j], vp = c.vp[j];
        const unsigned a0 = __builtin_amdgcn_alignbit(v.x, vp.x, 16), a1 = __builtin_amdgcn_alignbit(v.y, v.x, 16), a2 = __builtin_amdgcn_alignbit(v.z, v.y, 16), a3 = __builtin_amdgcn_alignbit(v.w, v.z, 16);
        const unsigned am = __builtin_amdgcn_alignbit(vp.x, c.vq[j], 16);
        LAS char* d = Lw + 32 + (2 * hf + j) * 1024 + lane * 16;
        *(LAS v4u*)(d) = v;
        *(LAS v4u*)(d + CV_COPY) = (v4u){a0, a1, a2, a3};
        *(LAS v4u*)(d + 2 * CV_COPY) = vp;
        *(LAS v4u*)(d + 3 * CV_COPY) = (v4u){am, a0, a1, a2};
        *(LAS v4u*)(Lw + CV_X + 480 + (2 * hf + j) * 1024 + lane * 16) = c.x[j];
    }
    const float Dd = c.Dd;
    pair_sync(Lw + CV_SYNC + 16 * round, hf);
    if (chn >= 0) { conv_load_h(p, c, chn, hf, lane); conv_load_x(c, rs, chn, hf, lane); asm volatile("" ::: "memory"); }
    if (round == 0) STAMP(8); else STAMP(12);
    const unsigned la = (unsigned)(size_t)Lw + q * CV_COPY + 32 - 16 * g + 8 * pp + 24 + 256 * hf;
    const unsigned lb = (unsigned)(size_t)Lw + CV_X + 32 * n + 16 * g + 448 - 256 * hf - 192;
    f32x4 acc[8];
#pragma unroll
    for (int t = 0; t < 8; ++t) acc[t] = (f32x4){0.f, 0.f, 0.f, 0.f};
    v2u Alo[2][8], Ahi[2][8];
    v4u B[2][8];
#define CV_RD(set, rs_, k) do { \
        if ((k) % 3 == 0) A_RDTR(Alo[set][(k) / 3], la, 64 * (rs_) + 512 * ((k) / 3) + 8); \
        else if ((k) % 3 == 1) A_RDTR(Ahi[set][(k) / 3], la, 64 * (rs_) + 512 * ((k) / 3)); \
        else A_RD128(B[set][(k) / 3], lb, 192 - 64 * (rs_) + 512 * ((k) / 3)); } while (0)
    auto step = [&](auto SET, auto NEXT) {
        constexpr int set = decltype(SET)::value, nx = decltype(NEXT)::value;
        int k = 0;
#pragma unroll
        for (int l = 0; l < 8; ++l)
#pragma unroll
            for (int i = 0; i + l < 8; ++i) {
                const v4u a = {Alo[set][i].x, Alo[set][i].y, Ahi[set][i].x, Ahi[set][i].y};
                A_MFMA(acc[i + l], a, B[set][l]);
                if (nx >= 0 && k < 24) { CV_RD(1 - set, (nx >= 0 ? nx : 0), k); }
                ++k;
            }
        if (nx >= 0) A_LGKM(0);
    };
#pragma unroll
    for (int k = 0; k < 24; ++k) CV_RD(0, 0, k);
    A_LGKM(0);
    step(std::integral_constant<int, 0>{}, std::integral_constant<int, 1>{});
    step(std::integral_constant<int, 1>{}, std::integral_constant<int, 2>{});
    step(std::integral_constant<int, 0>{}, std::integral_constant<int, 3>{});
    step(std::integral_constant<int, 1>{}, std::integral_constant<int, -1>{});
#undef CV_RD
    A_MFMA_DRAIN();
    if (round == 0) STAMP(9); else STAMP(13);
#pragma unroll
    for (int t = 0; t < 4; ++t) *(LAS f32x4*)(Lx + hf * 4096 + t * 1024 + lane * 16) = acc[4 * (1 - hf) + t];
    v2u xv[4];
#pragma unroll
    for (int t = 0; t < 4; ++t) xv[t] = *(LAS v2u*)(Lw + CV_X + 480 + 2 * (1024 * hf + 256 * t + 16 * n + 4 * g));
    pair_sync(Lw + CV_SYNC + 16 * round + 8, hf);
    f32x4 o[4];
#pragma unroll
    for (int t = 0; t < 4; ++t) o[t] = *(LAS f32x4*)(Lx + (1 - hf) * 4096 + t * 1024 + lane * 16);
    v2u ov[4];
#pragma unroll
    for (int t = 0; t < 4; ++t) {
        const f32x4 y = acc[4 * hf + t] + o[t];
        ov[t] = (v2u){pk2(bf_2(xv[t].x) * Dd + (f32x2){y[0], y[1]}), pk2(bf_2(xv[t].y) * Dd + (f32x2){y[2], y[3]})};
    }
    LAS char* Ly = Lx + (1 - hf) * 4096;
#pragma unroll
    for (int t = 0; t < 4; ++t) *(LAS v2u*)(Ly + 2 * (256 * t + 16 * n + 4 * g)) = ov[t];
    WAIT_LGKM0();
#pragma unroll
    for (int j = 0; j < 2; ++j) {
        const v4u val = *(LAS v4u*)(Ly + j * 1024 + lane * 16);
        st16_sc1(val, rs, WS_Y + ch * (S * 2) + hf * 2048 + j * 1024 + lane * 16);
    }
    if (round == 0) STAMP(10); else STAMP(14);
}

__device__ __forceinline__ void conv_rounds(const Params& p, LAS char* L, __amdgpu_buffer_rsrc_t rs, ConvIn& c, int ch, int chn, int pr, int hf, int lane STAMP_ARG) {
    LAS char* Lw = L + pr * CV_PAIR; LAS char* Lx = L + CV_EX + pr * 8192;
#pragma unroll 1
    for (int round = 0; round < 2; ++round) {
        if (hf) conv_half<1>(p, Lw, Lx, rs, c, ch, chn, round, lane STAMP_PASS); else conv_half<0>(p, Lw, Lx, rs, c, ch, chn, round, lane STAMP_PASS);
        if (chn < 0) break;
        ch = chn; chn = -1;
    }
}

__device__ __forceinline__ void g2_loader(const Params& p, LAS char* L, __amdgpu_buffer_rsrc_t rs, unsigned tag, const W2Src& w2s, int panel, int ct, int lw, int lane STAMP_ARG) {
    gu32* ctl = (gu32*)(p.ws + WS_CTL);
    const int grow = panel * 128 + (ct & 7) * 16 + 4 * lw;
    if (lw == 0) poll_flags(ctl, W_H + panel * 12, 12, tag, lane);
    w2_issue(w2s, 24 + lw);
    GateIn gi;
    gate_load(p, gi, grow, lane);
    asm volatile("" ::: "memory");
    w2_repack(w2s, L, 0 + lw, lane); w2_repack(w2s, L, 4 + lw, lane);
    STAMP(5);
    BAR();
    w2_repack(w2s, L, 8 + lw, lane); w2_repack(w2s, L, 12 + lw, lane);
    if (ct < 8) gate_finish(gi, rs, grow, lane);
    else WAIT_VM(0);
    BAR();
    w2_issue(w2s, 40 + lw); w2_issue(w2s, 44 + lw);
    w2_repack(w2s, L, 16 + lw, lane); w2_repack(w2s, L, 20 + lw, lane);
    WAIT_VM(8);
    BAR();
    if (ct < 8 && lw == 0 && lane == 0) set_flag(ctl, W_G + panel * 8 + ct, tag);
#pragma unroll
    for (int i = 0; i < 4; ++i) {
        const int r = lane >> 3, c = lane & 7, row = 32 * lw + 8 * i + r;
        glds16((unsigned)(row * (D * 2) + ((c ^ ((row >> 1) & 7)) * 16)), (const char*)p.q + (size_t)panel * 128 * D * 2 + ct * 128, G2_Q + (32 * lw + 8 * i) * 128);
    }
#pragma unroll 1
    for (int b = 24; b < 40; b += 4) w2_repack(w2s, L, b + lw, lane);
    BAR();
    WAIT_VM(4);
    w2_repack(w2s, L, 40 + lw, lane); w2_repack(w2s, L, 44 + lw, lane);
    BAR();
    const int y0 = ct < 7 ? (64 * ct) / 7 : 64 + (64 * ct - 448) / 6, y1 = ct < 7 ? (64 * ct + 63) / 7 : 64 + (64 * ct + 63 - 448) / 6;
    poll_flags(ctl, W_Y + y0, y1 - y0 + 1, tag, lane);
    poll_flags(ctl, W_G + panel * 8, 8, tag, lane);
    STAMP(6);
    v4u yv[4];
#pragma unroll
    for (int i = 0; i < 4; ++i) {
        const int r = lane >> 4, c = lane & 15, d = 16 * lw + 4 * i + r;
        yv[i] = __builtin_amdgcn_raw_buffer_load_b128(rs, d * (S * 2) + c * 16, WS_Y + ct * 64 * S * 2 + panel * 256, AUX_SC1);
    }
    const unsigned gv = __builtin_amdgcn_raw_buffer_load_b32(rs, (lw * 32 + (lane & 31)) * 4, WS_G + panel * 512, AUX_SC1);
#pragma unroll
    for (int i = 0; i < 4; ++i) {
        const int r = lane >> 4, c = lane & 15, d = 16 * lw + 4 * i + r;
        *(LAS v4u*)(L + G2_Y + d * 256 + ((c ^ ((d & 7) << 1)) * 16)) = yv[i];
    }
    if (lane < 32) *(LAS unsigned*)(L + G2_G + (lw * 32 + lane) * 4) = gv;
    WAIT_VM(0);
    WAIT_LGKM0();
    BAR();
}
__device__ __forceinline__ void g2_compute(const Params& p, LAS char* L, __amdgpu_buffer_rsrc_t rs, int panel, int ct, int off2, int w, int lane STAMP_ARG) {
    const int g = lane >> 4, q = (lane & 15) >> 2, pp = lane & 3, s = lane & 15;
    const unsigned hbase = WS_H + (panel * 8 + 2 * w) * (48 * 1024);
    int kld = off2;
    f32x4 acc[4][2];
#pragma unroll
    for (int a = 0; a < 4; ++a) { acc[a][0] = (f32x4){0.f, 0.f, 0.f, 0.f}; acc[a][1] = (f32x4){0.f, 0.f, 0.f, 0.f}; }
    v4u H[G2_HD][2][2];
    auto ldh = [&](int slot) {
#pragma unroll
        for (int ks = 0; ks < 2; ++ks)
#pragma unroll
            for (int sx = 0; sx < 2; ++sx) H[slot][ks][sx] = __builtin_amdgcn_raw_buffer_load_b128(rs, lane * 16 + sx * (48 * 1024) + ks * 1024, hbase + kld * 2048, H_AUX);
        kld = kld + 1 == G2_NST ? 0 : kld + 1;
    };
    BAR();
    STAMP(5);
#pragma unroll
    for (int st = 0; st < G2_HD - 1; ++st) ldh(st);
#pragma unroll
    for (int a = 0; a < 4; ++a) H[G2_HD - 1][a >> 1][a & 1] = (v4u){0u, 0u, 0u, 0u};
    SCHED();
    v4u A[2][4];
#pragma unroll
    for (int a = 0; a < 4; ++a) A[1][a] = (v4u){0u, 0u, 0u, 0u};
    auto half = [&](int set, int slot, int ks, unsigned addr, int off) {
        A_LGKM(3);
        A_MFMA(acc[0][0], A[set][0], H[slot][ks][0]); A_RD128(A[1 - set][0], addr, off);
        A_MFMA(acc[0][1], A[set][0], H[slot][ks][1]); A_RD128(A[1 - set][1], addr, off + 1024);
        A_LGKM(4);
        A_MFMA(acc[1][0], A[set][1], H[slot][ks][0]); A_RD128(A[1 - set][2], addr, off + 2048);
        A_MFMA(acc[1][1], A[set][1], H[slot][ks][1]); A_RD128(A[1 - set][3], addr, off + 3072);
        A_LGKM(5);
        A_MFMA(acc[2][0], A[set][2], H[slot][ks][0]);
        A_MFMA(acc[2][1], A[set][2], H[slot][ks][1]);
        A_LGKM(4);
        A_MFMA(acc[3][0], A[set][3], H[slot][ks][0]);
        A_MFMA(acc[3][1], A[set][3], H[slot][ks][1]);
    };
    const unsigned lbase = (unsigned)(size_t)L + lane * 16;
    unsigned sb = 0;
    auto stage = [&](int u, bool load, bool drain) {
        const unsigned addr = lbase + sb;
        half(1, (u + G2_HD - 1) % G2_HD, 1, addr, 0);
        if (load) { ldh((u + G2_HD - 1) % G2_HD); asm volatile("" ::: "memory"); }
        half(0, u, 0, addr, 4096);
        if (drain) WAIT_LGKM0();
        sb = sb + 8192 == G2_RING ? 0 : sb + 8192;
    };
#pragma unroll 1
    for (int jj = 0; jj < 2; ++jj) {
#pragma unroll
        for (int u = 0; u < 8; ++u) {
            if (u == 4 || (u == 0 && jj == 1)) BAR();
            if (u == 0) { if (jj == 0) STAMP(14); else STAMP(12); }
            stage(u, true, (u & 3) == 3);
        }
    }
    BAR();
    STAMP(13);
#pragma unroll
    for (int u = 0; u < 8; ++u) stage(u, u == 0, false);
    WAIT_LGKM0();
#pragma unroll
    for (int dt = 0; dt < 4; ++dt) { A_MFMA(acc[dt][0], A[1][dt], H[G2_HD - 1][1][0]); A_MFMA(acc[dt][1], A[1][dt], H[G2_HD - 1][1][1]); }
    A_MFMA_DRAIN();
    STAMP(6);
    BAR();
    STAMP(7);
    {
        float gg[2]; v2u yv[2][4], qv[2][4];
#pragma unroll
        for (int sx = 0; sx < 2; ++sx) {
            const int row = 32 * w + 16 * sx + s;
            gg[sx] = *(LAS float*)(L + G2_G + row * 4);
#pragma unroll
            for (int dt = 0; dt < 4; ++dt) {
                const int d = 16 * dt + 4 * g + q;
                yv[sx][dt] = tr_read(L + G2_Y + d * 256 + (((2 * w + sx) ^ (d & 7)) * 32) + 8 * pp);
                qv[sx][dt] = *(LAS v2u*)(L + G2_Q + row * 128 + (((2 * dt + (g >> 1)) ^ ((row >> 1) & 7)) * 16) + 8 * (g & 1));
            }
        }
        SCHED();
#pragma unroll
        for (int sx = 0; sx < 2; ++sx) {
            const int row = 32 * w + 16 * sx + s;
            const float omg = 1.f - gg[sx];
#pragma unroll
            for (int dt = 0; dt < 4; ++dt) {
                const f32x4 a = acc[dt][sx];
                const f32x2 o01 = bf_2(qv[sx][dt].x) * bf_2(yv[sx][dt].x) * gg[sx] + (f32x2){a[0], a[1]} * omg;
                const f32x2 o23 = bf_2(qv[sx][dt].y) * bf_2(yv[sx][dt].y) * gg[sx] + (f32x2){a[2], a[3]} * omg;
                *(LAS v2u*)(L + G2_Q + row * 128 + (((2 * dt + (g >> 1)) ^ ((row >> 1) & 7)) * 16) + 8 * (g & 1)) = (v2u){pk2(o01), pk2(o23)};
            }
        }
    }
    WAIT_LGKM0();
#pragma unroll
    for (int i = 0; i < 4; ++i) {
        const int row = 32 * w + 8 * i + (lane >> 3), c = lane & 7;
        const v4u val = *(LAS v4u*)(L + G2_Q + row * 128 + c * 16);
        *(GAS v4u*)((char*)p.out + (size_t)(panel * 128 + row) * (D * 2) + ct * 128 + ((c ^ ((row >> 1) & 7)) * 16)) = val;
    }
    STAMP(11);
}

__global__ void __launch_bounds__(NTHR, 2) k_fused(Params p) {
    extern __shared__ __attribute__((aligned(16))) char smem[];
    LAS char* L = (LAS char*)smem;
    const int tid = threadIdx.x, lane = tid & 63, w = __builtin_amdgcn_readfirstlane(tid >> 6);
    const int x = blockIdx.x & 7, idx = blockIdx.x >> 3, xr = x >> 1, xc = x & 1;
    STAMP(0);
    gu32* ctl = (gu32*)(p.ws + WS_CTL);
    unsigned epoch_v = __hip_atomic_load(ctl + W_EPOCH * FL, RLX_AGENT);
#define GET_TAG() asm volatile("" : "+v"(epoch_v)); const unsigned tag = __builtin_amdgcn_readfirstlane(epoch_v) + 1u
    const __amdgpu_buffer_rsrc_t rs = __builtin_amdgcn_make_buffer_rsrc(p.ws, 0, WS_END, 0x00020000);
    if (idx < 24) {
        const int panel = 4 * xr + idx / 6, ct = 6 * xc + idx % 6;
        const int off1 = 5 * ((idx / 6 + idx % 6) & 1);
        const bool typeA = idx < 20;
        const int panel2 = 4 * xr + idx / 5, ct2 = 5 * xc + idx % 5;
        const int off2 = 12 * ((idx / 5 + idx % 5) & 1);
        if (typeA) {
            if (w < 4) {
                ConvIn cin;
                g1_compute<true>(p, L, rs, panel, ct, w, lane, cin, 0 STAMP_PASS);
                STAMP(3);
                WAIT_VM(0);
                GET_TAG();
                STAMP(4);
                BAR();
                if (tid == 0) set_flag(ctl, W_H + panel * 12 + ct, tag);
                g2_compute(p, L, rs, panel2, ct2, off2, w, lane STAMP_PASS);
            } else {
                const int lw = w - 4;
                W2Src w2s;
                w2_init(w2s, p, ct2, off2, lane);
                g1_loader<true>(p, panel, ct, off1, w2s, lw, lane STAMP_PASS);
                BAR();
                g1_loader_epilogue(L, rs, panel, ct, lw, lane);
                w2_issue(w2s, 32 + lw); w2_issue(w2s, 36 + lw);
                WAIT_VM(8);
                GET_TAG();
                BAR();
                g2_loader(p, L, rs, tag, w2s, panel2, ct2, lw, lane STAMP_PASS);
            }
        } else {
            const int bw = x * 4 + idx - 20, pr = w >> 1, hf = w & 1;
            const int ch = 448 + 6 * bw + pr, chn = pr < 2 ? ch + 4 : -1;
            ConvIn cin;
            if (w < 4) {
                g1_compute<false>(p, L, rs, panel, ct, w, lane, cin, ch STAMP_PASS);
                STAMP(3);
            } else {
                W2Src w2s;
                g1_loader<false>(p, panel, ct, off1, w2s, w - 4, lane STAMP_PASS);
                conv_load_h(p, cin, ch, hf, lane);
                { GET_TAG(); if (w == 7) poll_flags(ctl, W_KV, 64, tag, lane); }
                BAR();
                conv_load_x(cin, rs, ch, hf, lane);
                g1_loader_epilogue(L, rs, panel, ct, w - 4, lane);
            }
            WAIT_VM(0);
            GET_TAG();
            STAMP(4);
            BAR();
            if (tid == 0) set_flag(ctl, W_H + panel * 12 + ct, tag);
            conv_rounds(p, L, rs, cin, ch, chn, pr, hf, lane STAMP_PASS);
            WAIT_VM(0);
            BAR();
            if (tid == 0) set_flag(ctl, W_Y + 64 + bw, tag);
            STAMP(16);
        }
    } else {
        const int cw = x * 8 + (idx - 24), pr = w >> 1, hf = w & 1;
        const int ch = 7 * cw + pr, chn = pr < 3 ? ch + 4 : -1;
        ConvIn cin;
        conv_load_h(p, cin, ch, hf, lane);
        prepass(p, L, rs, cw, tid STAMP_PASS);
        STAMP(2);
        WAIT_VM(0);
        GET_TAG();
        BAR();
        if (tid == 0) set_flag(ctl, W_KV + cw, tag);
        if (hf == 0 && lane < 8) *(LAS unsigned*)(L + pr * CV_PAIR + CV_SYNC + 4 * lane) = 0u;
        STAMP(3);
        if (w == 1) poll_flags(ctl, W_KV, 64, tag, lane);
        WAIT_LGKM0();
        BAR();
        STAMP(4);
        conv_load_x(cin, rs, ch, hf, lane);
        conv_rounds(p, L, rs, cin, ch, chn, pr, hf, lane STAMP_PASS);
        WAIT_VM(0);
        BAR();
        if (tid == 0) set_flag(ctl, W_Y + cw, tag);
        STAMP(16);
        if (w == 7 && blockIdx.x == 255) {
#pragma unroll 1
            for (int pn = 0; pn < 3; ++pn) poll_flags(ctl, W_H + pn * 64, 64, tag, lane);
            if (lane == 0) set_flag(ctl, W_EPOCH, tag);
        }
    }
    STAMP_FLUSH();
}

extern "C" void kernel_launch(void* const* d_in, const int* in_sizes, int n_in, void* d_out, int out_size, void* d_ws, size_t ws_size, hipStream_t stream) {
    Params p;
    p.q = (const u16*)d_in[0]; p.k = (const u16*)d_in[1]; p.v = (const u16*)d_in[2]; p.shk = (const u16*)d_in[3];
    p.ssm = (const u16*)d_in[4]; p.Dv = (const u16*)d_in[5]; p.w1 = (const u16*)d_in[7]; p.w2 = (const u16*)d_in[8]; p.gw = (const u16*)d_in[9];
    p.out = (u16*)d_out; p.ws = (char*)d_ws;
    (void)hipFuncSetAttribute((const void*)k_fused, hipFuncAttributeMaxDynamicSharedMemorySize, LDS_BYTES);
    k_fused<<<256, NTHR, LDS_BYTES, stream>>>(p);
}
```

```cpp
#define X_TLB 65536
#include <hip/hip_runtime.h>
#include <stdint.h>
#include <type_traits>

typedef unsigned short u16;
typedef unsigned v4u __attribute__((ext_vector_type(4)));
typedef unsigned v2u __attribute__((ext_vector_type(2)));
typedef float f32x4 __attribute__((ext_vector_type(4)));
typedef float f32x2 __attribute__((ext_vector_type(2)));
typedef short bf16x8 __attribute__((ext_vector_type(8)));
typedef short s16x4 __attribute__((ext_vector_type(4)));
typedef __bf16 bf16x2_t __attribute__((ext_vector_type(2)));
#define LAS __attribute__((address_space(3)))
#define GAS __attribute__((address_space(1)))
typedef GAS unsigned gu32;
typedef GAS unsigned long long gu64;
#define RLX_AGENT __ATOMIC_RELAXED, __HIP_MEMORY_SCOPE_AGENT

constexpr int S = 2048, D = 640, F = 1536;
constexpr int NTHR = 512;
constexpr unsigned WS_H = 0;
constexpr unsigned WS_KV = WS_H + S * F * 2;
constexpr unsigned WS_Y = WS_KV + S * D * 2;
constexpr unsigned WS_G = WS_Y + S * D * 2;
constexpr unsigned WS_CTL = WS_G + S * 4;
constexpr int FL = 32;
constexpr int W_EPOCH = 0, W_H = 1, W_KV = W_H + 192, W_G = W_KV + 64, W_Y = W_G + 128, W_LINES = W_Y + 96;
constexpr unsigned WS_END = WS_CTL + W_LINES * 128;
constexpr int AUX_SC1 = 16;
constexpr int H_AUX = 16;
constexpr unsigned kSpinCap = 1u << 18;

struct Params {
    const u16 *q, *k, *v, *shk, *ssm, *Dv, *w1, *w2, *gw;
    u16* out;
    char* ws;
};

__device__ __forceinline__ float bf_lo(unsigned u) { return __uint_as_float(u << 16); }
__device__ __forceinline__ float bf_hi(unsigned u) { return __uint_as_float(u & 0xffff0000u); }
__device__ __forceinline__ f32x2 bf_2(unsigned u) { return (f32x2){bf_lo(u), bf_hi(u)}; }
__device__ __forceinline__ unsigned pk2(f32x2 v) { return __builtin_bit_cast(unsigned, __builtin_convertvector(v, bf16x2_t)); }
__device__ __forceinline__ unsigned pk_bf16(float lo, float hi) { return pk2((f32x2){lo, hi}); }
__device__ __forceinline__ float rbf(float x) { return bf_lo(pk_bf16(x, x)); }
__device__ __forceinline__ f32x4 mfma16(v4u a, v4u b, f32x4 c) { return __builtin_amdgcn_mfma_f32_16x16x32_bf16(__builtin_bit_cast(bf16x8, a), __builtin_bit_cast(bf16x8, b), c, 0, 0, 0); }
__device__ __forceinline__ v2u tr_read(LAS char* p) { return __builtin_bit_cast(v2u, __builtin_amdgcn_ds_read_tr16_b64_v4i16((LAS s16x4*)p)); }
__device__ __forceinline__ v4u lds_rd16(LAS char* p) { return *(LAS v4u*)p; }
__device__ __forceinline__ void glds16(unsigned voff, const void* sbase, unsigned lds_dst) {
    asm volatile("s_mov_b32 m0, %2\n\ts_nop 0\n\tglobal_load_lds_dwordx4 %0, %1" :: "v"(voff), "s"(sbase), "s"(lds_dst) : "memory");
}
template <bool NT> __device__ __forceinline__ void glds16t(unsigned voff, const void* sbase, unsigned lds_dst) {
    if (NT) asm volatile("s_mov_b32 m0, %2\n\ts_nop 0\n\tglobal_load_lds_dwordx4 %0, %1 nt" :: "v"(voff), "s"(sbase), "s"(lds_dst) : "memory");
    else glds16(voff, sbase, lds_dst);
}
#define NT_X 0
#define NT_W1 0
#define NT_W2 0
#define NT_KV 0
#define NT_SSM 0
template <bool NT> __device__ __forceinline__ v4u gld16(const void* ptr) {
    if (NT) return __builtin_nontemporal_load((const GAS v4u*)ptr);
    return *(const GAS v4u*)ptr;
}
#define WAIT_VM(n) asm volatile("s_waitcnt vmcnt(%0)" :: "n"(n) : "memory")
#define WAIT_LGKM0() asm volatile("s_waitcnt lgkmcnt(0)" ::: "memory")
#define BAR() do { asm volatile("" ::: "memory"); __builtin_amdgcn_s_barrier(); asm volatile("" ::: "memory"); } while (0)
#define SCHED() __builtin_amdgcn_sched_barrier(0)
#define A_MFMA(acc, a, b) asm volatile("v_mfma_f32_16x16x32_bf16 %0, %1, %2, %0" : "+v"(acc) : "v"(a), "v"(b))
#define A_RD128(dst, addr, off) asm volatile("ds_read_b128 %0, %1 offset:%2" : "=v"(dst) : "v"(addr), "n"(off))
#define A_RDTR(dst, addr, off) asm volatile("ds_read_b64_tr_b16 %0, %1 offset:%2" : "=v"(dst) : "v"(addr), "n"(off))
#define A_LGKM(n) asm volatile("s_waitcnt lgkmcnt(%0)" :: "n"(n))
#define A_MFMA_DRAIN() asm volatile("s_nop 15\n\ts_nop 7" ::: "memory")
template <int NM, int NR, int PER> __device__ __forceinline__ void interleave() {
#pragma unroll
    for (int i = 0; i < NM; ++i) {
        __builtin_amdgcn_sched_group_barrier(0x8, 1, 0);
        if (i * PER < NR) __builtin_amdgcn_sched_group_barrier(0x100, PER, 0);
    }
}
#define STAMP(id) do {} while (0)
#define STAMP_ARG
#define STAMP_PASS
#define STAMP_FLUSH() do {} while (0)

__device__ __forceinline__ void st16_sc1(v4u v, __amdgpu_buffer_rsrc_t rs, unsigned off) { __builtin_amdgcn_raw_buffer_store_b128(v, rs, off, 0, AUX_SC1); }
__device__ __forceinline__ void poll_flags(gu32* ctl, int line, int n, unsigned tag, int lane) {
    gu32* f = ctl + (line + (lane < n ? lane : 0)) * FL;
#pragma unroll 1
    for (unsigned spins = 0; spins < kSpinCap; ++spins) {
        const unsigned v = __hip_atomic_load(f, RLX_AGENT);
        if (__all(v == tag)) break;
        __builtin_amdgcn_s_sleep(4);
    }
    __builtin_amdgcn_fence(__ATOMIC_ACQUIRE, "wavefront");
}
__device__ __forceinline__ void set_flag(gu32* ctl, int line, unsigned tag) { __hip_atomic_store(ctl + line * FL, tag, RLX_AGENT); }
__device__ __forceinline__ unsigned silu2f(f32x2 h) {
    const f32x2 t = h * -1.44269504f;
    const f32x2 den = (f32x2){__builtin_amdgcn_exp2f(t.x), __builtin_amdgcn_exp2f(t.y)} + 1.f;
    const f32x2 r = (f32x2){__builtin_amdgcn_rcpf(den.x), __builtin_amdgcn_rcpf(den.y)};
    return pk2(h * r);
}
__device__ __forceinline__ unsigned silu2(unsigned hb) { return silu2f(bf_2(hb)); }
__device__ __forceinline__ float row_sum16(float v) {
    v += __builtin_bit_cast(float, __builtin_amdgcn_update_dpp(0, __builtin_bit_cast(int, v), 0x128, 0xf, 0xf, false));
    v += __builtin_bit_cast(float, __builtin_amdgcn_update_dpp(0, __builtin_bit_cast(int, v), 0x124, 0xf, 0xf, false));
    v += __builtin_bit_cast(float, __builtin_amdgcn_update_dpp(0, __builtin_bit_cast(int, v), 0x122, 0xf, 0xf, false));
    v += __builtin_bit_cast(float, __builtin_amdgcn_update_dpp(0, __builtin_bit_cast(int, v), 0x121, 0xf, 0xf, false));
    return v;
}

constexpr int G1_SLOT = 32768, G1_NS = 5, G1_NST = D / 64, G1_STG = 3 * G1_SLOT;
constexpr int G2_NST = F / 64, G2_RING = 40 * 4096, G2_HD = 8;
constexpr int G2_Q = 8 * 4096, G2_Y = 12 * 4096, G2_G = 16 * 4096;
constexpr int CV_COPY = 4160, CV_X = 4 * CV_COPY, CV_PAIR = CV_X + 4608;
constexpr int CV_EX = 4 * CV_PAIR;
constexpr int LDS_BYTES = 163840;
static_assert(CV_EX + 4 * 8192 <= LDS_BYTES, "LDS map");

struct W2Src { unsigned vw[4]; const char* base; int kb0; unsigned aw[4]; };
__device__ __forceinline__ void w2_init(W2Src& s, const Params& p, int ct, int off2, int lane) {
#pragma unroll
    for (int i = 0; i < 4; ++i) {
        const int r = lane >> 3, c = lane & 7, k = 8 * i + r;
        s.vw[i] = (unsigned)(k * (D * 2) + ((c ^ (((k >> 1) & 3) << 1)) * 16));
    }
    const int g = lane >> 4, q = (lane & 15) >> 2, pp = lane & 3, sw = ((g & 1) << 1) | (q >> 1);
#pragma unroll
    for (int dt = 0; dt < 4; ++dt) s.aw[dt] = (4 * g + q) * 128 + ((dt ^ sw) * 32) + 8 * pp;
    s.base = (const char*)p.w2 + ct * 128; s.kb0 = 2 * off2;
}
__device__ __forceinline__ void w2_issue(const W2Src& s, int b) {
    const int kb = s.kb0 + b >= 48 ? s.kb0 + b - 48 : s.kb0 + b;
    const char* src = s.base + (size_t)kb * (32 * D * 2);
    const unsigned slot = (unsigned)(b >= 40 ? b - 40 : b) * 4096;
#pragma unroll
    for (int i = 0; i < 4; ++i) glds16t<NT_W2>(s.vw[i], src, slot + i * 1024);
}
__device__ __forceinline__ void w2_repack(const W2Src& s, LAS char* L, int b, int lane) {
    LAS char* blk = L + (b >= 40 ? b - 40 : b) * 4096;
    v4u f[4];
#pragma unroll
    for (int dt = 0; dt < 4; ++dt) { const v2u lo = tr_read(blk + s.aw[dt]), hi = tr_read(blk + s.aw[dt] + 2048); f[dt] = (v4u){lo.x, lo.y, hi.x, hi.y}; }
    WAIT_LGKM0();
#pragma unroll
    for (int dt = 0; dt < 4; ++dt) *(LAS v4u*)(blk + dt * 1024 + lane * 16) = f[dt];
    WAIT_LGKM0();
}

template <bool TYPE_A>
__device__ __forceinline__ void g1_loader(const Params& p, int panel, int ct, int off1, const W2Src& w2s, int lw, int lane STAMP_ARG) {
    unsigned vx[4], vw[4];
#pragma unroll
    for (int i = 0; i < 4; ++i) {
        const int r = lane >> 3, c = lane & 7, row = 32 * lw + 8 * i + r;
        vx[i] = (unsigned)(row * (D * 2) + ((c ^ ((row >> 1) & 7)) * 16));
        const int r2 = lane >> 4, c2 = lane & 15, k = 16 * lw + 4 * i + r2, phi = (k & 3) | (((k >> 3) & 1) << 2);
        vw[i] = (unsigned)(k * (F * 2) + ((c2 ^ (phi << 1)) * 16));
    }
    const char* xb = (const char*)p.q + (size_t)panel * 128 * D * 2;
    const char* wb = (const char*)p.w1 + (size_t)ct * 256;
    int kst = off1;
    auto issue = [&](int slot_i) {
        const unsigned slot = (unsigned)slot_i * G1_SLOT;
        const char* xs = xb + kst * 128; const char* wsrc = wb + (size_t)kst * (64 * F * 2);
#pragma unroll
        for (int i = 0; i < 4; ++i) glds16t<NT_X>(vx[i], xs, slot + (32 * lw + 8 * i) * 128);
#pragma unroll
        for (int i = 0; i < 4; ++i) glds16t<NT_W1>(vw[i], wsrc, slot + 16384 + (16 * lw + 4 * i) * 256);
        kst = kst + 1 == G1_NST ? 0 : kst + 1;
    };
#pragma unroll
    for (int st = 0; st < 3; ++st) issue(st);
    WAIT_VM(16); BAR(); issue(3); issue(4);
#pragma unroll 1
    for (int i = 1; i < 6; ++i) { WAIT_VM(24); BAR(); issue(i - 1); }
    STAMP(1);
    if (TYPE_A) {
        WAIT_VM(24); BAR(); w2_issue(w2s, 0 + lw); w2_issue(w2s, 4 + lw);
        WAIT_VM(24); BAR(); w2_issue(w2s, 8 + lw); w2_issue(w2s, 12 + lw);
        WAIT_VM(24); BAR(); w2_issue(w2s, 16 + lw); w2_issue(w2s, 20 + lw);
        WAIT_VM(24); BAR(); w2_issue(w2s, 28 + lw);
    } else {
        WAIT_VM(24); BAR(); WAIT_VM(16); BAR(); WAIT_VM(8); BAR(); WAIT_VM(0); BAR();
    }
}
__device__ __forceinline__ void g1_store(__amdgpu_buffer_rsrc_t rs, int stg, int ksg, int lane, v4u hb) {
    const v4u o = {silu2(hb.x), silu2(hb.y), silu2(hb.z), silu2(hb.w)};
    st16_sc1(o, rs, WS_H + (stg * 48 + ksg) * 1024 + lane * 16);
}
struct ConvIn { v4u v[2], vp[2], x[2]; unsigned vq[2]; float Dd; };
__device__ __forceinline__ void conv_load_h(const Params& p, ConvIn& c, int ch, int hf, int lane) {
    const char* hsrc = (const char*)p.ssm + (size_t)ch * (S * 2);
#pragma unroll
    for (int j = 0; j < 2; ++j) {
        const int o = (2 * hf + j) * 1024 + lane * 16;
        c.v[j] = gld16<NT_SSM>(hsrc + o);
        c.vp[j] = gld16<NT_SSM>(hsrc + (o < 4 ? 0 : o - 4));
        c.vq[j] = *(const GAS unsigned*)(hsrc + (o < 8 ? 0 : o - 8));
    }
    c.Dd = bf_lo((unsigned)p.Dv[ch]);
}
__device__ __forceinline__ void conv_load_x(ConvIn& c, __amdgpu_buffer_rsrc_t rs, int ch, int hf, int lane) {
#pragma unroll
    for (int j = 0; j < 2; ++j) c.x[j] = __builtin_amdgcn_raw_buffer_load_b128(rs, (2 * hf + j) * 1024 + lane * 16, WS_KV + ch * (S * 2), AUX_SC1);
}
constexpr int CV_SYNC = 4128;
template <bool TYPE_A>
__device__ __forceinline__ void g1_compute(const Params& p, LAS char* L, __amdgpu_buffer_rsrc_t rs, int panel, int ct, int w, int lane, ConvIn& cin, int ch STAMP_ARG) {
    const int wf = w & 1, wsx = w >> 1;
    const int g = lane >> 4, q = (lane & 15) >> 2, pp = lane & 3, s = lane & 15;
    const int phi = q | ((g & 1) << 2);
    unsigned aw[4], ax[2];
#pragma unroll
    for (int ft = 0; ft < 4; ++ft) aw[ft] = 16384 + (8 * g + q) * 256 + (((4 * wf + ft) ^ phi) * 32) + 8 * pp;
#pragma unroll
    for (int ks = 0; ks < 2; ++ks) ax[ks] = (64 * wsx + s) * 128 + (((4 * ks + g) ^ ((s >> 1) & 7)) * 16);
    f32x4 acc[4][4];
#pragma unroll
    for (int a = 0; a < 4; ++a)
#pragma unroll
        for (int b = 0; b < 4; ++b) acc[a][b] = (f32x4){0.f, 0.f, 0.f, 0.f};
    v4u A[2][4], B[2][4];
#pragma unroll
    for (int a = 0; a < 4; ++a) { A[1][a] = (v4u){0u, 0u, 0u, 0u}; B[1][a] = (v4u){0u, 0u, 0u, 0u}; }
    auto rd = [&](int set, LAS char* base, int ks) {
#pragma unroll
        for (int ft = 0; ft < 4; ++ft) {
            const v2u lo = tr_read(base + aw[ft] + ks * 8192), hi = tr_read(base + aw[ft] + ks * 8192 + 1024);
            A[set][ft] = (v4u){lo.x, lo.y, hi.x, hi.y};
            if (ft == 0) {
#pragma unroll
                for (int sx = 0; sx < 4; ++sx) B[set][sx] = lds_rd16(base + ax[ks] + sx * 2048);
            }
        }
    };
    auto mm = [&](int set) {
#pragma unroll
        for (int ft = 0; ft < 4; ++ft)
#pragma unroll
            for (int sx = 0; sx < 4; ++sx) acc[ft][sx] = mfma16(A[set][ft], B[set][sx], acc[ft][sx]);
    };
    unsigned tl = 0;
    {
        const unsigned st = X_TLB;
        const char* b = w == 0 ? (const char*)p.w2 : (w == 1 ? (const char*)p.out : (const char*)p.ws);
        const unsigned lim = w == 0 ? (unsigned)(F * D * 2) : (w == 1 ? (unsigned)(S * D * 2) : WS_END);
        const unsigned o0 = (w == 3 ? 64u : 0u) * st + lane * st;
#pragma unroll
        for (int r = 0; r < 2; ++r) {
            const unsigned o = o0 + (w >= 2 ? 128u : 64u) * st * r;
            const unsigned oo = o < lim ? o : 0u;
            asm volatile("global_load_dword %0, %1, %2" : "+v"(tl) : "v"(oo), "s"(b));
        }
    }
    unsigned slot = 0;
#define X_G1REP 1
#pragma unroll 1
    for (int i = 0; i < G1_NST * X_G1REP; ++i) {
        LAS char* base = L + slot;
        slot = slot + G1_SLOT == G1_NS * G1_SLOT ? 0 : slot + G1_SLOT;
        BAR();
        if (i == 0) STAMP(1);
        rd(0, base, 0);
        mm(1);
        interleave<16, 12, 1>();
        SCHED();
        rd(1, base, 1);
        mm(0);
        interleave<16, 12, 1>();
        SCHED();
        WAIT_LGKM0();
    }
    mm(1);
    asm volatile("s_waitcnt vmcnt(0)" : "+v"(tl));
    STAMP(2);
    if (!TYPE_A) conv_load_h(p, cin, ch, w & 1, lane);
    SCHED();
#pragma unroll
    for (int j = 0; j < 4; ++j) {
        const f32x4 a = acc[2 * (j & 1)][2 + (j >> 1)], b = acc[2 * (j & 1) + 1][2 + (j >> 1)];
        *(LAS v4u*)(L + G1_STG + w * 4096 + j * 1024 + lane * 16) = (v4u){pk_bf16(a[0], a[1]), pk_bf16(a[2], a[3]), pk_bf16(b[0], b[1]), pk_bf16(b[2], b[3])};
    }
    if (!TYPE_A && lane < 8) *(LAS unsigned*)(L + w * CV_PAIR + CV_SYNC + 4 * lane) = 0u;
    WAIT_LGKM0();
    BAR();
    if (!TYPE_A) conv_load_x(cin, rs, ch, w & 1, lane);
    SCHED();
#pragma unroll
    for (int j = 0; j < 4; ++j) {
        const f32x4 a = acc[2 * (j & 1)][j >> 1], b = acc[2 * (j & 1) + 1][j >> 1];
        const v4u o = {silu2f((f32x2){a[0], a[1]}), silu2f((f32x2){a[2], a[3]}), silu2f((f32x2){b[0], b[1]}), silu2f((f32x2){b[2], b[3]})};
        st16_sc1(o, rs, WS_H + ((panel * 8 + wsx * 4 + (j >> 1)) * 48 + ct * 4 + wf * 2 + (j & 1)) * 1024 + lane * 16);
    }
}
__device__ __forceinline__ void g1_loader_epilogue(LAS char* L, __amdgpu_buffer_rsrc_t rs, int panel, int ct, int lw, int lane) {
    const int wf = lw & 1, wsx = lw >> 1;
    v4u hb[4];
#pragma unroll
    for (int j = 0; j < 4; ++j) hb[j] = *(LAS v4u*)(L + G1_STG + lw * 4096 + j * 1024 + lane * 16);
#pragma unroll
    for (int j = 0; j < 4; ++j) g1_store(rs, panel * 8 + wsx * 4 + 2 + (j >> 1), ct * 4 + wf * 2 + (j & 1), lane, hb[j]);
}

__device__ __forceinline__ void prepass(const Params& p, LAS char* L, __amdgpu_buffer_rsrc_t rs, int wg, int tid STAMP_ARG) {
    const int s0 = 64 * (wg >> 1), c0 = 320 * (wg & 1);
    {
        const int w = __builtin_amdgcn_readfirstlane(tid >> 6);
        const int item = w < 4 ? tid : 256 + (tid & 63);
        const int cc = item % 40, rg = item / 40;
        const int ch = c0 + 8 * cc, r0 = s0 + 8 * rg;
        v4u kk[11], vv[8], sw[4];
#pragma unroll
        for (int j = 0; j < 4; ++j) sw[j] = *(const GAS v4u*)((const char*)p.shk + (size_t)ch * 8 + j * 16);
#pragma unroll
        for (int j = 0; j < 11; ++j) {
            const int row = r0 - 3 + j;
            kk[j] = gld16<NT_KV>((const char*)p.k + (size_t)(row < 0 ? 0 : row) * (D * 2) + ch * 2);
        }
#pragma unroll
        for (int j = 0; j < 8; ++j) vv[j] = gld16<NT_KV>((const char*)p.v + (size_t)(r0 + j) * (D * 2) + ch * 2);
        if (r0 == 0) { kk[0] = (v4u){0u, 0u, 0u, 0u}; kk[1] = kk[0]; kk[2] = kk[0]; }
#pragma unroll
        for (int e = 0; e < 4; ++e) {
            if (w >= 4 && w - 4 != e) continue;
            const f32x2 W0 = {bf_lo(sw[e].x), bf_lo(sw[e].z)}, W1 = {bf_hi(sw[e].x), bf_hi(sw[e].z)};
            const f32x2 W2 = {bf_lo(sw[e].y), bf_lo(sw[e].w)}, W3 = {bf_hi(sw[e].y), bf_hi(sw[e].w)};
            f32x2 kf[11], o[8];
#pragma unroll
            for (int j = 0; j < 11; ++j) kf[j] = bf_2(kk[j][e]);
#pragma unroll
            for (int j = 0; j < 8; ++j) {
                const f32x2 ksh = kf[j] * W3 + kf[j + 1] * W2 + kf[j + 2] * W1 + kf[j + 3] * W0;
                o[j] = ksh * bf_2(vv[j][e]);
            }
            const v4u p0 = {pk_bf16(o[0].x, o[1].x), pk_bf16(o[2].x, o[3].x), pk_bf16(o[4].x, o[5].x), pk_bf16(o[6].x, o[7].x)};
            const v4u p1 = {pk_bf16(o[0].y, o[1].y), pk_bf16(o[2].y, o[3].y), pk_bf16(o[4].y, o[5].y), pk_bf16(o[6].y, o[7].y)};
            *(LAS v4u*)(L + (8 * cc + 2 * e) * 128 + ((rg ^ (cc & 7)) * 16)) = p0;
            *(LAS v4u*)(L + (8 * cc + 2 * e + 1) * 128 + ((rg ^ (cc & 7)) * 16)) = p1;
        }
    }
    STAMP(1);
    WAIT_LGKM0();
    BAR();
#pragma unroll
    for (int j = 0; j < 5; ++j) {
        const int n = tid + NTHR * j, ch = n >> 3, pc = n & 7;
        const v4u val = *(LAS v4u*)(L + ch * 128 + ((pc ^ ((ch >> 3) & 7)) * 16));
        st16_sc1(val, rs, WS_KV + (c0 + ch) * (S * 2) + s0 * 2 + pc * 16);
    }
}
struct GateIn { v4u gw[5], q[5]; };
__device__ __forceinline__ void gate_load(const Params& p, GateIn& gi, int row0, int lane) {
    const int i = lane & 15, row = row0 + (lane >> 4);
#pragma unroll
    for (int j = 0; j < 5; ++j) {
        gi.gw[j] = *(const GAS v4u*)((const char*)p.gw + (i + 16 * j) * 16);
        gi.q[j] = *(const GAS v4u*)((const char*)p.q + (size_t)row * (D * 2) + (i + 16 * j) * 16);
    }
}
__device__ __forceinline__ void gate_finish(const GateIn& gi, __amdgpu_buffer_rsrc_t rs, int row0, int lane) {
    f32x2 a2 = {0.f, 0.f};
#pragma unroll
    for (int j = 0; j < 5; ++j)
#pragma unroll
        for (int e = 0; e < 4; ++e) a2 += bf_2(gi.q[j][e]) * bf_2(gi.gw[j][e]);
    const float z = rbf(row_sum16(a2.x + a2.y));
    const float gv = rbf(__builtin_amdgcn_rcpf(1.f + __builtin_amdgcn_exp2f(z * -1.44269504f)));
    if ((lane & 15) == 0) __builtin_amdgcn_raw_buffer_store_b32(__float_as_uint(gv), rs, WS_G + (row0 + (lane >> 4)) * 4, 0, AUX_SC1);
}

__device__ __forceinline__ void pair_sync(LAS char* words, int hf) {
    WAIT_LGKM0();
    *(volatile LAS unsigned*)(words + 4 * hf) = 1u;
#pragma unroll 1
    for (unsigned spins = 0; spins < (1u << 22); ++spins) { if (__builtin_amdgcn_readfirstlane(*(volatile LAS unsigned*)(words + 4 * (1 - hf))) != 0u) break; }
    asm volatile("" ::: "memory");
}
template <int hf>
__device__ __forceinline__ void conv_half(const Params& p, LAS char* Lw, LAS char* Lx, __amdgpu_buffer_rsrc_t rs, ConvIn& c, int ch, int chn, int round, int lane STAMP_ARG) {
    const int g = lane >> 4, q = (lane & 15) >> 2, pp = lane & 3, n = lane & 15;
    if (hf == 0) {
        if (lane < 30) *(LAS v4u*)(Lw + CV_X + lane * 16) = (v4u){0u, 0u, 0u, 0u};
        if (lane < 8) *(LAS v4u*)(Lw + (lane >> 1) * CV_COPY + (lane & 1) * 16) = (v4u){0u, 0u, 0u, 0u};
        if (lane == 0) { c.vp[0] = (v4u){0u, c.v[0].x, c.v[0].y, c.v[0].z}; c.vq[0] = 0u; }
    }
#pragma unroll
    for (int j = 0; j < 2; ++j) {
        const v4u v = c.v[j], vp = c.vp[j];
        const unsigned a0 = __builtin_amdgcn_alignbit(v.x, vp.x, 16), a1 = __builtin_amdgcn_alignbit(v.y, v.x, 16), a2 = __builtin_amdgcn_alignbit(v.z, v.y, 16), a3 = __builtin_amdgcn_alignbit(v.w, v.z, 16);
        const unsigned am = __builtin_amdgcn_alignbit(vp.x, c.vq[j], 16);
        LAS char* d = Lw + 32 + (2 * hf + j) * 1024 + lane * 16;
        *(LAS v4u*)(d) = v;
        *(LAS v4u*)(d + CV_COPY) = (v4u){a0, a1, a2, a3};
        *(LAS v4u*)(d + 2 * CV_COPY) = vp;
        *(LAS v4u*)(d + 3 * CV_COPY) = (v4u){am, a0, a1, a2};
        *(LAS v4u*)(Lw + CV_X + 480 + (2 * hf + j) * 1024 + lane * 16) = c.x[j];
    }
    const float Dd = c.Dd;
    pair_sync(Lw + CV_SYNC + 16 * round, hf);
    if (chn >= 0) { conv_load_h(p, c, chn, hf, lane); conv_load_x(c, rs, chn, hf, lane); asm volatile("" ::: "memory"); }
    if (round == 0) STAMP(8); else STAMP(12);
    const unsigned la = (unsigned)(size_t)Lw + q * CV_COPY + 32 - 16 * g + 8 * pp + 24 + 256 * hf;
    const unsigned lb = (unsigned)(size_t)Lw + CV_X + 32 * n + 16 * g + 448 - 256 * hf - 192;
    f32x4 acc[8];
#pragma unroll
    for (int t = 0; t < 8; ++t) acc[t] = (f32x4){0.f, 0.f, 0.f, 0.f};
    v2u Alo[2][8], Ahi[2][8];
    v4u B[2][8];
#define CV_RD(set, rs_, k) do { \
        if ((k) % 3 == 0) A_RDTR(Alo[set][(k) / 3], la, 64 * (rs_) + 512 * ((k) / 3) + 8); \
        else if ((k) % 3 == 1) A_RDTR(Ahi[set][(k) / 3], la, 64 * (rs_) + 512 * ((k) / 3)); \
        else A_RD128(B[set][(k) / 3], lb, 192 - 64 * (rs_) + 512 * ((k) / 3)); } while (0)
    auto step = [&](auto SET, auto NEXT) {
        constexpr int set = decltype(SET)::value, nx = decltype(NEXT)::value;
        int k = 0;
#pragma unroll
        for (int l = 0; l < 8; ++l)
#pragma unroll
            for (int i = 0; i + l < 8; ++i) {
                const v4u a = {Alo[set][i].x, Alo[set][i].y, Ahi[set][i].x, Ahi[set][i].y};
                A_MFMA(acc[i + l], a, B[set][l]);
                if (nx >= 0 && k < 24) { CV_RD(1 - set, (nx >= 0 ? nx : 0), k); }
                ++k;
            }
        if (nx >= 0) A_LGKM(0);
    };
#pragma unroll
    for (int k = 0; k < 24; ++k) CV_RD(0, 0, k);
    A_LGKM(0);
    step(std::integral_constant<int, 0>{}, std::integral_constant<int, 1>{});
    step(std::integral_constant<int, 1>{}, std::integral_constant<int, 2>{});
    step(std::integral_constant<int, 0>{}, std::integral_constant<int, 3>{});
    step(std::integral_constant<int, 1>{}, std::integral_constant<int, -1>{});
#undef CV_RD
    A_MFMA_DRAIN();
    if (round == 0) STAMP(9); else STAMP(13);
#pragma unroll
    for (int t = 0; t < 4; ++t) *(LAS f32x4*)(Lx + hf * 4096 + t * 1024 + lane * 16) = acc[4 * (1 - hf) + t];
    v2u xv[4];
#pragma unroll
    for (int t = 0; t < 4; ++t) xv[t] = *(LAS v2u*)(Lw + CV_X + 480 + 2 * (1024 * hf + 256 * t + 16 * n + 4 * g));
    pair_sync(Lw + CV_SYNC + 16 * round + 8, hf);
    f32x4 o[4];
#pragma unroll
    for (int t = 0; t < 4; ++t) o[t] = *(LAS f32x4*)(Lx + (1 - hf) * 4096 + t * 1024 + lane * 16);
    v2u ov[4];
#pragma unroll
    for (int t = 0; t < 4; ++t) {
        const f32x4 y = acc[4 * hf + t] + o[t];
        ov[t] = (v2u){pk2(bf_2(xv[t].x) * Dd + (f32x2){y[0], y[1]}), pk2(bf_2(xv[t].y) * Dd + (f32x2){y[2], y[3]})};
    }
    LAS char* Ly = Lx + (1 - hf) * 4096;
#pragma unroll
    for (int t = 0; t < 4; ++t) *(LAS v2u*)(Ly + 2 * (256 * t + 16 * n + 4 * g)) = ov[t];
    WAIT_LGKM0();
#pragma unroll
    for (int j = 0; j < 2; ++j) {
        const v4u val = *(LAS v4u*)(Ly + j * 1024 + lane * 16);
        st16_sc1(val, rs, WS_Y + ch * (S * 2) + hf * 2048 + j * 1024 + lane * 16);
    }
    if (round == 0) STAMP(10); else STAMP(14);
}

__device__ __forceinline__ void conv_rounds(const Params& p, LAS char* L, __amdgpu_buffer_rsrc_t rs, ConvIn& c, int ch, int chn, int pr, int hf, int lane STAMP_ARG) {
    LAS char* Lw = L + pr * CV_PAIR; LAS char* Lx = L + CV_EX + pr * 8192;
#pragma unroll 1
    for (int round = 0; round < 2; ++round) {
        if (hf) conv_half<1>(p, Lw, Lx, rs, c, ch, chn, round, lane STAMP_PASS); else conv_half<0>(p, Lw, Lx, rs, c, ch, chn, round, lane STAMP_PASS);
        if (chn < 0) break;
        ch = chn; chn = -1;
    }
}

__device__ __forceinline__ void g2_loader(const Params& p, LAS char* L, __amdgpu_buffer_rsrc_t rs, unsigned tag, const W2Src& w2s, int panel, int ct, int lw, int lane STAMP_ARG) {
    gu32* ctl = (gu32*)(p.ws + WS_CTL);
    const int grow = panel * 128 + (ct & 7) * 16 + 4 * lw;
    if (lw == 0) poll_flags(ctl, W_H + panel * 12, 12, tag, lane);
    w2_issue(w2s, 24 + lw);
    GateIn gi;
    gate_load(p, gi, grow, lane);
    asm volatile("" ::: "memory");
    w2_repack(w2s, L, 0 + lw, lane); w2_repack(w2s, L, 4 + lw, lane);
    STAMP(5);
    BAR();
    w2_repack(w2s, L, 8 + lw, lane); w2_repack(w2s, L, 12 + lw, lane);
    if (ct < 8) gate_finish(gi, rs, grow, lane);
    else WAIT_VM(0);
    BAR();
    w2_issue(w2s, 40 + lw); w2_issue(w2s, 44 + lw);
    w2_repack(w2s, L, 16 + lw, lane); w2_repack(w2s, L, 20 + lw, lane);
    WAIT_VM(8);
    BAR();
    if (ct < 8 && lw == 0 && lane == 0) set_flag(ctl, W_G + panel * 8 + ct, tag);
#pragma unroll
    for (int i = 0; i < 4; ++i) {
        const int r = lane >> 3, c = lane & 7, row = 32 * lw + 8 * i + r;
        glds16((unsigned)(row * (D * 2) + ((c ^ ((row >> 1) & 7)) * 16)), (const char*)p.q + (size_t)panel * 128 * D * 2 + ct * 128, G2_Q + (32 * lw + 8 * i) * 128);
    }
#pragma unroll 1
    for (int b = 24; b < 40; b += 4) w2_repack(w2s, L, b + lw, lane);
    BAR();
    WAIT_VM(4);
    w2_repack(w2s, L, 40 + lw, lane); w2_repack(w2s, L, 44 + lw, lane);
    BAR();
    const int y0 = ct < 7 ? (64 * ct) / 7 : 64 + (64 * ct - 448) / 6, y1 = ct < 7 ? (64 * ct + 63) / 7 : 64 + (64 * ct + 63 - 448) / 6;
    poll_flags(ctl, W_Y + y0, y1 - y0 + 1, tag, lane);
    poll_flags(ctl, W_G + panel * 8, 8, tag, lane);
    STAMP(6);
    v4u yv[4];
#pragma unroll
    for (int i = 0; i < 4; ++i) {
        const int r = lane >> 4, c = lane & 15, d = 16 * lw + 4 * i + r;
        yv[i] = __builtin_amdgcn_raw_buffer_load_b128(rs, d * (S * 2) + c * 16, WS_Y + ct * 64 * S * 2 + panel * 256, AUX_SC1);
    }
    const unsigned gv = __builtin_amdgcn_raw_buffer_load_b32(rs, (lw * 32 + (lane & 31)) * 4, WS_G + panel * 512, AUX_SC1);
#pragma unroll
    for (int i = 0; i < 4; ++i) {
        const int r = lane >> 4, c = lane & 15, d = 16 * lw + 4 * i + r;
        *(LAS v4u*)(L + G2_Y + d * 256 + ((c ^ ((d & 7) << 1)) * 16)) = yv[i];
    }
    if (lane < 32) *(LAS unsigned*)(L + G2_G + (lw * 32 + lane) * 4) = gv;
    WAIT_VM(0);
    WAIT_LGKM0();
    BAR();
}
__device__ __forceinline__ void g2_compute(const Params& p, LAS char* L, __amdgpu_buffer_rsrc_t rs, int panel, int ct, int off2, int w, int lane STAMP_ARG) {
    const int g = lane >> 4, q = (lane & 15) >> 2, pp = lane & 3, s = lane & 15;
    const unsigned hbase = WS_H + (panel * 8 + 2 * w) * (48 * 1024);
    int kld = off2;
    f32x4 acc[4][2];
#pragma unroll
    for (int a = 0; a < 4; ++a) { acc[a][0] = (f32x4){0.f, 0.f, 0.f, 0.f}; acc[a][1] = (f32x4){0.f, 0.f, 0.f, 0.f}; }
    v4u H[G2_HD][2][2];
    auto ldh = [&](int slot) {
#pragma unroll
        for (int ks = 0; ks < 2; ++ks)
#pragma unroll
            for (int sx = 0; sx < 2; ++sx) H[slot][ks][sx] = __builtin_amdgcn_raw_buffer_load_b128(rs, lane * 16 + sx * (48 * 1024) + ks * 1024, hbase + kld * 2048, H_AUX);
        kld = kld + 1 == G2_NST ? 0 : kld + 1;
    };
    BAR();
    STAMP(5);
#pragma unroll
    for (int st = 0; st < G2_HD - 1; ++st) ldh(st);
#pragma unroll
    for (int a = 0; a < 4; ++a) H[G2_HD - 1][a >> 1][a & 1] = (v4u){0u, 0u, 0u, 0u};
    SCHED();
    v4u A[2][4];
#pragma unroll
    for (int a = 0; a < 4; ++a) A[1][a] = (v4u){0u, 0u, 0u, 0u};
    auto half = [&](int set, int slot, int ks, unsigned addr, int off) {
        A_LGKM(3);
        A_MFMA(acc[0][0], A[set][0], H[slot][ks][0]); A_RD128(A[1 - set][0], addr, off);
        A_MFMA(acc[0][1], A[set][0], H[slot][ks][1]); A_RD128(A[1 - set][1], addr, off + 1024);
        A_LGKM(4);
        A_MFMA(acc[1][0], A[set][1], H[slot][ks][0]); A_RD128(A[1 - set][2], addr, off + 2048);
        A_MFMA(acc[1][1], A[set][1], H[slot][ks][1]); A_RD128(A[1 - set][3], addr, off + 3072);
        A_LGKM(5);
        A_MFMA(acc[2][0], A[set][2], H[slot][ks][0]);
        A_MFMA(acc[2][1], A[set][2], H[slot][ks][1]);
        A_LGKM(4);
        A_MFMA(acc[3][0], A[set][3], H[slot][ks][0]);
        A_MFMA(acc[3][1], A[set][3], H[slot][ks][1]);
    };
    const unsigned lbase = (unsigned)(size_t)L + lane * 16;
    unsigned sb = 0;
    auto stage = [&](int u, bool load, bool drain) {
        const unsigned addr = lbase + sb;
        half(1, (u + G2_HD - 1) % G2_HD, 1, addr, 0);
        if (load) { ldh((u + G2_HD - 1) % G2_HD); asm volatile("" ::: "memory"); }
        half(0, u, 0, addr, 4096);
        if (drain) WAIT_LGKM0();
        sb = sb + 8192 == G2_RING ? 0 : sb + 8192;
    };
#pragma unroll 1
    for (int jj = 0; jj < 2; ++jj) {
#pragma unroll
        for (int u = 0; u < 8; ++u) {
            if (u == 4 || (u == 0 && jj == 1)) BAR();
            if (u == 0) { if (jj == 0) STAMP(14); else STAMP(12); }
            stage(u, true, (u & 3) == 3);
        }
    }
    BAR();
    STAMP(13);
#pragma unroll
    for (int u = 0; u < 8; ++u) stage(u, u == 0, false);
    WAIT_LGKM0();
#pragma unroll
    for (int dt = 0; dt < 4; ++dt) { A_MFMA(acc[dt][0], A[1][dt], H[G2_HD - 1][1][0]); A_MFMA(acc[dt][1], A[1][dt], H[G2_HD - 1][1][1]); }
    A_MFMA_DRAIN();
    STAMP(6);
    BAR();
    STAMP(7);
    {
        float gg[2]; v2u yv[2][4], qv[2][4];
#pragma unroll
        for (int sx = 0; sx < 2; ++sx) {
            const int row = 32 * w + 16 * sx + s;
            gg[sx] = *(LAS float*)(L + G2_G + row * 4);
#pragma unroll
            for (int dt = 0; dt < 4; ++dt) {
                const int d = 16 * dt + 4 * g + q;
                yv[sx][dt] = tr_read(L + G2_Y + d * 256 + (((2 * w + sx) ^ (d & 7)) * 32) + 8 * pp);
                qv[sx][dt] = *(LAS v2u*)(L + G2_Q + row * 128 + (((2 * dt + (g >> 1)) ^ ((row >> 1) & 7)) * 16) + 8 * (g & 1));
            }
        }
        SCHED();
#pragma unroll
        for (int sx = 0; sx < 2; ++sx) {
            const int row = 32 * w + 16 * sx + s;
            const float omg = 1.f - gg[sx];
#pragma unroll
            for (int dt = 0; dt < 4; ++dt) {
                const f32x4 a = acc[dt][sx];
                const f32x2 o01 = bf_2(qv[sx][dt].x) * bf_2(yv[sx][dt].x) * gg[sx] + (f32x2){a[0], a[1]} * omg;
                const f32x2 o23 = bf_2(qv[sx][dt].y) * bf_2(yv[sx][dt].y) * gg[sx] + (f32x2){a[2], a[3]} * omg;
                *(LAS v2u*)(L + G2_Q + row * 128 + (((2 * dt + (g >> 1)) ^ ((row >> 1) & 7)) * 16) + 8 * (g & 1)) = (v2u){pk2(o01), pk2(o23)};
            }
        }
    }
    WAIT_LGKM0();
#pragma unroll
    for (int i = 0; i < 4; ++i) {
        const int row = 32 * w + 8 * i + (lane >> 3), c = lane & 7;
        const v4u val = *(LAS v4u*)(L + G2_Q + row * 128 + c * 16);
        *(GAS v4u*)((char*)p.out + (size_t)(panel * 128 + row) * (D * 2) + ct * 128 + ((c ^ ((row >> 1) & 7)) * 16)) = val;
    }
    STAMP(11);
}

__global__ void __launch_bounds__(NTHR, 2) k_fused(Params p) {
    extern __shared__ __attribute__((aligned(16))) char smem[];
    LAS char* L = (LAS char*)smem;
    const int tid = threadIdx.x, lane = tid & 63, w = __builtin_amdgcn_readfirstlane(tid >> 6);
    const int x = blockIdx.x & 7, idx = blockIdx.x >> 3, xr = x >> 1, xc = x & 1;
    STAMP(0);
    gu32* ctl = (gu32*)(p.ws + WS_CTL);
    unsigned epoch_v = __hip_atomic_load(ctl + W_EPOCH * FL, RLX_AGENT);
#define GET_TAG() asm volatile("" : "+v"(epoch_v)); const unsigned tag = __builtin_amdgcn_readfirstlane(epoch_v) + 1u
    const __amdgpu_buffer_rsrc_t rs = __builtin_amdgcn_make_buffer_rsrc(p.ws, 0, WS_END, 0x00020000);
    if (idx < 24) {
        const int panel = 4 * xr + idx / 6, ct = 6 * xc + idx % 6;
        const int off1 = 5 * ((idx / 6 + idx % 6) & 1);
        const bool typeA = idx < 20;
        const int panel2 = 4 * xr + idx / 5, ct2 = 5 * xc + idx % 5;
        const int off2 = 12 * ((idx / 5 + idx % 5) & 1);
        if (typeA) {
            if (w < 4) {
                ConvIn cin;
                g1_compute<true>(p, L, rs, panel, ct, w, lane, cin, 0 STAMP_PASS);
                STAMP(3);
                WAIT_VM(0);
                GET_TAG();
                STAMP(4);
                BAR();
                if (tid == 0) set_flag(ctl, W_H + panel * 12 + ct, tag);
                g2_compute(p, L, rs, panel2, ct2, off2, w, lane STAMP_PASS);
            } else {
                const int lw = w - 4;
                W2Src w2s;
                w2_init(w2s, p, ct2, off2, lane);
                g1_loader<true>(p, panel, ct, off1, w2s, lw, lane STAMP_PASS);
                BAR();
                g1_loader_epilogue(L, rs, panel, ct, lw, lane);
                w2_issue(w2s, 32 + lw); w2_issue(w2s, 36 + lw);
                WAIT_VM(8);
                GET_TAG();
                BAR();
                g2_loader(p, L, rs, tag, w2s, panel2, ct2, lw, lane STAMP_PASS);
            }
        } else {
            const int bw = x * 4 + idx - 20, pr = w >> 1, hf = w & 1;
            const int ch = 448 + 6 * bw + pr, chn = pr < 2 ? ch + 4 : -1;
            ConvIn cin;
            if (w < 4) {
                g1_compute<false>(p, L, rs, panel, ct, w, lane, cin, ch STAMP_PASS);
                STAMP(3);
            } else {
                W2Src w2s;
                g1_loader<false>(p, panel, ct, off1, w2s, w - 4, lane STAMP_PASS);
                conv_load_h(p, cin, ch, hf, lane);
                { GET_TAG(); if (w == 7) poll_flags(ctl, W_KV, 64, tag, lane); }
                BAR();
                conv_load_x(cin, rs, ch, hf, lane);
                g1_loader_epilogue(L, rs, panel, ct, w - 4, lane);
            }
            WAIT_VM(0);
            GET_TAG();
            STAMP(4);
            BAR();
            if (tid == 0) set_flag(ctl, W_H + panel * 12 + ct, tag);
            conv_rounds(p, L, rs, cin, ch, chn, pr, hf, lane STAMP_PASS);
            WAIT_VM(0);
            BAR();
            if (tid == 0) set_flag(ctl, W_Y + 64 + bw, tag);
            STAMP(16);
        }
    } else {
        const int cw = x * 8 + (idx - 24), pr = w >> 1, hf = w & 1;
        const int ch = 7 * cw + pr, chn = pr < 3 ? ch + 4 : -1;
        ConvIn cin;
        conv_load_h(p, cin, ch, hf, lane);
        prepass(p, L, rs, cw, tid STAMP_PASS);
        STAMP(2);
        WAIT_VM(0);
        GET_TAG();
        BAR();
        if (tid == 0) set_flag(ctl, W_KV + cw, tag);
        if (hf == 0 && lane < 8) *(LAS unsigned*)(L + pr * CV_PAIR + CV_SYNC + 4 * lane) = 0u;
        STAMP(3);
        if (w == 1) poll_flags(ctl, W_KV, 64, tag, lane);
        WAIT_LGKM0();
        BAR();
        STAMP(4);
        conv_load_x(cin, rs, ch, hf, lane);
        conv_rounds(p, L, rs, cin, ch, chn, pr, hf, lane STAMP_PASS);
        WAIT_VM(0);
        BAR();
        if (tid == 0) set_flag(ctl, W_Y + cw, tag);
        STAMP(16);
        if (w == 7 && blockIdx.x == 255) {
#pragma unroll 1
            for (int pn = 0; pn < 3; ++pn) poll_flags(ctl, W_H + pn * 64, 64, tag, lane);
            if (lane == 0) set_flag(ctl, W_EPOCH, tag);
        }
    }
    STAMP_FLUSH();
}

extern "C" void kernel_launch(void* const* d_in, const int* in_sizes, int n_in, void* d_out, int out_size, void* d_ws, size_t ws_size, hipStream_t stream) {
    Params p;
    p.q = (const u16*)d_in[0]; p.k = (const u16*)d_in[1]; p.v = (const u16*)d_in[2]; p.shk = (const u16*)d_in[3];
    p.ssm = (const u16*)d_in[4]; p.Dv = (const u16*)d_in[5]; p.w1 = (const u16*)d_in[7]; p.w2 = (const u16*)d_in[8]; p.gw = (const u16*)d_in[9];
    p.out = (u16*)d_out; p.ws = (char*)d_ws;
    (void)hipFuncSetAttribute((const void*)k_fused, hipFuncAttributeMaxDynamicSharedMemorySize, LDS_BYTES);
    k_fused<<<256, NTHR, LDS_BYTES, stream>>>(p);
}
```

```cpp
#include <hip/hip_runtime.h>
#include <stdint.h>
#include <type_traits>

typedef unsigned short u16;
typedef unsigned v4u __attribute__((ext_vector_type(4)));
typedef unsigned v2u __attribute__((ext_vector_type(2)));
typedef float f32x4 __attribute__((ext_vector_type(4)));
typedef float f32x2 __attribute__((ext_vector_type(2)));
typedef short bf16x8 __attribute__((ext_vector_type(8)));
typedef short s16x4 __attribute__((ext_vector_type(4)));
typedef __bf16 bf16x2_t __attribute__((ext_vector_type(2)));
#define LAS __attribute__((address_space(3)))
#define GAS __attribute__((address_space(1)))
typedef GAS unsigned gu32;
typedef GAS unsigned long long gu64;
#define RLX_AGENT __ATOMIC_RELAXED, __HIP_MEMORY_SCOPE_AGENT

constexpr int S = 2048, D = 640, F = 1536;
constexpr int NTHR = 512;
constexpr unsigned WS_H = 0;
constexpr unsigned WS_KV = WS_H + S * F * 2;
constexpr unsigned WS_Y = WS_KV + S * D * 2;
constexpr unsigned WS_G = WS_Y + S * D * 2;
constexpr unsigned WS_CTL = WS_G + S * 4;
constexpr int FL = 32;
constexpr int W_EPOCH = 0, W_H = 1, W_KV = W_H + 192, W_G = W_KV + 64, W_Y = W_G + 128, W_LINES = W_Y + 96;
constexpr unsigned WS_END = WS_CTL + W_LINES * 128;
constexpr int AUX_SC1 = 16;
constexpr int H_AUX = 16;
constexpr unsigned kSpinCap = 1u << 18;

struct Params {
    const u16 *q, *k, *v, *shk, *ssm, *Dv, *w1, *w2, *gw;
    u16* out;
    char* ws;
};

__device__ __forceinline__ float bf_lo(unsigned u) { return __uint_as_float(u << 16); }
__device__ __forceinline__ float bf_hi(unsigned u) { return __uint_as_float(u & 0xffff0000u); }
__device__ __forceinline__ f32x2 bf_2(unsigned u) { return (f32x2){bf_lo(u), bf_hi(u)}; }
__device__ __forceinline__ unsigned pk2(f32x2 v) { return __builtin_bit_cast(unsigned, __builtin_convertvector(v, bf16x2_t)); }
__device__ __forceinline__ unsigned pk_bf16(float lo, float hi) { return pk2((f32x2){lo, hi}); }
__device__ __forceinline__ float rbf(float x) { return bf_lo(pk_bf16(x, x)); }
__device__ __forceinline__ f32x4 mfma16(v4u a, v4u b, f32x4 c) { return __builtin_amdgcn_mfma_f32_16x16x32_bf16(__builtin_bit_cast(bf16x8, a), __builtin_bit_cast(bf16x8, b), c, 0, 0, 0); }
__device__ __forceinline__ v2u tr_read(LAS char* p) { return __builtin_bit_cast(v2u, __builtin_amdgcn_ds_read_tr16_b64_v4i16((LAS s16x4*)p)); }
__device__ __forceinline__ v4u lds_rd16(LAS char* p) { return *(LAS v4u*)p; }
__device__ __forceinline__ void glds16(unsigned voff, const void* sbase, unsigned lds_dst) {
    asm volatile("s_mov_b32 m0, %2\n\ts_nop 0\n\tglobal_load_lds_dwordx4 %0, %1" :: "v"(voff), "s"(sbase), "s"(lds_dst) : "memory");
}
template <bool NT> __device__ __forceinline__ void glds16t(unsigned voff, const void* sbase, unsigned lds_dst) {
    if (NT) asm volatile("s_mov_b32 m0, %2\n\ts_nop 0\n\tglobal_load_lds_dwordx4 %0, %1 nt" :: "v"(voff), "s"(sbase), "s"(lds_dst) : "memory");
    else glds16(voff, sbase, lds_dst);
}
#define NT_X 0
#define NT_W1 0
#define NT_W2 0
#define NT_KV 0
#define NT_SSM 0
template <bool NT> __device__ __forceinline__ v4u gld16(const void* ptr) {
    if (NT) return __builtin_nontemporal_load((const GAS v4u*)ptr);
    return *(const GAS v4u*)ptr;
}
#define WAIT_VM(n) asm volatile("s_waitcnt vmcnt(%0)" :: "n"(n) : "memory")
#define WAIT_LGKM0() asm volatile("s_waitcnt lgkmcnt(0)" ::: "memory")
#define BAR() do { asm volatile("" ::: "memory"); __builtin_amdgcn_s_barrier(); asm volatile("" ::: "memory"); } while (0)
#define SCHED() __builtin_amdgcn_sched_barrier(0)
#define A_MFMA(acc, a, b) asm volatile("v_mfma_f32_16x16x32_bf16 %0, %1, %2, %0" : "+v"(acc) : "v"(a), "v"(b))
#define A_RD128(dst, addr, off) asm volatile("ds_read_b128 %0, %1 offset:%2" : "=v"(dst) : "v"(addr), "n"(off))
#define A_RDTR(dst, addr, off) asm volatile("ds_read_b64_tr_b16 %0, %1 offset:%2" : "=v"(dst) : "v"(addr), "n"(off))
#define A_LGKM(n) asm volatile("s_waitcnt lgkmcnt(%0)" :: "n"(n))
#define A_MFMA_DRAIN() asm volatile("s_nop 15\n\ts_nop 7" ::: "memory")
template <int NM, int NR, int PER> __device__ __forceinline__ void interleave() {
#pragma unroll
    for (int i = 0; i < NM; ++i) {
        __builtin_amdgcn_sched_group_barrier(0x8, 1, 0);
        if (i * PER < NR) __builtin_amdgcn_sched_group_barrier(0x100, PER, 0);
    }
}
constexpr size_t WS_DBG = 64u << 20;
struct Stamps { unsigned r0, r1; };
#define STAMP(id) do { if ((id) == 0 || (id) == 0) { unsigned long long r_; asm volatile("s_memrealtime %0\n\ts_waitcnt lgkmcnt(0)" : "=s"(r_) :: "memory"); if ((id) == 0) sp.r0 = (unsigned)r_; if ((id) == 0) sp.r1 = (unsigned)r_; } } while (0)
#define STAMP_ARG , Stamps& sp
#define STAMP_PASS , sp
#define STAMP_FLUSH() do { if ((threadIdx.x & 63) == 0) { GAS unsigned* d_ = (GAS unsigned*)(p.ws + WS_DBG) + ((size_t)blockIdx.x * 8 + (threadIdx.x >> 6)) * 2; d_[0] = sp.r0; d_[1] = sp.r1; } } while (0)

__device__ __forceinline__ void st16_sc1(v4u v, __amdgpu_buffer_rsrc_t rs, unsigned off) { __builtin_amdgcn_raw_buffer_store_b128(v, rs, off, 0, AUX_SC1); }
__device__ __forceinline__ void poll_flags(gu32* ctl, int line, int n, unsigned tag, int lane) {
    gu32* f = ctl + (line + (lane < n ? lane : 0)) * FL;
#pragma unroll 1
    for (unsigned spins = 0; spins < kSpinCap; ++spins) {
        const unsigned v = __hip_atomic_load(f, RLX_AGENT);
        if (__all(v == tag)) break;
        __builtin_amdgcn_s_sleep(4);
    }
    __builtin_amdgcn_fence(__ATOMIC_ACQUIRE, "wavefront");
}
__device__ __forceinline__ void set_flag(gu32* ctl, int line, unsigned tag) { __hip_atomic_store(ctl + line * FL, tag, RLX_AGENT); }
__device__ __forceinline__ unsigned silu2f(f32x2 h) {
    const f32x2 t = h * -1.44269504f;
    const f32x2 den = (f32x2){__builtin_amdgcn_exp2f(t.x), __builtin_amdgcn_exp2f(t.y)} + 1.f;
    const f32x2 r = (f32x2){__builtin_amdgcn_rcpf(den.x), __builtin_amdgcn_rcpf(den.y)};
    return pk2(h * r);
}
__device__ __forceinline__ unsigned silu2(unsigned hb) { return silu2f(bf_2(hb)); }
__device__ __forceinline__ float row_sum16(float v) {
    v += __builtin_bit_cast(float, __builtin_amdgcn_update_dpp(0, __builtin_bit_cast(int, v), 0x128, 0xf, 0xf, false));
    v += __builtin_bit_cast(float, __builtin_amdgcn_update_dpp(0, __builtin_bit_cast(int, v), 0x124, 0xf, 0xf, false));
    v += __builtin_bit_cast(float, __builtin_amdgcn_update_dpp(0, __builtin_bit_cast(int, v), 0x122, 0xf, 0xf, false));
    v += __builtin_bit_cast(float, __builtin_amdgcn_update_dpp(0, __builtin_bit_cast(int, v), 0x121, 0xf, 0xf, false));
    return v;
}

constexpr int G1_SLOT = 32768, G1_NS = 5, G1_NST = D / 64, G1_STG = 3 * G1_SLOT;
constexpr int G2_NST = F / 64, G2_RING = 40 * 4096, G2_HD = 8;
constexpr int G2_Q = 8 * 4096, G2_Y = 12 * 4096, G2_G = 16 * 4096;
constexpr int CV_COPY = 4160, CV_X = 4 * CV_COPY, CV_PAIR = CV_X + 4608;
constexpr int CV_EX = 4 * CV_PAIR;
constexpr int LDS_BYTES = 163840;
static_assert(CV_EX + 4 * 8192 <= LDS_BYTES, "LDS map");

struct W2Src { unsigned vw[4]; const char* base; int kb0; unsigned aw[4]; };
__device__ __forceinline__ void w2_init(W2Src& s, const Params& p, int ct, int off2, int lane) {
#pragma unroll
    for (int i = 0; i < 4; ++i) {
        const int r = lane >> 3, c = lane & 7, k = 8 * i + r;
        s.vw[i] = (unsigned)(k * (D * 2) + ((c ^ (((k >> 1) & 3) << 1)) * 16));
    }
    const int g = lane >> 4, q = (lane & 15) >> 2, pp = lane & 3, sw = ((g & 1) << 1) | (q >> 1);
#pragma unroll
    for (int dt = 0; dt < 4; ++dt) s.aw[dt] = (4 * g + q) * 128 + ((dt ^ sw) * 32) + 8 * pp;
    s.base = (const char*)p.w2 + ct * 128; s.kb0 = 2 * off2;
}
__device__ __forceinline__ void w2_issue(const W2Src& s, int b) {
    const int kb = s.kb0 + b >= 48 ? s.kb0 + b - 48 : s.kb0 + b;
    const char* src = s.base + (size_t)kb * (32 * D * 2);
    const unsigned slot = (unsigned)(b >= 40 ? b - 40 : b) * 4096;
#pragma unroll
    for (int i = 0; i < 4; ++i) glds16t<NT_W2>(s.vw[i], src, slot + i * 1024);
}
__device__ __forceinline__ void w2_repack(const W2Src& s, LAS char* L, int b, int lane) {
    LAS char* blk = L + (b >= 40 ? b - 40 : b) * 4096;
    v4u f[4];
#pragma unroll
    for (int dt = 0; dt < 4; ++dt) { const v2u lo = tr_read(blk + s.aw[dt]), hi = tr_read(blk + s.aw[dt] + 2048); f[dt] = (v4u){lo.x, lo.y, hi.x, hi.y}; }
    WAIT_LGKM0();
#pragma unroll
    for (int dt = 0; dt < 4; ++dt) *(LAS v4u*)(blk + dt * 1024 + lane * 16) = f[dt];
    WAIT_LGKM0();
}

template <bool TYPE_A>
__device__ __forceinline__ void g1_loader(const Params& p, int panel, int ct, int off1, const W2Src& w2s, int lw, int lane STAMP_ARG) {
    unsigned vx[4], vw[4];
#pragma unroll
    for (int i = 0; i < 4; ++i) {
        const int r = lane >> 3, c = lane & 7, row = 32 * lw + 8 * i + r;
        vx[i] = (unsigned)(row * (D * 2) + ((c ^ ((row >> 1) & 7)) * 16));
        const int r2 = lane >> 4, c2 = lane & 15, k = 16 * lw + 4 * i + r2, phi = (k & 3) | (((k >> 3) & 1) << 2);
        vw[i] = (unsigned)(k * (F * 2) + ((c2 ^ (phi << 1)) * 16));
    }
    const char* xb = (const char*)p.q + (size_t)panel * 128 * D * 2;
    const char* wb = (const char*)p.w1 + (size_t)ct * 256;
    int kst = off1;
    auto issue = [&](int slot_i) {
        const unsigned slot = (unsigned)slot_i * G1_SLOT;
        const char* xs = xb + kst * 128; const char* wsrc = wb + (size_t)kst * (64 * F * 2);
#pragma unroll
        for (int i = 0; i < 4; ++i) glds16t<NT_X>(vx[i], xs, slot + (32 * lw + 8 * i) * 128);
#pragma unroll
        for (int i = 0; i < 4; ++i) glds16t<NT_W1>(vw[i], wsrc, slot + 16384 + (16 * lw + 4 * i) * 256);
        kst = kst + 1 == G1_NST ? 0 : kst + 1;
    };
#pragma unroll
    for (int st = 0; st < 3; ++st) issue(st);
    WAIT_VM(16); BAR(); issue(3); issue(4);
#pragma unroll 1
    for (int i = 1; i < 6; ++i) { WAIT_VM(24); BAR(); issue(i - 1); }
    STAMP(1);
    if (TYPE_A) {
        WAIT_VM(24); BAR(); w2_issue(w2s, 0 + lw); w2_issue(w2s, 4 + lw);
        WAIT_VM(24); BAR(); w2_issue(w2s, 8 + lw); w2_issue(w2s, 12 + lw);
        WAIT_VM(24); BAR(); w2_issue(w2s, 16 + lw); w2_issue(w2s, 20 + lw);
        WAIT_VM(24); BAR(); w2_issue(w2s, 28 + lw);
    } else {
        WAIT_VM(24); BAR(); WAIT_VM(16); BAR(); WAIT_VM(8); BAR(); WAIT_VM(0); BAR();
    }
}
__device__ __forceinline__ void g1_store(__amdgpu_buffer_rsrc_t rs, int stg, int ksg, int lane, v4u hb) {
    const v4u o = {silu2(hb.x), silu2(hb.y), silu2(hb.z), silu2(hb.w)};
    st16_sc1(o, rs, WS_H + (stg * 48 + ksg) * 1024 + lane * 16);
}
struct ConvIn { v4u v[2], vp[2], x[2]; unsigned vq[2]; float Dd; };
__device__ __forceinline__ void conv_load_h(const Params& p, ConvIn& c, int ch, int hf, int lane) {
    const char* hsrc = (const char*)p.ssm + (size_t)ch * (S * 2);
#pragma unroll
    for (int j = 0; j < 2; ++j) {
        const int o = (2 * hf + j) * 1024 + lane * 16;
        c.v[j] = gld16<NT_SSM>(hsrc + o);
        c.vp[j] = gld16<NT_SSM>(hsrc + (o < 4 ? 0 : o - 4));
        c.vq[j] = *(const GAS unsigned*)(hsrc + (o < 8 ? 0 : o - 8));
    }
    c.Dd = bf_lo((unsigned)p.Dv[ch]);
}
__device__ __forceinline__ void conv_load_x(ConvIn& c, __amdgpu_buffer_rsrc_t rs, int ch, int hf, int lane) {
#pragma unroll
    for (int j = 0; j < 2; ++j) c.x[j] = __builtin_amdgcn_raw_buffer_load_b128(rs, (2 * hf + j) * 1024 + lane * 16, WS_KV + ch * (S * 2), AUX_SC1);
}
constexpr int CV_SYNC = 4128;
template <bool TYPE_A>
__device__ __forceinline__ void g1_compute(const Params& p, LAS char* L, __amdgpu_buffer_rsrc_t rs, int panel, int ct, int w, int lane, ConvIn& cin, int ch STAMP_ARG) {
    const int wf = w & 1, wsx = w >> 1;
    const int g = lane >> 4, q = (lane & 15) >> 2, pp = lane & 3, s = lane & 15;
    const int phi = q | ((g & 1) << 2);
    unsigned aw[4], ax[2];
#pragma unroll
    for (int ft = 0; ft < 4; ++ft) aw[ft] = 16384 + (8 * g + q) * 256 + (((4 * wf + ft) ^ phi) * 32) + 8 * pp;
#pragma unroll
    for (int ks = 0; ks < 2; ++ks) ax[ks] = (64 * wsx + s) * 128 + (((4 * ks + g) ^ ((s >> 1) & 7)) * 16);
    f32x4 acc[4][4];
#pragma unroll
    for (int a = 0; a < 4; ++a)
#pragma unroll
        for (int b = 0; b < 4; ++b) acc[a][b] = (f32x4){0.f, 0.f, 0.f, 0.f};
    v4u A[2][4], B[2][4];
#pragma unroll
    for (int a = 0; a < 4; ++a) { A[1][a] = (v4u){0u, 0u, 0u, 0u}; B[1][a] = (v4u){0u, 0u, 0u, 0u}; }
    auto rd = [&](int set, LAS char* base, int ks) {
#pragma unroll
        for (int ft = 0; ft < 4; ++ft) {
            const v2u lo = tr_read(base + aw[ft] + ks * 8192), hi = tr_read(base + aw[ft] + ks * 8192 + 1024);
            A[set][ft] = (v4u){lo.x, lo.y, hi.x, hi.y};
            if (ft == 0) {
#pragma unroll
                for (int sx = 0; sx < 4; ++sx) B[set][sx] = lds_rd16(base + ax[ks] + sx * 2048);
            }
        }
    };
    auto mm = [&](int set) {
#pragma unroll
        for (int ft = 0; ft < 4; ++ft)
#pragma unroll
            for (int sx = 0; sx < 4; ++sx) acc[ft][sx] = mfma16(A[set][ft], B[set][sx], acc[ft][sx]);
    };
    unsigned slot = 0;
#define X_G1REP 1
#pragma unroll 1
    for (int i = 0; i < G1_NST * X_G1REP; ++i) {
        LAS char* base = L + slot;
        slot = slot + G1_SLOT == G1_NS * G1_SLOT ? 0 : slot + G1_SLOT;
        BAR();
        if (i == 0) STAMP(1);
        rd(0, base, 0);
        mm(1);
        interleave<16, 12, 1>();
        SCHED();
        rd(1, base, 1);
        mm(0);
        interleave<16, 12, 1>();
        SCHED();
        WAIT_LGKM0();
    }
    mm(1);
    STAMP(2);
    if (!TYPE_A) conv_load_h(p, cin, ch, w & 1, lane);
    SCHED();
#pragma unroll
    for (int j = 0; j < 4; ++j) {
        const f32x4 a = acc[2 * (j & 1)][2 + (j >> 1)], b = acc[2 * (j & 1) + 1][2 + (j >> 1)];
        *(LAS v4u*)(L + G1_STG + w * 4096 + j * 1024 + lane * 16) = (v4u){pk_bf16(a[0], a[1]), pk_bf16(a[2], a[3]), pk_bf16(b[0], b[1]), pk_bf16(b[2], b[3])};
    }
    if (!TYPE_A && lane < 8) *(LAS unsigned*)(L + w * CV_PAIR + CV_SYNC + 4 * lane) = 0u;
    WAIT_LGKM0();
    BAR();
    if (!TYPE_A) conv_load_x(cin, rs, ch, w & 1, lane);
    SCHED();
#pragma unroll
    for (int j = 0; j < 4; ++j) {
        const f32x4 a = acc[2 * (j & 1)][j >> 1], b = acc[2 * (j & 1) + 1][j >> 1];
        const v4u o = {silu2f((f32x2){a[0], a[1]}), silu2f((f32x2){a[2], a[3]}), silu2f((f32x2){b[0], b[1]}), silu2f((f32x2){b[2], b[3]})};
        st16_sc1(o, rs, WS_H + ((panel * 8 + wsx * 4 + (j >> 1)) * 48 + ct * 4 + wf * 2 + (j & 1)) * 1024 + lane * 16);
    }
}
__device__ __forceinline__ void g1_loader_epilogue(LAS char* L, __amdgpu_buffer_rsrc_t rs, int panel, int ct, int lw, int lane) {
    const int wf = lw & 1, wsx = lw >> 1;
    v4u hb[4];
#pragma unroll
    for (int j = 0; j < 4; ++j) hb[j] = *(LAS v4u*)(L + G1_STG + lw * 4096 + j * 1024 + lane * 16);
#pragma unroll
    for (int j = 0; j < 4; ++j) g1_store(rs, panel * 8 + wsx * 4 + 2 + (j >> 1), ct * 4 + wf * 2 + (j & 1), lane, hb[j]);
}

__device__ __forceinline__ void prepass(const Params& p, LAS char* L, __amdgpu_buffer_rsrc_t rs, int wg, int tid STAMP_ARG) {
    const int s0 = 64 * (wg >> 1), c0 = 320 * (wg & 1);
    {
        const int w = __builtin_amdgcn_readfirstlane(tid >> 6);
        const int item = w < 4 ? tid : 256 + (tid & 63);
        const int cc = item % 40, rg = item / 40;
        const int ch = c0 + 8 * cc, r0 = s0 + 8 * rg;
        v4u kk[11], vv[8], sw[4];
#pragma unroll
        for (int j = 0; j < 4; ++j) sw[j] = *(const GAS v4u*)((const char*)p.shk + (size_t)ch * 8 + j * 16);
#pragma unroll
        for (int j = 0; j < 11; ++j) {
            const int row = r0 - 3 + j;
            kk[j] = gld16<NT_KV>((const char*)p.k + (size_t)(row < 0 ? 0 : row) * (D * 2) + ch * 2);
        }
#pragma unroll
        for (int j = 0; j < 8; ++j) vv[j] = gld16<NT_KV>((const char*)p.v + (size_t)(r0 + j) * (D * 2) + ch * 2);
        if (r0 == 0) { kk[0] = (v4u){0u, 0u, 0u, 0u}; kk[1] = kk[0]; kk[2] = kk[0]; }
#pragma unroll
        for (int e = 0; e < 4; ++e) {
            if (w >= 4 && w - 4 != e) continue;
            const f32x2 W0 = {bf_lo(sw[e].x), bf_lo(sw[e].z)}, W1 = {bf_hi(sw[e].x), bf_hi(sw[e].z)};
            const f32x2 W2 = {bf_lo(sw[e].y), bf_lo(sw[e].w)}, W3 = {bf_hi(sw[e].y), bf_hi(sw[e].w)};
            f32x2 kf[11], o[8];
#pragma unroll
            for (int j = 0; j < 11; ++j) kf[j] = bf_2(kk[j][e]);
#pragma unroll
            for (int j = 0; j < 8; ++j) {
                const f32x2 ksh = kf[j] * W3 + kf[j + 1] * W2 + kf[j + 2] * W1 + kf[j + 3] * W0;
                o[j] = ksh * bf_2(vv[j][e]);
            }
            const v4u p0 = {pk_bf16(o[0].x, o[1].x), pk_bf16(o[2].x, o[3].x), pk_bf16(o[4].x, o[5].x), pk_bf16(o[6].x, o[7].x)};
            const v4u p1 = {pk_bf16(o[0].y, o[1].y), pk_bf16(o[2].y, o[3].y), pk_bf16(o[4].y, o[5].y), pk_bf16(o[6].y, o[7].y)};
            *(LAS v4u*)(L + (8 * cc + 2 * e) * 128 + ((rg ^ (cc & 7)) * 16)) = p0;
            *(LAS v4u*)(L + (8 * cc + 2 * e + 1) * 128 + ((rg ^ (cc & 7)) * 16)) = p1;
        }
    }
    STAMP(1);
    WAIT_LGKM0();
    BAR();
#pragma unroll
    for (int j = 0; j < 5; ++j) {
        const int n = tid + NTHR * j, ch = n >> 3, pc = n & 7;
        const v4u val = *(LAS v4u*)(L + ch * 128 + ((pc ^ ((ch >> 3) & 7)) * 16));
        st16_sc1(val, rs, WS_KV + (c0 + ch) * (S * 2) + s0 * 2 + pc * 16);
    }
}
struct GateIn { v4u gw[5], q[5]; };
__device__ __forceinline__ void gate_load(const Params& p, GateIn& gi, int row0, int lane) {
    const int i = lane & 15, row = row0 + (lane >> 4);
#pragma unroll
    for (int j = 0; j < 5; ++j) {
        gi.gw[j] = *(const GAS v4u*)((const char*)p.gw + (i + 16 * j) * 16);
        gi.q[j] = *(const GAS v4u*)((const char*)p.q + (size_t)row * (D * 2) + (i + 16 * j) * 16);
    }
}
__device__ __forceinline__ void gate_finish(const GateIn& gi, __amdgpu_buffer_rsrc_t rs, int row0, int lane) {
    f32x2 a2 = {0.f, 0.f};
#pragma unroll
    for (int j = 0; j < 5; ++j)
#pragma unroll
        for (int e = 0; e < 4; ++e) a2 += bf_2(gi.q[j][e]) * bf_2(gi.gw[j][e]);
    const float z = rbf(row_sum16(a2.x + a2.y));
    const float gv = rbf(__builtin_amdgcn_rcpf(1.f + __builtin_amdgcn_exp2f(z * -1.44269504f)));
    if ((lane & 15) == 0) __builtin_amdgcn_raw_buffer_store_b32(__float_as_uint(gv), rs, WS_G + (row0 + (lane >> 4)) * 4, 0, AUX_SC1);
}

__device__ __forceinline__ void pair_sync(LAS char* words, int hf) {
    WAIT_LGKM0();
    *(volatile LAS unsigned*)(words + 4 * hf) = 1u;
#pragma unroll 1
    for (unsigned spins = 0; spins < (1u << 22); ++spins) { if (__builtin_amdgcn_readfirstlane(*(volatile LAS unsigned*)(words + 4 * (1 - hf))) != 0u) break; }
    asm volatile("" ::: "memory");
}
template <int hf>
__device__ __forceinline__ void conv_half(const Params& p, LAS char* Lw, LAS char* Lx, __amdgpu_buffer_rsrc_t rs, ConvIn& c, int ch, int chn, int round, int lane STAMP_ARG) {
    const int g = lane >> 4, q = (lane & 15) >> 2, pp = lane & 3, n = lane & 15;
    if (hf == 0) {
        if (lane < 30) *(LAS v4u*)(Lw + CV_X + lane * 16) = (v4u){0u, 0u, 0u, 0u};
        if (lane < 8) *(LAS v4u*)(Lw + (lane >> 1) * CV_COPY + (lane & 1) * 16) = (v4u){0u, 0u, 0u, 0u};
        if (lane == 0) { c.vp[0] = (v4u){0u, c.v[0].x, c.v[0].y, c.v[0].z}; c.vq[0] = 0u; }
    }
#pragma unroll
    for (int j = 0; j < 2; ++j) {
        const v4u v = c.v[j], vp = c.vp[j];
        const unsigned a0 = __builtin_amdgcn_alignbit(v.x, vp.x, 16), a1 = __builtin_amdgcn_alignbit(v.y, v.x, 16), a2 = __builtin_amdgcn_alignbit(v.z, v.y, 16), a3 = __builtin_amdgcn_alignbit(v.w, v.z, 16);
        const unsigned am = __builtin_amdgcn_alignbit(vp.x, c.vq[j], 16);
        LAS char* d = Lw + 32 + (2 * hf + j) * 1024 + lane * 16;
        *(LAS v4u*)(d) = v;
        *(LAS v4u*)(d + CV_COPY) = (v4u){a0, a1, a2, a3};
        *(LAS v4u*)(d + 2 * CV_COPY) = vp;
        *(LAS v4u*)(d + 3 * CV_COPY) = (v4u){am, a0, a1, a2};
        *(LAS v4u*)(Lw + CV_X + 480 + (2 * hf + j) * 1024 + lane * 16) = c.x[j];
    }
    const float Dd = c.Dd;
    pair_sync(Lw + CV_SYNC + 16 * round, hf);
    if (chn >= 0) { conv_load_h(p, c, chn, hf, lane); conv_load_x(c, rs, chn, hf, lane); asm volatile("" ::: "memory"); }
    if (round == 0) STAMP(8); else STAMP(12);
    const unsigned la = (unsigned)(size_t)Lw + q * CV_COPY + 32 - 16 * g + 8 * pp + 24 + 256 * hf;
    const unsigned lb = (unsigned)(size_t)Lw + CV_X + 32 * n + 16 * g + 448 - 256 * hf - 192;
    f32x4 acc[8];
#pragma unroll
    for (int t = 0; t < 8; ++t) acc[t] = (f32x4){0.f, 0.f, 0.f, 0.f};
    v2u Alo[2][8], Ahi[2][8];
    v4u B[2][8];
#define CV_RD(set, rs_, k) do { \
        if ((k) % 3 == 0) A_RDTR(Alo[set][(k) / 3], la, 64 * (rs_) + 512 * ((k) / 3) + 8); \
        else if ((k) % 3 == 1) A_RDTR(Ahi[set][(k) / 3], la, 64 * (rs_) + 512 * ((k) / 3)); \
        else A_RD128(B[set][(k) / 3], lb, 192 - 64 * (rs_) + 512 * ((k) / 3)); } while (0)
    auto step = [&](auto SET, auto NEXT) {
        constexpr int set = decltype(SET)::value, nx = decltype(NEXT)::value;
        int k = 0;
#pragma unroll
        for (int l = 0; l < 8; ++l)
#pragma unroll
            for (int i = 0; i + l < 8; ++i) {
                const v4u a = {Alo[set][i].x, Alo[set][i].y, Ahi[set][i].x, Ahi[set][i].y};
                A_MFMA(acc[i + l], a, B[set][l]);
                if (nx >= 0 && k < 24) { CV_RD(1 - set, (nx >= 0 ? nx : 0), k); }
                ++k;
            }
        if (nx >= 0) A_LGKM(0);
    };
#pragma unroll
    for (int k = 0; k < 24; ++k) CV_RD(0, 0, k);
    A_LGKM(0);
    step(std::integral_constant<int, 0>{}, std::integral_constant<int, 1>{});
    step(std::integral_constant<int, 1>{}, std::integral_constant<int, 2>{});
    step(std::integral_constant<int, 0>{}, std::integral_constant<int, 3>{});
    step(std::integral_constant<int, 1>{}, std::integral_constant<int, -1>{});
#undef CV_RD
    A_MFMA_DRAIN();
    if (round == 0) STAMP(9); else STAMP(13);
#pragma unroll
    for (int t = 0; t < 4; ++t) *(LAS f32x4*)(Lx + hf * 4096 + t * 1024 + lane * 16) = acc[4 * (1 - hf) + t];
    v2u xv[4];
#pragma unroll
    for (int t = 0; t < 4; ++t) xv[t] = *(LAS v2u*)(Lw + CV_X + 480 + 2 * (1024 * hf + 256 * t + 16 * n + 4 * g));
    pair_sync(Lw + CV_SYNC + 16 * round + 8, hf);
    f32x4 o[4];
#pragma unroll
    for (int t = 0; t < 4; ++t) o[t] = *(LAS f32x4*)(Lx + (1 - hf) * 4096 + t * 1024 + lane * 16);
    v2u ov[4];
#pragma unroll
    for (int t = 0; t < 4; ++t) {
        const f32x4 y = acc[4 * hf + t] + o[t];
        ov[t] = (v2u){pk2(bf_2(xv[t].x) * Dd + (f32x2){y[0], y[1]}), pk2(bf_2(xv[t].y) * Dd + (f32x2){y[2], y[3]})};
    }
    LAS char* Ly = Lx + (1 - hf) * 4096;
#pragma unroll
    for (int t = 0; t < 4; ++t) *(LAS v2u*)(Ly + 2 * (256 * t + 16 * n + 4 * g)) = ov[t];
    WAIT_LGKM0();
#pragma unroll
    for (int j = 0; j < 2; ++j) {
        const v4u val = *(LAS v4u*)(Ly + j * 1024 + lane * 16);
        st16_sc1(val, rs, WS_Y + ch * (S * 2) + hf * 2048 + j * 1024 + lane * 16);
    }
    if (round == 0) STAMP(10); else STAMP(14);
}

__device__ __forceinline__ void conv_rounds(const Params& p, LAS char* L, __amdgpu_buffer_rsrc_t rs, ConvIn& c, int ch, int chn, int pr, int hf, int lane STAMP_ARG) {
    LAS char* Lw = L + pr * CV_PAIR; LAS char* Lx = L + CV_EX + pr * 8192;
#pragma unroll 1
    for (int round = 0; round < 2; ++round) {
        if (hf) conv_half<1>(p, Lw, Lx, rs, c, ch, chn, round, lane STAMP_PASS); else conv_half<0>(p, Lw, Lx, rs, c, ch, chn, round, lane STAMP_PASS);
        if (chn < 0) break;
        ch = chn; chn = -1;
    }
}

__device__ __forceinline__ void g2_loader(const Params& p, LAS char* L, __amdgpu_buffer_rsrc_t rs, unsigned tag, const W2Src& w2s, int panel, int ct, int lw, int lane STAMP_ARG) {
    gu32* ctl = (gu32*)(p.ws + WS_CTL);
    const int grow = panel * 128 + (ct & 7) * 16 + 4 * lw;
    if (lw == 0) poll_flags(ctl, W_H + panel * 12, 12, tag, lane);
    w2_issue(w2s, 24 + lw);
    GateIn gi;
    gate_load(p, gi, grow, lane);
    asm volatile("" ::: "memory");
    w2_repack(w2s, L, 0 + lw, lane); w2_repack(w2s, L, 4 + lw, lane);
    STAMP(5);
    BAR();
    w2_repack(w2s, L, 8 + lw, lane); w2_repack(w2s, L, 12 + lw, lane);
    if (ct < 8) gate_finish(gi, rs, grow, lane);
    else WAIT_VM(0);
    BAR();
    w2_issue(w2s, 40 + lw); w2_issue(w2s, 44 + lw);
    w2_repack(w2s, L, 16 + lw, lane); w2_repack(w2s, L, 20 + lw, lane);
    WAIT_VM(8);
    BAR();
    if (ct < 8 && lw == 0 && lane == 0) set_flag(ctl, W_G + panel * 8 + ct, tag);
#pragma unroll
    for (int i = 0; i < 4; ++i) {
        const int r = lane >> 3, c = lane & 7, row = 32 * lw + 8 * i + r;
        glds16((unsigned)(row * (D * 2) + ((c ^ ((row >> 1) & 7)) * 16)), (const char*)p.q + (size_t)panel * 128 * D * 2 + ct * 128, G2_Q + (32 * lw + 8 * i) * 128);
    }
#pragma unroll 1
    for (int b = 24; b < 40; b += 4) w2_repack(w2s, L, b + lw, lane);
    BAR();
    WAIT_VM(4);
    w2_repack(w2s, L, 40 + lw, lane); w2_repack(w2s, L, 44 + lw, lane);
    BAR();
    const int y0 = ct < 7 ? (64 * ct) / 7 : 64 + (64 * ct - 448) / 6, y1 = ct < 7 ? (64 * ct + 63) / 7 : 64 + (64 * ct + 63 - 448) / 6;
    poll_flags(ctl, W_Y + y0, y1 - y0 + 1, tag, lane);
    poll_flags(ctl, W_G + panel * 8, 8, tag, lane);
    STAMP(6);
    v4u yv[4];
#pragma unroll
    for (int i = 0; i < 4; ++i) {
        const int r = lane >> 4, c = lane & 15, d = 16 * lw + 4 * i + r;
        yv[i] = __builtin_amdgcn_raw_buffer_load_b128(rs, d * (S * 2) + c * 16, WS_Y + ct * 64 * S * 2 + panel * 256, AUX_SC1);
    }
    const unsigned gv = __builtin_amdgcn_raw_buffer_load_b32(rs, (lw * 32 + (lane & 31)) * 4, WS_G + panel * 512, AUX_SC1);
#pragma unroll
    for (int i = 0; i < 4; ++i) {
        const int r = lane >> 4, c = lane & 15, d = 16 * lw + 4 * i + r;
        *(LAS v4u*)(L + G2_Y + d * 256 + ((c ^ ((d & 7) << 1)) * 16)) = yv[i];
    }
    if (lane < 32) *(LAS unsigned*)(L + G2_G + (lw * 32 + lane) * 4) = gv;
    WAIT_VM(0);
    WAIT_LGKM0();
    BAR();
}
__device__ __forceinline__ void g2_compute(const Params& p, LAS char* L, __amdgpu_buffer_rsrc_t rs, int panel, int ct, int off2, int w, int lane STAMP_ARG) {
    const int g = lane >> 4, q = (lane & 15) >> 2, pp = lane & 3, s = lane & 15;
    const unsigned hbase = WS_H + (panel * 8 + 2 * w) * (48 * 1024);
    int kld = off2;
    f32x4 acc[4][2];
#pragma unroll
    for (int a = 0; a < 4; ++a) { acc[a][0] = (f32x4){0.f, 0.f, 0.f, 0.f}; acc[a][1] = (f32x4){0.f, 0.f, 0.f, 0.f}; }
    v4u H[G2_HD][2][2];
    auto ldh = [&](int slot) {
#pragma unroll
        for (int ks = 0; ks < 2; ++ks)
#pragma unroll
            for (int sx = 0; sx < 2; ++sx) H[slot][ks][sx] = __builtin_amdgcn_raw_buffer_load_b128(rs, lane * 16 + sx * (48 * 1024) + ks * 1024, hbase + kld * 2048, H_AUX);
        kld = kld + 1 == G2_NST ? 0 : kld + 1;
    };
    BAR();
    STAMP(5);
#pragma unroll
    for (int st = 0; st < G2_HD - 1; ++st) ldh(st);
#pragma unroll
    for (int a = 0; a < 4; ++a) H[G2_HD - 1][a >> 1][a & 1] = (v4u){0u, 0u, 0u, 0u};
    SCHED();
    v4u A[2][4];
#pragma unroll
    for (int a = 0; a < 4; ++a) A[1][a] = (v4u){0u, 0u, 0u, 0u};
    auto half = [&](int set, int slot, int ks, unsigned addr, int off) {
        A_LGKM(3);
        A_MFMA(acc[0][0], A[set][0], H[slot][ks][0]); A_RD128(A[1 - set][0], addr, off);
        A_MFMA(acc[0][1], A[set][0], H[slot][ks][1]); A_RD128(A[1 - set][1], addr, off + 1024);
        A_LGKM(4);
        A_MFMA(acc[1][0], A[set][1], H[slot][ks][0]); A_RD128(A[1 - set][2], addr, off + 2048);
        A_MFMA(acc[1][1], A[set][1], H[slot][ks][1]); A_RD128(A[1 - set][3], addr, off + 3072);
        A_LGKM(5);
        A_MFMA(acc[2][0], A[set][2], H[slot][ks][0]);
        A_MFMA(acc[2][1], A[set][2], H[slot][ks][1]);
        A_LGKM(4);
        A_MFMA(acc[3][0], A[set][3], H[slot][ks][0]);
        A_MFMA(acc[3][1], A[set][3], H[slot][ks][1]);
    };
    const unsigned lbase = (unsigned)(size_t)L + lane * 16;
    unsigned sb = 0;
    auto stage = [&](int u, bool load, bool drain) {
        const unsigned addr = lbase + sb;
        half(1, (u + G2_HD - 1) % G2_HD, 1, addr, 0);
        if (load) { ldh((u + G2_HD - 1) % G2_HD); asm volatile("" ::: "memory"); }
        half(0, u, 0, addr, 4096);
        if (drain) WAIT_LGKM0();
        sb = sb + 8192 == G2_RING ? 0 : sb + 8192;
    };
#pragma unroll 1
    for (int jj = 0; jj < 2; ++jj) {
#pragma unroll
        for (int u = 0; u < 8; ++u) {
            if (u == 4 || (u == 0 && jj == 1)) BAR();
            if (u == 0) { if (jj == 0) STAMP(14); else STAMP(12); }
            stage(u, true, (u & 3) == 3);
        }
    }
    BAR();
    STAMP(13);
#pragma unroll
    for (int u = 0; u < 8; ++u) stage(u, u == 0, false);
    WAIT_LGKM0();
#pragma unroll
    for (int dt = 0; dt < 4; ++dt) { A_MFMA(acc[dt][0], A[1][dt], H[G2_HD - 1][1][0]); A_MFMA(acc[dt][1], A[1][dt], H[G2_HD - 1][1][1]); }
    A_MFMA_DRAIN();
    STAMP(6);
    BAR();
    STAMP(7);
    {
        float gg[2]; v2u yv[2][4], qv[2][4];
#pragma unroll
        for (int sx = 0; sx < 2; ++sx) {
            const int row = 32 * w + 16 * sx + s;
            gg[sx] = *(LAS float*)(L + G2_G + row * 4);
#pragma unroll
            for (int dt = 0; dt < 4; ++dt) {
                const int d = 16 * dt + 4 * g + q;
                yv[sx][dt] = tr_read(L + G2_Y + d * 256 + (((2 * w + sx) ^ (d & 7)) * 32) + 8 * pp);
                qv[sx][dt] = *(LAS v2u*)(L + G2_Q + row * 128 + (((2 * dt + (g >> 1)) ^ ((row >> 1) & 7)) * 16) + 8 * (g & 1));
            }
        }
        SCHED();
#pragma unroll
        for (int sx = 0; sx < 2; ++sx) {
            const int row = 32 * w + 16 * sx + s;
            const float omg = 1.f - gg[sx];
#pragma unroll
            for (int dt = 0; dt < 4; ++dt) {
                const f32x4 a = acc[dt][sx];
                const f32x2 o01 = bf_2(qv[sx][dt].x) * bf_2(yv[sx][dt].x) * gg[sx] + (f32x2){a[0], a[1]} * omg;
                const f32x2 o23 = bf_2(qv[sx][dt].y) * bf_2(yv[sx][dt].y) * gg[sx] + (f32x2){a[2], a[3]} * omg;
                *(LAS v2u*)(L + G2_Q + row * 128 + (((2 * dt + (g >> 1)) ^ ((row >> 1) & 7)) * 16) + 8 * (g & 1)) = (v2u){pk2(o01), pk2(o23)};
            }
        }
    }
    WAIT_LGKM0();
#pragma unroll
    for (int i = 0; i < 4; ++i) {
        const int row = 32 * w + 8 * i + (lane >> 3), c = lane & 7;
        const v4u val = *(LAS v4u*)(L + G2_Q + row * 128 + c * 16);
        *(GAS v4u*)((char*)p.out + (size_t)(panel * 128 + row) * (D * 2) + ct * 128 + ((c ^ ((row >> 1) & 7)) * 16)) = val;
    }
    STAMP(11);
}

__global__ void __launch_bounds__(NTHR, 2) k_fused(Params p) {
    extern __shared__ __attribute__((aligned(16))) char smem[];
    LAS char* L = (LAS char*)smem;
    const int tid = threadIdx.x, lane = tid & 63, w = __builtin_amdgcn_readfirstlane(tid >> 6);
    const int x = blockIdx.x & 7, idx = blockIdx.x >> 3, xr = x >> 1, xc = x & 1;
    Stamps sp; sp.r0 = 0; sp.r1 = 0;
    STAMP(0);
    gu32* ctl = (gu32*)(p.ws + WS_CTL);
    unsigned epoch_v = __hip_atomic_load(ctl + W_EPOCH * FL, RLX_AGENT);
#define GET_TAG() asm volatile("" : "+v"(epoch_v)); const unsigned tag = __builtin_amdgcn_readfirstlane(epoch_v) + 1u
    const __amdgpu_buffer_rsrc_t rs = __builtin_amdgcn_make_buffer_rsrc(p.ws, 0, WS_END, 0x00020000);
    if (idx < 24) {
        const int panel = 4 * xr + idx / 6, ct = 6 * xc + idx % 6;
        const int off1 = 5 * ((idx / 6 + idx % 6) & 1);
        const bool typeA = idx < 20;
        const int panel2 = 4 * xr + idx / 5, ct2 = 5 * xc + idx % 5;
        const int off2 = 12 * ((idx / 5 + idx % 5) & 1);
        if (typeA) {
            if (w < 4) {
                ConvIn cin;
                g1_compute<true>(p, L, rs, panel, ct, w, lane, cin, 0 STAMP_PASS);
                STAMP(3);
                WAIT_VM(0);
                GET_TAG();
                STAMP(4);
                BAR();
                if (tid == 0) set_flag(ctl, W_H + panel * 12 + ct, tag);
                g2_compute(p, L, rs, panel2, ct2, off2, w, lane STAMP_PASS);
            } else {
                const int lw = w - 4;
                W2Src w2s;
                w2_init(w2s, p, ct2, off2, lane);
                g1_loader<true>(p, panel, ct, off1, w2s, lw, lane STAMP_PASS);
                BAR();
                g1_loader_epilogue(L, rs, panel, ct, lw, lane);
                w2_issue(w2s, 32 + lw); w2_issue(w2s, 36 + lw);
                WAIT_VM(8);
                GET_TAG();
                BAR();
                g2_loader(p, L, rs, tag, w2s, panel2, ct2, lw, lane STAMP_PASS);
            }
        } else {
            const int bw = x * 4 + idx - 20, pr = w >> 1, hf = w & 1;
            const int ch = 448 + 6 * bw + pr, chn = pr < 2 ? ch + 4 : -1;
            ConvIn cin;
            if (w < 4) {
                g1_compute<false>(p, L, rs, panel, ct, w, lane, cin, ch STAMP_PASS);
                STAMP(3);
            } else {
                W2Src w2s;
                g1_loader<false>(p, panel, ct, off1, w2s, w - 4, lane STAMP_PASS);
                conv_load_h(p, cin, ch, hf, lane);
                { GET_TAG(); if (w == 7) poll_flags(ctl, W_KV, 64, tag, lane); }
                BAR();
                conv_load_x(cin, rs, ch, hf, lane);
                g1_loader_epilogue(L, rs, panel, ct, w - 4, lane);
            }
            WAIT_VM(0);
            GET_TAG();
            STAMP(4);
            BAR();
            if (tid == 0) set_flag(ctl, W_H + panel * 12 + ct, tag);
            conv_rounds(p, L, rs, cin, ch, chn, pr, hf, lane STAMP_PASS);
            WAIT_VM(0);
            BAR();
            if (tid == 0) set_flag(ctl, W_Y + 64 + bw, tag);
            STAMP(16);
        }
    } else {
        const int cw = x * 8 + (idx - 24), pr = w >> 1, hf = w & 1;
        const int ch = 7 * cw + pr, chn = pr < 3 ? ch + 4 : -1;
        ConvIn cin;
        conv_load_h(p, cin, ch, hf, lane);
        prepass(p, L, rs, cw, tid STAMP_PASS);
        STAMP(2);
        WAIT_VM(0);
        GET_TAG();
        BAR();
        if (tid == 0) set_flag(ctl, W_KV + cw, tag);
        if (hf == 0 && lane < 8) *(LAS unsigned*)(L + pr * CV_PAIR + CV_SYNC + 4 * lane) = 0u;
        STAMP(3);
        if (w == 1) poll_flags(ctl, W_KV, 64, tag, lane);
        WAIT_LGKM0();
        BAR();
        STAMP(4);
        conv_load_x(cin, rs, ch, hf, lane);
        conv_rounds(p, L, rs, cin, ch, chn, pr, hf, lane STAMP_PASS);
        WAIT_VM(0);
        BAR();
        if (tid == 0) set_flag(ctl, W_Y + cw, tag);
        STAMP(16);
        if (w == 7 && blockIdx.x == 255) {
#pragma unroll 1
            for (int pn = 0; pn < 3; ++pn) poll_flags(ctl, W_H + pn * 64, 64, tag, lane);
            if (lane == 0) set_flag(ctl, W_EPOCH, tag);
        }
    }
    STAMP_FLUSH();
}

__global__ void k_probe(char* ws) {
    const unsigned* d = (const unsigned*)(ws + WS_DBG);
    const unsigned ref = d[0];
    int mx = -2000000000, mi = 2000000000;
    for (int e = threadIdx.x; e < 2048; e += 64) {
        const int b = e >> 3, w = e & 7, idx = b >> 3;
        mi = min(mi, (int)(d[2 * e] - ref));
        if (idx >= 0 && idx < 32 && (w >> 2) == 0) { const unsigned r = d[2 * e + 1]; if (r) mx = max(mx, (int)(r - ref)); }
    }
    for (int o = 32; o >= 1; o >>= 1) { mx = max(mx, __shfl_xor(mx, o)); mi = min(mi, __shfl_xor(mi, o)); }
    const unsigned long long dur = (unsigned long long)(mx - mi) * 10ull;
    const unsigned long long t0 = __builtin_amdgcn_s_memrealtime();
    while (__builtin_amdgcn_s_memrealtime() - t0 < dur && __builtin_amdgcn_s_memrealtime() - t0 < 100000ull) __builtin_amdgcn_s_sleep(1);
}
extern "C" void kernel_launch(void* const* d_in, const int* in_sizes, int n_in, void* d_out, int out_size, void* d_ws, size_t ws_size, hipStream_t stream) {
    Params p;
    p.q = (const u16*)d_in[0]; p.k = (const u16*)d_in[1]; p.v = (const u16*)d_in[2]; p.shk = (const u16*)d_in[3];
    p.ssm = (const u16*)d_in[4]; p.Dv = (const u16*)d_in[5]; p.w1 = (const u16*)d_in[7]; p.w2 = (const u16*)d_in[8]; p.gw = (const u16*)d_in[9];
    p.out = (u16*)d_out; p.ws = (char*)d_ws;
    (void)hipFuncSetAttribute((const void*)k_fused, hipFuncAttributeMaxDynamicSharedMemorySize, LDS_BYTES);
    k_fused<<<256, NTHR, LDS_BYTES, stream>>>(p);
    k_probe<<<1, 64, 0, stream>>>(p.ws);
}
```

```cpp
#define OUT_AUX 16
#include <hip/hip_runtime.h>
#include <stdint.h>
#include <type_traits>

typedef unsigned short u16;
typedef unsigned v4u __attribute__((ext_vector_type(4)));
typedef unsigned v2u __attribute__((ext_vector_type(2)));
typedef float f32x4 __attribute__((ext_vector_type(4)));
typedef float f32x2 __attribute__((ext_vector_type(2)));
typedef short bf16x8 __attribute__((ext_vector_type(8)));
typedef short s16x4 __attribute__((ext_vector_type(4)));
typedef __bf16 bf16x2_t __attribute__((ext_vector_type(2)));
#define LAS __attribute__((address_space(3)))
#define GAS __attribute__((address_space(1)))
typedef GAS unsigned gu32;
typedef GAS unsigned long long gu64;
#define RLX_AGENT __ATOMIC_RELAXED, __HIP_MEMORY_SCOPE_AGENT

constexpr int S = 2048, D = 640, F = 1536;
constexpr int NTHR = 512;
constexpr unsigned WS_H = 0;
constexpr unsigned WS_KV = WS_H + S * F * 2;
constexpr unsigned WS_Y = WS_KV + S * D * 2;
constexpr unsigned WS_G = WS_Y + S * D * 2;
constexpr unsigned WS_CTL = WS_G + S * 4;
constexpr int FL = 32;
constexpr int W_EPOCH = 0, W_H = 1, W_KV = W_H + 192, W_G = W_KV + 64, W_Y = W_G + 128, W_LINES = W_Y + 96;
constexpr unsigned WS_END = WS_CTL + W_LINES * 128;
constexpr int AUX_SC1 = 16;
constexpr int H_AUX = 16;
constexpr unsigned kSpinCap = 1u << 18;

struct Params {
    const u16 *q, *k, *v, *shk, *ssm, *Dv, *w1, *w2, *gw;
    u16* out;
    char* ws;
};

__device__ __forceinline__ float bf_lo(unsigned u) { return __uint_as_float(u << 16); }
__device__ __forceinline__ float bf_hi(unsigned u) { return __uint_as_float(u & 0xffff0000u); }
__device__ __forceinline__ f32x2 bf_2(unsigned u) { return (f32x2){bf_lo(u), bf_hi(u)}; }
__device__ __forceinline__ unsigned pk2(f32x2 v) { return __builtin_bit_cast(unsigned, __builtin_convertvector(v, bf16x2_t)); }
__device__ __forceinline__ unsigned pk_bf16(float lo, float hi) { return pk2((f32x2){lo, hi}); }
__device__ __forceinline__ float rbf(float x) { return bf_lo(pk_bf16(x, x)); }
__device__ __forceinline__ f32x4 mfma16(v4u a, v4u b, f32x4 c) { return __builtin_amdgcn_mfma_f32_16x16x32_bf16(__builtin_bit_cast(bf16x8, a), __builtin_bit_cast(bf16x8, b), c, 0, 0, 0); }
__device__ __forceinline__ v2u tr_read(LAS char* p) { return __builtin_bit_cast(v2u, __builtin_amdgcn_ds_read_tr16_b64_v4i16((LAS s16x4*)p)); }
__device__ __forceinline__ v4u lds_rd16(LAS char* p) { return *(LAS v4u*)p; }
__device__ __forceinline__ void glds16(unsigned voff, const void* sbase, unsigned lds_dst) {
    asm volatile("s_mov_b32 m0, %2\n\ts_nop 0\n\tglobal_load_lds_dwordx4 %0, %1" :: "v"(voff), "s"(sbase), "s"(lds_dst) : "memory");
}
template <bool NT> __device__ __forceinline__ void glds16t(unsigned voff, const void* sbase, unsigned lds_dst) {
    if (NT) asm volatile("s_mov_b32 m0, %2\n\ts_nop 0\n\tglobal_load_lds_dwordx4 %0, %1 nt" :: "v"(voff), "s"(sbase), "s"(lds_dst) : "memory");
    else glds16(voff, sbase, lds_dst);
}
#define NT_X 0
#define NT_W1 0
#define NT_W2 0
#define NT_KV 0
#define NT_SSM 0
template <bool NT> __device__ __forceinline__ v4u gld16(const void* ptr) {
    if (NT) return __builtin_nontemporal_load((const GAS v4u*)ptr);
    return *(const GAS v4u*)ptr;
}
#define WAIT_VM(n) asm volatile("s_waitcnt vmcnt(%0)" :: "n"(n) : "memory")
#define WAIT_LGKM0() asm volatile("s_waitcnt lgkmcnt(0)" ::: "memory")
#define BAR() do { asm volatile("" ::: "memory"); __builtin_amdgcn_s_barrier(); asm volatile("" ::: "memory"); } while (0)
#define SCHED() __builtin_amdgcn_sched_barrier(0)
#define A_MFMA(acc, a, b) asm volatile("v_mfma_f32_16x16x32_bf16 %0, %1, %2, %0" : "+v"(acc) : "v"(a), "v"(b))
#define A_RD128(dst, addr, off) asm volatile("ds_read_b128 %0, %1 offset:%2" : "=v"(dst) : "v"(addr), "n"(off))
#define A_RDTR(dst, addr, off) asm volatile("ds_read_b64_tr_b16 %0, %1 offset:%2" : "=v"(dst) : "v"(addr), "n"(off))
#define A_LGKM(n) asm volatile("s_waitcnt lgkmcnt(%0)" :: "n"(n))
#define A_MFMA_DRAIN() asm volatile("s_nop 15\n\ts_nop 7" ::: "memory")
template <int NM, int NR, int PER> __device__ __forceinline__ void interleave() {
#pragma unroll
    for (int i = 0; i < NM; ++i) {
        __builtin_amdgcn_sched_group_barrier(0x8, 1, 0);
        if (i * PER < NR) __builtin_amdgcn_sched_group_barrier(0x100, PER, 0);
    }
}
#define STAMP(id) do {} while (0)
#define STAMP_ARG
#define STAMP_PASS
#define STAMP_FLUSH() do {} while (0)

__device__ __forceinline__ void st16_sc1(v4u v, __amdgpu_buffer_rsrc_t rs, unsigned off) { __builtin_amdgcn_raw_buffer_store_b128(v, rs, off, 0, AUX_SC1); }
__device__ __forceinline__ void poll_flags(gu32* ctl, int line, int n, unsigned tag, int lane) {
    gu32* f = ctl + (line + (lane < n ? lane : 0)) * FL;
#pragma unroll 1
    for (unsigned spins = 0; spins < kSpinCap; ++spins) {
        const unsigned v = __hip_atomic_load(f, RLX_AGENT);
        if (__all(v == tag)) break;
        __builtin_amdgcn_s_sleep(4);
    }
    __builtin_amdgcn_fence(__ATOMIC_ACQUIRE, "wavefront");
}
__device__ __forceinline__ void set_flag(gu32* ctl, int line, unsigned tag) { __hip_atomic_store(ctl + line * FL, tag, RLX_AGENT); }
__device__ __forceinline__ unsigned silu2f(f32x2 h) {
    const f32x2 t = h * -1.44269504f;
    const f32x2 den = (f32x2){__builtin_amdgcn_exp2f(t.x), __builtin_amdgcn_exp2f(t.y)} + 1.f;
    const f32x2 r = (f32x2){__builtin_amdgcn_rcpf(den.x), __builtin_amdgcn_rcpf(den.y)};
    return pk2(h * r);
}
__device__ __forceinline__ unsigned silu2(unsigned hb) { return silu2f(bf_2(hb)); }
__device__ __forceinline__ float row_sum16(float v) {
    v += __builtin_bit_cast(float, __builtin_amdgcn_update_dpp(0, __builtin_bit_cast(int, v), 0x128, 0xf, 0xf, false));
    v += __builtin_bit_cast(float, __builtin_amdgcn_update_dpp(0, __builtin_bit_cast(int, v), 0x124, 0xf, 0xf, false));
    v += __builtin_bit_cast(float, __builtin_amdgcn_update_dpp(0, __builtin_bit_cast(int, v), 0x122, 0xf, 0xf, false));
    v += __builtin_bit_cast(float, __builtin_amdgcn_update_dpp(0, __builtin_bit_cast(int, v), 0x121, 0xf, 0xf, false));
    return v;
}

constexpr int G1_SLOT = 32768, G1_NS = 5, G1_NST = D / 64, G1_STG = 3 * G1_SLOT;
constexpr int G2_NST = F / 64, G2_RING = 40 * 4096, G2_HD = 8;
constexpr int G2_Q = 8 * 4096, G2_Y = 12 * 4096, G2_G = 16 * 4096;
constexpr int CV_COPY = 4160, CV_X = 4 * CV_COPY, CV_PAIR = CV_X + 4608;
constexpr int CV_EX = 4 * CV_PAIR;
constexpr int LDS_BYTES = 163840;
static_assert(CV_EX + 4 * 8192 <= LDS_BYTES, "LDS map");

struct W2Src { unsigned vw[4]; const char* base; int kb0; unsigned aw[4]; };
__device__ __forceinline__ void w2_init(W2Src& s, const Params& p, int ct, int off2, int lane) {
#pragma unroll
    for (int i = 0; i < 4; ++i) {
        const int r = lane >> 3, c = lane & 7, k = 8 * i + r;
        s.vw[i] = (unsigned)(k * (D * 2) + ((c ^ (((k >> 1) & 3) << 1)) * 16));
    }
    const int g = lane >> 4, q = (lane & 15) >> 2, pp = lane & 3, sw = ((g & 1) << 1) | (q >> 1);
#pragma unroll
    for (int dt = 0; dt < 4; ++dt) s.aw[dt] = (4 * g + q) * 128 + ((dt ^ sw) * 32) + 8 * pp;
    s.base = (const char*)p.w2 + ct * 128; s.kb0 = 2 * off2;
}
__device__ __forceinline__ void w2_issue(const W2Src& s, int b) {
    const int kb = s.kb0 + b >= 48 ? s.kb0 + b - 48 : s.kb0 + b;
    const char* src = s.base + (size_t)kb * (32 * D * 2);
    const unsigned slot = (unsigned)(b >= 40 ? b - 40 : b) * 4096;
#pragma unroll
    for (int i = 0; i < 4; ++i) glds16t<NT_W2>(s.vw[i], src, slot + i * 1024);
}
__device__ __forceinline__ void w2_repack(const W2Src& s, LAS char* L, int b, int lane) {
    LAS char* blk = L + (b >= 40 ? b - 40 : b) * 4096;
    v4u f[4];
#pragma unroll
    for (int dt = 0; dt < 4; ++dt) { const v2u lo = tr_read(blk + s.aw[dt]), hi = tr_read(blk + s.aw[dt] + 2048); f[dt] = (v4u){lo.x, lo.y, hi.x, hi.y}; }
    WAIT_LGKM0();
#pragma unroll
    for (int dt = 0; dt < 4; ++dt) *(LAS v4u*)(blk + dt * 1024 + lane * 16) = f[dt];
    WAIT_LGKM0();
}

template <bool TYPE_A>
__device__ __forceinline__ void g1_loader(const Params& p, int panel, int ct, int off1, const W2Src& w2s, int lw, int lane STAMP_ARG) {
    unsigned vx[4], vw[4];
#pragma unroll
    for (int i = 0; i < 4; ++i) {
        const int r = lane >> 3, c = lane & 7, row = 32 * lw + 8 * i + r;
        vx[i] = (unsigned)(row * (D * 2) + ((c ^ ((row >> 1) & 7)) * 16));
        const int r2 = lane >> 4, c2 = lane & 15, k = 16 * lw + 4 * i + r2, phi = (k & 3) | (((k >> 3) & 1) << 2);
        vw[i] = (unsigned)(k * (F * 2) + ((c2 ^ (phi << 1)) * 16));
    }
    const char* xb = (const char*)p.q + (size_t)panel * 128 * D * 2;
    const char* wb = (const char*)p.w1 + (size_t)ct * 256;
    int kst = off1;
    auto issue = [&](int slot_i) {
        const unsigned slot = (unsigned)slot_i * G1_SLOT;
        const char* xs = xb + kst * 128; const char* wsrc = wb + (size_t)kst * (64 * F * 2);
#pragma unroll
        for (int i = 0; i < 4; ++i) glds16t<NT_X>(vx[i], xs, slot + (32 * lw + 8 * i) * 128);
#pragma unroll
        for (int i = 0; i < 4; ++i) glds16t<NT_W1>(vw[i], wsrc, slot + 16384 + (16 * lw + 4 * i) * 256);
        kst = kst + 1 == G1_NST ? 0 : kst + 1;
    };
#pragma unroll
    for (int st = 0; st < 3; ++st) issue(st);
    WAIT_VM(16); BAR(); issue(3); issue(4);
#pragma unroll 1
    for (int i = 1; i < 6; ++i) { WAIT_VM(24); BAR(); issue(i - 1); }
    STAMP(1);
    if (TYPE_A) {
        WAIT_VM(24); BAR(); w2_issue(w2s, 0 + lw); w2_issue(w2s, 4 + lw);
        WAIT_VM(24); BAR(); w2_issue(w2s, 8 + lw); w2_issue(w2s, 12 + lw);
        WAIT_VM(24); BAR(); w2_issue(w2s, 16 + lw); w2_issue(w2s, 20 + lw);
        WAIT_VM(24); BAR(); w2_issue(w2s, 28 + lw);
    } else {
        WAIT_VM(24); BAR(); WAIT_VM(16); BAR(); WAIT_VM(8); BAR(); WAIT_VM(0); BAR();
    }
}
__device__ __forceinline__ void g1_store(__amdgpu_buffer_rsrc_t rs, int stg, int ksg, int lane, v4u hb) {
    const v4u o = {silu2(hb.x), silu2(hb.y), silu2(hb.z), silu2(hb.w)};
    st16_sc1(o, rs, WS_H + (stg * 48 + ksg) * 1024 + lane * 16);
}
struct ConvIn { v4u v[2], vp[2], x[2]; unsigned vq[2]; float Dd; };
__device__ __forceinline__ void conv_load_h(const Params& p, ConvIn& c, int ch, int hf, int lane) {
    const char* hsrc = (const char*)p.ssm + (size_t)ch * (S * 2);
#pragma unroll
    for (int j = 0; j < 2; ++j) {
        const int o = (2 * hf + j) * 1024 + lane * 16;
        c.v[j] = gld16<NT_SSM>(hsrc + o);
        c.vp[j] = gld16<NT_SSM>(hsrc + (o < 4 ? 0 : o - 4));
        c.vq[j] = *(const GAS unsigned*)(hsrc + (o < 8 ? 0 : o - 8));
    }
    c.Dd = bf_lo((unsigned)p.Dv[ch]);
}
__device__ __forceinline__ void conv_load_x(ConvIn& c, __amdgpu_buffer_rsrc_t rs, int ch, int hf, int lane) {
#pragma unroll
    for (int j = 0; j < 2; ++j) c.x[j] = __builtin_amdgcn_raw_buffer_load_b128(rs, (2 * hf + j) * 1024 + lane * 16, WS_KV + ch * (S * 2), AUX_SC1);
}
constexpr int CV_SYNC = 4128;
template <bool TYPE_A>
__device__ __forceinline__ void g1_compute(const Params& p, LAS char* L, __amdgpu_buffer_rsrc_t rs, int panel, int ct, int w, int lane, ConvIn& cin, int ch STAMP_ARG) {
    const int wf = w & 1, wsx = w >> 1;
    const int g = lane >> 4, q = (lane & 15) >> 2, pp = lane & 3, s = lane & 15;
    const int phi = q | ((g & 1) << 2);
    unsigned aw[4], ax[2];
#pragma unroll
    for (int ft = 0; ft < 4; ++ft) aw[ft] = 16384 + (8 * g + q) * 256 + (((4 * wf + ft) ^ phi) * 32) + 8 * pp;
#pragma unroll
    for (int ks = 0; ks < 2; ++ks) ax[ks] = (64 * wsx + s) * 128 + (((4 * ks + g) ^ ((s >> 1) & 7)) * 16);
    f32x4 acc[4][4];
#pragma unroll
    for (int a = 0; a < 4; ++a)
#pragma unroll
        for (int b = 0; b < 4; ++b) acc[a][b] = (f32x4){0.f, 0.f, 0.f, 0.f};
    v4u A[2][4], B[2][4];
#pragma unroll
    for (int a = 0; a < 4; ++a) { A[1][a] = (v4u){0u, 0u, 0u, 0u}; B[1][a] = (v4u){0u, 0u, 0u, 0u}; }
    auto rd = [&](int set, LAS char* base, int ks) {
#pragma unroll
        for (int ft = 0; ft < 4; ++ft) {
            const v2u lo = tr_read(base + aw[ft] + ks * 8192), hi = tr_read(base + aw[ft] + ks * 8192 + 1024);
            A[set][ft] = (v4u){lo.x, lo.y, hi.x, hi.y};
            if (ft == 0) {
#pragma unroll
                for (int sx = 0; sx < 4; ++sx) B[set][sx] = lds_rd16(base + ax[ks] + sx * 2048);
            }
        }
    };
    auto mm = [&](int set) {
#pragma unroll
        for (int ft = 0; ft < 4; ++ft)
#pragma unroll
            for (int sx = 0; sx < 4; ++sx) acc[ft][sx] = mfma16(A[set][ft], B[set][sx], acc[ft][sx]);
    };
    unsigned slot = 0;
#define X_G1REP 1
#pragma unroll 1
    for (int i = 0; i < G1_NST * X_G1REP; ++i) {
        LAS char* base = L + slot;
        slot = slot + G1_SLOT == G1_NS * G1_SLOT ? 0 : slot + G1_SLOT;
        BAR();
        if (i == 0) STAMP(1);
        rd(0, base, 0);
        mm(1);
        interleave<16, 12, 1>();
        SCHED();
        rd(1, base, 1);
        mm(0);
        interleave<16, 12, 1>();
        SCHED();
        WAIT_LGKM0();
    }
    mm(1);
    STAMP(2);
    if (!TYPE_A) conv_load_h(p, cin, ch, w & 1, lane);
    SCHED();
#pragma unroll
    for (int j = 0; j < 4; ++j) {
        const f32x4 a = acc[2 * (j & 1)][2 + (j >> 1)], b = acc[2 * (j & 1) + 1][2 + (j >> 1)];
        *(LAS v4u*)(L + G1_STG + w * 4096 + j * 1024 + lane * 16) = (v4u){pk_bf16(a[0], a[1]), pk_bf16(a[2], a[3]), pk_bf16(b[0], b[1]), pk_bf16(b[2], b[3])};
    }
    if (!TYPE_A && lane < 8) *(LAS unsigned*)(L + w * CV_PAIR + CV_SYNC + 4 * lane) = 0u;
    WAIT_LGKM0();
    BAR();
    if (!TYPE_A) conv_load_x(cin, rs, ch, w & 1, lane);
    SCHED();
#pragma unroll
    for (int j = 0; j < 4; ++j) {
        const f32x4 a = acc[2 * (j & 1)][j >> 1], b = acc[2 * (j & 1) + 1][j >> 1];
        const v4u o = {silu2f((f32x2){a[0], a[1]}), silu2f((f32x2){a[2], a[3]}), silu2f((f32x2){b[0], b[1]}), silu2f((f32x2){b[2], b[3]})};
        st16_sc1(o, rs, WS_H + ((panel * 8 + wsx * 4 + (j >> 1)) * 48 + ct * 4 + wf * 2 + (j & 1)) * 1024 + lane * 16);
    }
}
__device__ __forceinline__ void g1_loader_epilogue(LAS char* L, __amdgpu_buffer_rsrc_t rs, int panel, int ct, int lw, int lane) {
    const int wf = lw & 1, wsx = lw >> 1;
    v4u hb[4];
#pragma unroll
    for (int j = 0; j < 4; ++j) hb[j] = *(LAS v4u*)(L + G1_STG + lw * 4096 + j * 1024 + lane * 16);
#pragma unroll
    for (int j = 0; j < 4; ++j) g1_store(rs, panel * 8 + wsx * 4 + 2 + (j >> 1), ct * 4 + wf * 2 + (j & 1), lane, hb[j]);
}

__device__ __forceinline__ void prepass(const Params& p, LAS char* L, __amdgpu_buffer_rsrc_t rs, int wg, int tid STAMP_ARG) {
    const int s0 = 64 * (wg >> 1), c0 = 320 * (wg & 1);
    {
        const int w = __builtin_amdgcn_readfirstlane(tid >> 6);
        const int item = w < 4 ? tid : 256 + (tid & 63);
        const int cc = item % 40, rg = item / 40;
        const int ch = c0 + 8 * cc, r0 = s0 + 8 * rg;
        v4u kk[11], vv[8], sw[4];
#pragma unroll
        for (int j = 0; j < 4; ++j) sw[j] = *(const GAS v4u*)((const char*)p.shk + (size_t)ch * 8 + j * 16);
#pragma unroll
        for (int j = 0; j < 11; ++j) {
            const int row = r0 - 3 + j;
            kk[j] = gld16<NT_KV>((const char*)p.k + (size_t)(row < 0 ? 0 : row) * (D * 2) + ch * 2);
        }
#pragma unroll
        for (int j = 0; j < 8; ++j) vv[j] = gld16<NT_KV>((const char*)p.v + (size_t)(r0 + j) * (D * 2) + ch * 2);
        if (r0 == 0) { kk[0] = (v4u){0u, 0u, 0u, 0u}; kk[1] = kk[0]; kk[2] = kk[0]; }
#pragma unroll
        for (int e = 0; e < 4; ++e) {
            if (w >= 4 && w - 4 != e) continue;
            const f32x2 W0 = {bf_lo(sw[e].x), bf_lo(sw[e].z)}, W1 = {bf_hi(sw[e].x), bf_hi(sw[e].z)};
            const f32x2 W2 = {bf_lo(sw[e].y), bf_lo(sw[e].w)}, W3 = {bf_hi(sw[e].y), bf_hi(sw[e].w)};
            f32x2 kf[11], o[8];
#pragma unroll
            for (int j = 0; j < 11; ++j) kf[j] = bf_2(kk[j][e]);
#pragma unroll
            for (int j = 0; j < 8; ++j) {
                const f32x2 ksh = kf[j] * W3 + kf[j + 1] * W2 + kf[j + 2] * W1 + kf[j + 3] * W0;
                o[j] = ksh * bf_2(vv[j][e]);
            }
            const v4u p0 = {pk_bf16(o[0].x, o[1].x), pk_bf16(o[2].x, o[3].x), pk_bf16(o[4].x, o[5].x), pk_bf16(o[6].x, o[7].x)};
            const v4u p1 = {pk_bf16(o[0].y, o[1].y), pk_bf16(o[2].y, o[3].y), pk_bf16(o[4].y, o[5].y), pk_bf16(o[6].y, o[7].y)};
            *(LAS v4u*)(L + (8 * cc + 2 * e) * 128 + ((rg ^ (cc & 7)) * 16)) = p0;
            *(LAS v4u*)(L + (8 * cc + 2 * e + 1) * 128 + ((rg ^ (cc & 7)) * 16)) = p1;
        }
    }
    STAMP(1);
    WAIT_LGKM0();
    BAR();
#pragma unroll
    for (int j = 0; j < 5; ++j) {
        const int n = tid + NTHR * j, ch = n >> 3, pc = n & 7;
        const v4u val = *(LAS v4u*)(L + ch * 128 + ((pc ^ ((ch >> 3) & 7)) * 16));
        st16_sc1(val, rs, WS_KV + (c0 + ch) * (S * 2) + s0 * 2 + pc * 16);
    }
}
struct GateIn { v4u gw[5], q[5]; };
__device__ __forceinline__ void gate_load(const Params& p, GateIn& gi, int row0, int lane) {
    const int i = lane & 15, row = row0 + (lane >> 4);
#pragma unroll
    for (int j = 0; j < 5; ++j) {
        gi.gw[j] = *(const GAS v4u*)((const char*)p.gw + (i + 16 * j) * 16);
        gi.q[j] = *(const GAS v4u*)((const char*)p.q + (size_t)row * (D * 2) + (i + 16 * j) * 16);
    }
}
__device__ __forceinline__ void gate_finish(const GateIn& gi, __amdgpu_buffer_rsrc_t rs, int row0, int lane) {
    f32x2 a2 = {0.f, 0.f};
#pragma unroll
    for (int j = 0; j < 5; ++j)
#pragma unroll
        for (int e = 0; e < 4; ++e) a2 += bf_2(gi.q[j][e]) * bf_2(gi.gw[j][e]);
    const float z = rbf(row_sum16(a2.x + a2.y));
    const float gv = rbf(__builtin_amdgcn_rcpf(1.f + __builtin_amdgcn_exp2f(z * -1.44269504f)));
    if ((lane & 15) == 0) __builtin_amdgcn_raw_buffer_store_b32(__float_as_uint(gv), rs, WS_G + (row0 + (lane >> 4)) * 4, 0, AUX_SC1);
}

__device__ __forceinline__ void pair_sync(LAS char* words, int hf) {
    WAIT_LGKM0();
    *(volatile LAS unsigned*)(words + 4 * hf) = 1u;
#pragma unroll 1
    for (unsigned spins = 0; spins < (1u << 22); ++spins) { if (__builtin_amdgcn_readfirstlane(*(volatile LAS unsigned*)(words + 4 * (1 - hf))) != 0u) break; }
    asm volatile("" ::: "memory");
}
template <int hf>
__device__ __forceinline__ void conv_half(const Params& p, LAS char* Lw, LAS char* Lx, __amdgpu_buffer_rsrc_t rs, ConvIn& c, int ch, int chn, int round, int lane STAMP_ARG) {
    const int g = lane >> 4, q = (lane & 15) >> 2, pp = lane & 3, n = lane & 15;
    if (hf == 0) {
        if (lane < 30) *(LAS v4u*)(Lw + CV_X + lane * 16) = (v4u){0u, 0u, 0u, 0u};
        if (lane < 8) *(LAS v4u*)(Lw + (lane >> 1) * CV_COPY + (lane & 1) * 16) = (v4u){0u, 0u, 0u, 0u};
        if (lane == 0) { c.vp[0] = (v4u){0u, c.v[0].x, c.v[0].y, c.v[0].z}; c.vq[0] = 0u; }
    }
#pragma unroll
    for (int j = 0; j < 2; ++j) {
        const v4u v = c.v[j], vp = c.vp[j];
        const unsigned a0 = __builtin_amdgcn_alignbit(v.x, vp.x, 16), a1 = __builtin_amdgcn_alignbit(v.y, v.x, 16), a2 = __builtin_amdgcn_alignbit(v.z, v.y, 16), a3 = __builtin_amdgcn_alignbit(v.w, v.z, 16);
        const unsigned am = __builtin_amdgcn_alignbit(vp.x, c.vq[j], 16);
        LAS char* d = Lw + 32 + (2 * hf + j) * 1024 + lane * 16;
        *(LAS v4u*)(d) = v;
        *(LAS v4u*)(d + CV_COPY) = (v4u){a0, a1, a2, a3};
        *(LAS v4u*)(d + 2 * CV_COPY) = vp;
        *(LAS v4u*)(d + 3 * CV_COPY) = (v4u){am, a0, a1, a2};
        *(LAS v4u*)(Lw + CV_X + 480 + (2 * hf + j) * 1024 + lane * 16) = c.x[j];
    }
    const float Dd = c.Dd;
    pair_sync(Lw + CV_SYNC + 16 * round, hf);
    if (chn >= 0) { conv_load_h(p, c, chn, hf, lane); conv_load_x(c, rs, chn, hf, lane); asm volatile("" ::: "memory"); }
    if (round == 0) STAMP(8); else STAMP(12);
    const unsigned la = (unsigned)(size_t)Lw + q * CV_COPY + 32 - 16 * g + 8 * pp + 24 + 256 * hf;
    const unsigned lb = (unsigned)(size_t)Lw + CV_X + 32 * n + 16 * g + 448 - 256 * hf - 192;
    f32x4 acc[8];
#pragma unroll
    for (int t = 0; t < 8; ++t) acc[t] = (f32x4){0.f, 0.f, 0.f, 0.f};
    v2u Alo[2][8], Ahi[2][8];
    v4u B[2][8];
#define CV_RD(set, rs_, k) do { \
        if ((k) % 3 == 0) A_RDTR(Alo[set][(k) / 3], la, 64 * (rs_) + 512 * ((k) / 3) + 8); \
        else if ((k) % 3 == 1) A_RDTR(Ahi[set][(k) / 3], la, 64 * (rs_) + 512 * ((k) / 3)); \
        else A_RD128(B[set][(k) / 3], lb, 192 - 64 * (rs_) + 512 * ((k) / 3)); } while (0)
    auto step = [&](auto SET, auto NEXT) {
        constexpr int set = decltype(SET)::value, nx = decltype(NEXT)::value;
        int k = 0;
#pragma unroll
        for (int l = 0; l < 8; ++l)
#pragma unroll
            for (int i = 0; i + l < 8; ++i) {
                const v4u a = {Alo[set][i].x, Alo[set][i].y, Ahi[set][i].x, Ahi[set][i].y};
                A_MFMA(acc[i + l], a, B[set][l]);
                if (nx >= 0 && k < 24) { CV_RD(1 - set, (nx >= 0 ? nx : 0), k); }
                ++k;
            }
        if (nx >= 0) A_LGKM(0);
    };
#pragma unroll
    for (int k = 0; k < 24; ++k) CV_RD(0, 0, k);
    A_LGKM(0);
    step(std::integral_constant<int, 0>{}, std::integral_constant<int, 1>{});
    step(std::integral_constant<int, 1>{}, std::integral_constant<int, 2>{});
    step(std::integral_constant<int, 0>{}, std::integral_constant<int, 3>{});
    step(std::integral_constant<int, 1>{}, std::integral_constant<int, -1>{});
#undef CV_RD
    A_MFMA_DRAIN();
    if (round == 0) STAMP(9); else STAMP(13);
#pragma unroll
    for (int t = 0; t < 4; ++t) *(LAS f32x4*)(Lx + hf * 4096 + t * 1024 + lane * 16) = acc[4 * (1 - hf) + t];
    v2u xv[4];
#pragma unroll
    for (int t = 0; t < 4; ++t) xv[t] = *(LAS v2u*)(Lw + CV_X + 480 + 2 * (1024 * hf + 256 * t + 16 * n + 4 * g));
    pair_sync(Lw + CV_SYNC + 16 * round + 8, hf);
    f32x4 o[4];
#pragma unroll
    for (int t = 0; t < 4; ++t) o[t] = *(LAS f32x4*)(Lx + (1 - hf) * 4096 + t * 1024 + lane * 16);
    v2u ov[4];
#pragma unroll
    for (int t = 0; t < 4; ++t) {
        const f32x4 y = acc[4 * hf + t] + o[t];
        ov[t] = (v2u){pk2(bf_2(xv[t].x) * Dd + (f32x2){y[0], y[1]}), pk2(bf_2(xv[t].y) * Dd + (f32x2){y[2], y[3]})};
    }
    LAS char* Ly = Lx + (1 - hf) * 4096;
#pragma unroll
    for (int t = 0; t < 4; ++t) *(LAS v2u*)(Ly + 2 * (256 * t + 16 * n + 4 * g)) = ov[t];
    WAIT_LGKM0();
#pragma unroll
    for (int j = 0; j < 2; ++j) {
        const v4u val = *(LAS v4u*)(Ly + j * 1024 + lane * 16);
        st16_sc1(val, rs, WS_Y + ch * (S * 2) + hf * 2048 + j * 1024 + lane * 16);
    }
    if (round == 0) STAMP(10); else STAMP(14);
}

__device__ __forceinline__ void conv_rounds(const Params& p, LAS char* L, __amdgpu_buffer_rsrc_t rs, ConvIn& c, int ch, int chn, int pr, int hf, int lane STAMP_ARG) {
    LAS char* Lw = L + pr * CV_PAIR; LAS char* Lx = L + CV_EX + pr * 8192;
#pragma unroll 1
    for (int round = 0; round < 2; ++round) {
        if (hf) conv_half<1>(p, Lw, Lx, rs, c, ch, chn, round, lane STAMP_PASS); else conv_half<0>(p, Lw, Lx, rs, c, ch, chn, round, lane STAMP_PASS);
        if (chn < 0) break;
        ch = chn; chn = -1;
    }
}

__device__ __forceinline__ void g2_loader(const Params& p, LAS char* L, __amdgpu_buffer_rsrc_t rs, unsigned tag, const W2Src& w2s, int panel, int ct, int lw, int lane STAMP_ARG) {
    gu32* ctl = (gu32*)(p.ws + WS_CTL);
    const int grow = panel * 128 + (ct & 7) * 16 + 4 * lw;
    if (lw == 0) poll_flags(ctl, W_H + panel * 12, 12, tag, lane);
    w2_issue(w2s, 24 + lw);
    GateIn gi;
    gate_load(p, gi, grow, lane);
    asm volatile("" ::: "memory");
    w2_repack(w2s, L, 0 + lw, lane); w2_repack(w2s, L, 4 + lw, lane);
    STAMP(5);
    BAR();
    w2_repack(w2s, L, 8 + lw, lane); w2_repack(w2s, L, 12 + lw, lane);
    if (ct < 8) gate_finish(gi, rs, grow, lane);
    else WAIT_VM(0);
    BAR();
    w2_issue(w2s, 40 + lw); w2_issue(w2s, 44 + lw);
    w2_repack(w2s, L, 16 + lw, lane); w2_repack(w2s, L, 20 + lw, lane);
    WAIT_VM(8);
    BAR();
    if (ct < 8 && lw == 0 && lane == 0) set_flag(ctl, W_G + panel * 8 + ct, tag);
#pragma unroll
    for (int i = 0; i < 4; ++i) {
        const int r = lane >> 3, c = lane & 7, row = 32 * lw + 8 * i + r;
        glds16((unsigned)(row * (D * 2) + ((c ^ ((row >> 1) & 7)) * 16)), (const char*)p.q + (size_t)panel * 128 * D * 2 + ct * 128, G2_Q + (32 * lw + 8 * i) * 128);
    }
#pragma unroll 1
    for (int b = 24; b < 40; b += 4) w2_repack(w2s, L, b + lw, lane);
    BAR();
    WAIT_VM(4);
    w2_repack(w2s, L, 40 + lw, lane); w2_repack(w2s, L, 44 + lw, lane);
    BAR();
    const int y0 = ct < 7 ? (64 * ct) / 7 : 64 + (64 * ct - 448) / 6, y1 = ct < 7 ? (64 * ct + 63) / 7 : 64 + (64 * ct + 63 - 448) / 6;
    poll_flags(ctl, W_Y + y0, y1 - y0 + 1, tag, lane);
    poll_flags(ctl, W_G + panel * 8, 8, tag, lane);
    STAMP(6);
    v4u yv[4];
#pragma unroll
    for (int i = 0; i < 4; ++i) {
        const int r = lane >> 4, c = lane & 15, d = 16 * lw + 4 * i + r;
        yv[i] = __builtin_amdgcn_raw_buffer_load_b128(rs, d * (S * 2) + c * 16, WS_Y + ct * 64 * S * 2 + panel * 256, AUX_SC1);
    }
    const unsigned gv = __builtin_amdgcn_raw_buffer_load_b32(rs, (lw * 32 + (lane & 31)) * 4, WS_G + panel * 512, AUX_SC1);
#pragma unroll
    for (int i = 0; i < 4; ++i) {
        const int r = lane >> 4, c = lane & 15, d = 16 * lw + 4 * i + r;
        *(LAS v4u*)(L + G2_Y + d * 256 + ((c ^ ((d & 7) << 1)) * 16)) = yv[i];
    }
    if (lane < 32) *(LAS unsigned*)(L + G2_G + (lw * 32 + lane) * 4) = gv;
    WAIT_VM(0);
    WAIT_LGKM0();
    BAR();
}
__device__ __forceinline__ void g2_compute(const Params& p, LAS char* L, __amdgpu_buffer_rsrc_t rs, int panel, int ct, int off2, int w, int lane STAMP_ARG) {
    const int g = lane >> 4, q = (lane & 15) >> 2, pp = lane & 3, s = lane & 15;
    const unsigned hbase = WS_H + (panel * 8 + 2 * w) * (48 * 1024);
    int kld = off2;
    f32x4 acc[4][2];
#pragma unroll
    for (int a = 0; a < 4; ++a) { acc[a][0] = (f32x4){0.f, 0.f, 0.f, 0.f}; acc[a][1] = (f32x4){0.f, 0.f, 0.f, 0.f}; }
    v4u H[G2_HD][2][2];
    auto ldh = [&](int slot) {
#pragma unroll
        for (int ks = 0; ks < 2; ++ks)
#pragma unroll
            for (int sx = 0; sx < 2; ++sx) H[slot][ks][sx] = __builtin_amdgcn_raw_buffer_load_b128(rs, lane * 16 + sx * (48 * 1024) + ks * 1024, hbase + kld * 2048, H_AUX);
        kld = kld + 1 == G2_NST ? 0 : kld + 1;
    };
    BAR();
    STAMP(5);
#pragma unroll
    for (int st = 0; st < G2_HD - 1; ++st) ldh(st);
#pragma unroll
    for (int a = 0; a < 4; ++a) H[G2_HD - 1][a >> 1][a & 1] = (v4u){0u, 0u, 0u, 0u};
    SCHED();
    v4u A[2][4];
#pragma unroll
    for (int a = 0; a < 4; ++a) A[1][a] = (v4u){0u, 0u, 0u, 0u};
    auto half = [&](int set, int slot, int ks, unsigned addr, int off) {
        A_LGKM(3);
        A_MFMA(acc[0][0], A[set][0], H[slot][ks][0]); A_RD128(A[1 - set][0], addr, off);
        A_MFMA(acc[0][1], A[set][0], H[slot][ks][1]); A_RD128(A[1 - set][1], addr, off + 1024);
        A_LGKM(4);
        A_MFMA(acc[1][0], A[set][1], H[slot][ks][0]); A_RD128(A[1 - set][2], addr, off + 2048);
        A_MFMA(acc[1][1], A[set][1], H[slot][ks][1]); A_RD128(A[1 - set][3], addr, off + 3072);
        A_LGKM(5);
        A_MFMA(acc[2][0], A[set][2], H[slot][ks][0]);
        A_MFMA(acc[2][1], A[set][2], H[slot][ks][1]);
        A_LGKM(4);
        A_MFMA(acc[3][0], A[set][3], H[slot][ks][0]);
        A_MFMA(acc[3][1], A[set][3], H[slot][ks][1]);
    };
    const unsigned lbase = (unsigned)(size_t)L + lane * 16;
    unsigned sb = 0;
    auto stage = [&](int u, bool load, bool drain) {
        const unsigned addr = lbase + sb;
        half(1, (u + G2_HD - 1) % G2_HD, 1, addr, 0);
        if (load) { ldh((u + G2_HD - 1) % G2_HD); asm volatile("" ::: "memory"); }
        half(0, u, 0, addr, 4096);
        if (drain) WAIT_LGKM0();
        sb = sb + 8192 == G2_RING ? 0 : sb + 8192;
    };
#pragma unroll 1
    for (int jj = 0; jj < 2; ++jj) {
#pragma unroll
        for (int u = 0; u < 8; ++u) {
            if (u == 4 || (u == 0 && jj == 1)) BAR();
            if (u == 0) { if (jj == 0) STAMP(14); else STAMP(12); }
            stage(u, true, (u & 3) == 3);
        }
    }
    BAR();
    STAMP(13);
#pragma unroll
    for (int u = 0; u < 8; ++u) stage(u, u == 0, false);
    WAIT_LGKM0();
#pragma unroll
    for (int dt = 0; dt < 4; ++dt) { A_MFMA(acc[dt][0], A[1][dt], H[G2_HD - 1][1][0]); A_MFMA(acc[dt][1], A[1][dt], H[G2_HD - 1][1][1]); }
    A_MFMA_DRAIN();
    STAMP(6);
    BAR();
    STAMP(7);
    {
        float gg[2]; v2u yv[2][4], qv[2][4];
#pragma unroll
        for (int sx = 0; sx < 2; ++sx) {
            const int row = 32 * w + 16 * sx + s;
            gg[sx] = *(LAS float*)(L + G2_G + row * 4);
#pragma unroll
            for (int dt = 0; dt < 4; ++dt) {
                const int d = 16 * dt + 4 * g + q;
                yv[sx][dt] = tr_read(L + G2_Y + d * 256 + (((2 * w + sx) ^ (d & 7)) * 32) + 8 * pp);
                qv[sx][dt] = *(LAS v2u*)(L + G2_Q + row * 128 + (((2 * dt + (g >> 1)) ^ ((row >> 1) & 7)) * 16) + 8 * (g & 1));
            }
        }
        SCHED();
#pragma unroll
        for (int sx = 0; sx < 2; ++sx) {
            const int row = 32 * w + 16 * sx + s;
            const float omg = 1.f - gg[sx];
#pragma unroll
            for (int dt = 0; dt < 4; ++dt) {
                const f32x4 a = acc[dt][sx];
                const f32x2 o01 = bf_2(qv[sx][dt].x) * bf_2(yv[sx][dt].x) * gg[sx] + (f32x2){a[0], a[1]} * omg;
                const f32x2 o23 = bf_2(qv[sx][dt].y) * bf_2(yv[sx][dt].y) * gg[sx] + (f32x2){a[2], a[3]} * omg;
                *(LAS v2u*)(L + G2_Q + row * 128 + (((2 * dt + (g >> 1)) ^ ((row >> 1) & 7)) * 16) + 8 * (g & 1)) = (v2u){pk2(o01), pk2(o23)};
            }
        }
    }
    WAIT_LGKM0();
    const __amdgpu_buffer_rsrc_t ro = __builtin_amdgcn_make_buffer_rsrc(p.out, 0, S * D * 2, 0x00020000);
#pragma unroll
    for (int i = 0; i < 4; ++i) {
        const int row = 32 * w + 8 * i + (lane >> 3), c = lane & 7;
        const v4u val = *(LAS v4u*)(L + G2_Q + row * 128 + c * 16);
        __builtin_amdgcn_raw_buffer_store_b128(val, ro, (panel * 128 + row) * (D * 2) + ct * 128 + ((c ^ ((row >> 1) & 7)) * 16), 0, OUT_AUX);
    }
    STAMP(11);
}

__global__ void __launch_bounds__(NTHR, 2) k_fused(Params p) {
    extern __shared__ __attribute__((aligned(16))) char smem[];
    LAS char* L = (LAS char*)smem;
    const int tid = threadIdx.x, lane = tid & 63, w = __builtin_amdgcn_readfirstlane(tid >> 6);
    const int x = blockIdx.x & 7, idx = blockIdx.x >> 3, xr = x >> 1, xc = x & 1;
    STAMP(0);
    gu32* ctl = (gu32*)(p.ws + WS_CTL);
    unsigned epoch_v = __hip_atomic_load(ctl + W_EPOCH * FL, RLX_AGENT);
#define GET_TAG() asm volatile("" : "+v"(epoch_v)); const unsigned tag = __builtin_amdgcn_readfirstlane(epoch_v) + 1u
    const __amdgpu_buffer_rsrc_t rs = __builtin_amdgcn_make_buffer_rsrc(p.ws, 0, WS_END, 0x00020000);
    if (idx < 24) {
        const int panel = 4 * xr + idx / 6, ct = 6 * xc + idx % 6;
        const int off1 = 5 * ((idx / 6 + idx % 6) & 1);
        const bool typeA = idx < 20;
        const int panel2 = 4 * xr + idx / 5, ct2 = 5 * xc + idx % 5;
        const int off2 = 12 * ((idx / 5 + idx % 5) & 1);
        if (typeA) {
            if (w < 4) {
                ConvIn cin;
                g1_compute<true>(p, L, rs, panel, ct, w, lane, cin, 0 STAMP_PASS);
                STAMP(3);
                WAIT_VM(0);
                GET_TAG();
                STAMP(4);
                BAR();
                if (tid == 0) set_flag(ctl, W_H + panel * 12 + ct, tag);
                g2_compute(p, L, rs, panel2, ct2, off2, w, lane STAMP_PASS);
            } else {
                const int lw = w - 4;
                W2Src w2s;
                w2_init(w2s, p, ct2, off2, lane);
                g1_loader<true>(p, panel, ct, off1, w2s, lw, lane STAMP_PASS);
                BAR();
                g1_loader_epilogue(L, rs, panel, ct, lw, lane);
                w2_issue(w2s, 32 + lw); w2_issue(w2s, 36 + lw);
                WAIT_VM(8);
                GET_TAG();
                BAR();
                g2_loader(p, L, rs, tag, w2s, panel2, ct2, lw, lane STAMP_PASS);
            }
        } else {
            const int bw = x * 4 + idx - 20, pr = w >> 1, hf = w & 1;
            const int ch = 448 + 6 * bw + pr, chn = pr < 2 ? ch + 4 : -1;
            ConvIn cin;
            if (w < 4) {
                g1_compute<false>(p, L, rs, panel, ct, w, lane, cin, ch STAMP_PASS);
                STAMP(3);
            } else {
                W2Src w2s;
                g1_loader<false>(p, panel, ct, off1, w2s, w - 4, lane STAMP_PASS);
                conv_load_h(p, cin, ch, hf, lane);
                { GET_TAG(); if (w == 7) poll_flags(ctl, W_KV, 64, tag, lane); }
                BAR();
                conv_load_x(cin, rs, ch, hf, lane);
                g1_loader_epilogue(L, rs, panel, ct, w - 4, lane);
            }
            WAIT_VM(0);
            GET_TAG();
            STAMP(4);
            BAR();
            if (tid == 0) set_flag(ctl, W_H + panel * 12 + ct, tag);
            conv_rounds(p, L, rs, cin, ch, chn, pr, hf, lane STAMP_PASS);
            WAIT_VM(0);
            BAR();
            if (tid == 0) set_flag(ctl, W_Y + 64 + bw, tag);
            STAMP(16);
        }
    } else {
        const int cw = x * 8 + (idx - 24), pr = w >> 1, hf = w & 1;
        const int ch = 7 * cw + pr, chn = pr < 3 ? ch + 4 : -1;
        ConvIn cin;
        conv_load_h(p, cin, ch, hf, lane);
        prepass(p, L, rs, cw, tid STAMP_PASS);
        STAMP(2);
        WAIT_VM(0);
        GET_TAG();
        BAR();
        if (tid == 0) set_flag(ctl, W_KV + cw, tag);
        if (hf == 0 && lane < 8) *(LAS unsigned*)(L + pr * CV_PAIR + CV_SYNC + 4 * lane) = 0u;
        STAMP(3);
        if (w == 1) poll_flags(ctl, W_KV, 64, tag, lane);
        WAIT_LGKM0();
        BAR();
        STAMP(4);
        conv_load_x(cin, rs, ch, hf, lane);
        conv_rounds(p, L, rs, cin, ch, chn, pr, hf, lane STAMP_PASS);
        WAIT_VM(0);
        BAR();
        if (tid == 0) set_flag(ctl, W_Y + cw, tag);
        STAMP(16);
        if (w == 7 && blockIdx.x == 255) {
#pragma unroll 1
            for (int pn = 0; pn < 3; ++pn) poll_flags(ctl, W_H + pn * 64, 64, tag, lane);
            if (lane == 0) set_flag(ctl, W_EPOCH, tag);
        }
    }
    STAMP_FLUSH();
}

extern "C" void kernel_launch(void* const* d_in, const int* in_sizes, int n_in, void* d_out, int out_size, void* d_ws, size_t ws_size, hipStream_t stream) {
    Params p;
    p.q = (const u16*)d_in[0]; p.k = (const u16*)d_in[1]; p.v = (const u16*)d_in[2]; p.shk = (const u16*)d_in[3];
    p.ssm = (const u16*)d_in[4]; p.Dv = (const u16*)d_in[5]; p.w1 = (const u16*)d_in[7]; p.w2 = (const u16*)d_in[8]; p.gw = (const u16*)d_in[9];
    p.out = (u16*)d_out; p.ws = (char*)d_ws;
    (void)hipFuncSetAttribute((const void*)k_fused, hipFuncAttributeMaxDynamicSharedMemorySize, LDS_BYTES);
    k_fused<<<256, NTHR, LDS_BYTES, stream>>>(p);
}
```

```cpp
#include <hip/hip_runtime.h>
#include <stdint.h>
#include <type_traits>

typedef unsigned short u16;
typedef unsigned v4u __attribute__((ext_vector_type(4)));
typedef unsigned v2u __attribute__((ext_vector_type(2)));
typedef float f32x4 __attribute__((ext_vector_type(4)));
typedef float f32x2 __attribute__((ext_vector_type(2)));
typedef short bf16x8 __attribute__((ext_vector_type(8)));
typedef short s16x4 __attribute__((ext_vector_type(4)));
typedef __bf16 bf16x2_t __attribute__((ext_vector_type(2)));
#define LAS __attribute__((address_space(3)))
#define GAS __attribute__((address_space(1)))
typedef GAS unsigned gu32;
typedef GAS unsigned long long gu64;
#define RLX_AGENT __ATOMIC_RELAXED, __HIP_MEMORY_SCOPE_AGENT

constexpr int S = 2048, D = 640, F = 1536;
constexpr int NTHR = 512;
constexpr unsigned WS_H = 0;
constexpr unsigned WS_KV = WS_H + S * F * 2;
constexpr unsigned WS_Y = WS_KV + S * D * 2;
constexpr unsigned WS_G = WS_Y + S * D * 2;
constexpr unsigned WS_CTL = WS_G + S * 4;
constexpr int FL = 32;
constexpr int W_EPOCH = 0, W_H = 1, W_KV = W_H + 192, W_G = W_KV + 64, W_Y = W_G + 128, W_LINES = W_Y + 96;
constexpr unsigned WS_END = WS_CTL + W_LINES * 128;
constexpr int AUX_SC1 = 16;
constexpr int H_AUX = 16;
constexpr unsigned kSpinCap = 1u << 18;

struct Params {
    const u16 *q, *k, *v, *shk, *ssm, *Dv, *w1, *w2, *gw;
    u16* out;
    char* ws;
};

__device__ __forceinline__ float bf_lo(unsigned u) { return __uint_as_float(u << 16); }
__device__ __forceinline__ float bf_hi(unsigned u) { return __uint_as_float(u & 0xffff0000u); }
__device__ __forceinline__ f32x2 bf_2(unsigned u) { return (f32x2){bf_lo(u), bf_hi(u)}; }
__device__ __forceinline__ unsigned pk2(f32x2 v) { return __builtin_bit_cast(unsigned, __builtin_convertvector(v, bf16x2_t)); }
__device__ __forceinline__ unsigned pk_bf16(float lo, float hi) { return pk2((f32x2){lo, hi}); }
__device__ __forceinline__ float rbf(float x) { return bf_lo(pk_bf16(x, x)); }
__device__ __forceinline__ f32x4 mfma16(v4u a, v4u b, f32x4 c) { return __builtin_amdgcn_mfma_f32_16x16x32_bf16(__builtin_bit_cast(bf16x8, a), __builtin_bit_cast(bf16x8, b), c, 0, 0, 0); }
__device__ __forceinline__ v2u tr_read(LAS char* p) { return __builtin_bit_cast(v2u, __builtin_amdgcn_ds_read_tr16_b64_v4i16((LAS s16x4*)p)); }
__device__ __forceinline__ v4u lds_rd16(LAS char* p) { return *(LAS v4u*)p; }
__device__ __forceinline__ void glds16(unsigned voff, const void* sbase, unsigned lds_dst) {
    asm volatile("s_mov_b32 m0, %2\n\ts_nop 0\n\tglobal_load_lds_dwordx4 %0, %1" :: "v"(voff), "s"(sbase), "s"(lds_dst) : "memory");
}
template <bool NT> __device__ __forceinline__ void glds16t(unsigned voff, const void* sbase, unsigned lds_dst) {
    if (NT) asm volatile("s_mov_b32 m0, %2\n\ts_nop 0\n\tglobal_load_lds_dwordx4 %0, %1 nt" :: "v"(voff), "s"(sbase), "s"(lds_dst) : "memory");
    else glds16(voff, sbase, lds_dst);
}
#define NT_X 0
#define NT_W1 0
#define NT_W2 0
#define NT_KV 0
#define NT_SSM 0
template <bool NT> __device__ __forceinline__ v4u gld16(const void* ptr) {
    if (NT) return __builtin_nontemporal_load((const GAS v4u*)ptr);
    return *(const GAS v4u*)ptr;
}
#define WAIT_VM(n) asm volatile("s_waitcnt vmcnt(%0)" :: "n"(n) : "memory")
#define WAIT_LGKM0() asm volatile("s_waitcnt lgkmcnt(0)" ::: "memory")
#define BAR() do { asm volatile("" ::: "memory"); __builtin_amdgcn_s_barrier(); asm volatile("" ::: "memory"); } while (0)
#define SCHED() __builtin_amdgcn_sched_barrier(0)
#define A_MFMA(acc, a, b) asm volatile("v_mfma_f32_16x16x32_bf16 %0, %1, %2, %0" : "+v"(acc) : "v"(a), "v"(b))
#define A_RD128(dst, addr, off) asm volatile("ds_read_b128 %0, %1 offset:%2" : "=v"(dst) : "v"(addr), "n"(off))
#define A_RDTR(dst, addr, off) asm volatile("ds_read_b64_tr_b16 %0, %1 offset:%2" : "=v"(dst) : "v"(addr), "n"(off))
#define A_LGKM(n) asm volatile("s_waitcnt lgkmcnt(%0)" :: "n"(n))
#define A_MFMA_DRAIN() asm volatile("s_nop 15\n\ts_nop 7" ::: "memory")
template <int NM, int NR, int PER> __device__ __forceinline__ void interleave() {
#pragma unroll
    for (int i = 0; i < NM; ++i) {
        __builtin_amdgcn_sched_group_barrier(0x8, 1, 0);
        if (i * PER < NR) __builtin_amdgcn_sched_group_barrier(0x100, PER, 0);
    }
}
#define STAMP(id) do {} while (0)
#define STAMP_ARG
#define STAMP_PASS
#define STAMP_FLUSH() do {} while (0)

__device__ __forceinline__ void st16_sc1(v4u v, __amdgpu_buffer_rsrc_t rs, unsigned off) { __builtin_amdgcn_raw_buffer_store_b128(v, rs, off, 0, AUX_SC1); }
__device__ __forceinline__ void poll_flags(gu32* ctl, int line, int n, unsigned tag, int lane) {
    gu32* f = ctl + (line + (lane < n ? lane : 0)) * FL;
#pragma unroll 1
    for (unsigned spins = 0; spins < kSpinCap; ++spins) {
        const unsigned v = __hip_atomic_load(f, RLX_AGENT);
        if (__all(v == tag)) break;
        __builtin_amdgcn_s_sleep(4);
    }
    __builtin_amdgcn_fence(__ATOMIC_ACQUIRE, "wavefront");
}
__device__ __forceinline__ void set_flag(gu32* ctl, int line, unsigned tag) { __hip_atomic_store(ctl + line * FL, tag, RLX_AGENT); }
__device__ __forceinline__ unsigned silu2f(f32x2 h) {
    const f32x2 t = h * -1.44269504f;
    const f32x2 den = (f32x2){__builtin_amdgcn_exp2f(t.x), __builtin_amdgcn_exp2f(t.y)} + 1.f;
    const f32x2 r = (f32x2){__builtin_amdgcn_rcpf(den.x), __builtin_amdgcn_rcpf(den.y)};
    return pk2(h * r);
}
__device__ __forceinline__ unsigned silu2(unsigned hb) { return silu2f(bf_2(hb)); }
__device__ __forceinline__ float row_sum16(float v) {
    v += __builtin_bit_cast(float, __builtin_amdgcn_update_dpp(0, __builtin_bit_cast(int, v), 0x128, 0xf, 0xf, false));
    v += __builtin_bit_cast(float, __builtin_amdgcn_update_dpp(0, __builtin_bit_cast(int, v), 0x124, 0xf, 0xf, false));
    v += __builtin_bit_cast(float, __builtin_amdgcn_update_dpp(0, __builtin_bit_cast(int, v), 0x122, 0xf, 0xf, false));
    v += __builtin_bit_cast(float, __builtin_amdgcn_update_dpp(0, __builtin_bit_cast(int, v), 0x121, 0xf, 0xf, false));
    return v;
}

constexpr int G1_SLOT = 32768, G1_NS = 5, G1_NST = D / 64, G1_STG = 3 * G1_SLOT;
constexpr int G2_NST = F / 64, G2_RING = 40 * 4096, G2_HD = 8;
constexpr int G2_Q = 8 * 4096, G2_Y = 12 * 4096, G2_G = 16 * 4096;
constexpr int CV_COPY = 4160, CV_X = 4 * CV_COPY, CV_PAIR = CV_X + 4608;
constexpr int CV_EX = 4 * CV_PAIR;
constexpr int LDS_BYTES = 163840;
static_assert(CV_EX + 4 * 8192 <= LDS_BYTES, "LDS map");

struct W2Src { unsigned vw[4]; const char* base; int kb0; unsigned aw[4]; };
__device__ __forceinline__ void w2_init(W2Src& s, const Params& p, int ct, int off2, int lane) {
#pragma unroll
    for (int i = 0; i < 4; ++i) {
        const int r = lane >> 3, c = lane & 7, k = 8 * i + r;
        s.vw[i] = (unsigned)(k * (D * 2) + ((c ^ (((k >> 1) & 3) << 1)) * 16));
    }
    const int g = lane >> 4, q = (lane & 15) >> 2, pp = lane & 3, sw = ((g & 1) << 1) | (q >> 1);
#pragma unroll
    for (int dt = 0; dt < 4; ++dt) s.aw[dt] = (4 * g + q) * 128 + ((dt ^ sw) * 32) + 8 * pp;
    s.base = (const char*)p.w2 + ct * 128; s.kb0 = 2 * off2;
}
__device__ __forceinline__ void w2_issue(const W2Src& s, int b) {
    const int kb = s.kb0 + b >= 48 ? s.kb0 + b - 48 : s.kb0 + b;
    const char* src = s.base + (size_t)kb * (32 * D * 2);
    const unsigned slot = (unsigned)(b >= 40 ? b - 40 : b) * 4096;
#pragma unroll
    for (int i = 0; i < 4; ++i) glds16t<NT_W2>(s.vw[i], src, slot + i * 1024);
}
__device__ __forceinline__ void w2_repack(const W2Src& s, LAS char* L, int b, int lane) {
    LAS char* blk = L + (b >= 40 ? b - 40 : b) * 4096;
    v4u f[4];
#pragma unroll
    for (int dt = 0; dt < 4; ++dt) { const v2u lo = tr_read(blk + s.aw[dt]), hi = tr_read(blk + s.aw[dt] + 2048); f[dt] = (v4u){lo.x, lo.y, hi.x, hi.y}; }
    WAIT_LGKM0();
#pragma unroll
    for (int dt = 0; dt < 4; ++dt) *(LAS v4u*)(blk + dt * 1024 + lane * 16) = f[dt];
    WAIT_LGKM0();
}

template <bool TYPE_A>
__device__ __forceinline__ void g1_loader(const Params& p, int panel, int ct, int off1, const W2Src& w2s, int lw, int lane STAMP_ARG) {
    unsigned vx[4], vw[4];
#pragma unroll
    for (int i = 0; i < 4; ++i) {
        const int r = lane >> 3, c = lane & 7, row = 32 * lw + 8 * i + r;
        vx[i] = (unsigned)(row * (D * 2) + ((c ^ ((row >> 1) & 7)) * 16));
        const int r2 = lane >> 4, c2 = lane & 15, k = 16 * lw + 4 * i + r2, phi = (k & 3) | (((k >> 3) & 1) << 2);
        vw[i] = (unsigned)(k * (F * 2) + ((c2 ^ (phi << 1)) * 16));
    }
    const char* xb = (const char*)p.q + (size_t)panel * 128 * D * 2;
    const char* wb = (const char*)p.w1 + (size_t)ct * 256;
    int kst = off1;
    auto issue = [&](int slot_i) {
        const unsigned slot = (unsigned)slot_i * G1_SLOT;
        const char* xs = xb + kst * 128; const char* wsrc = wb + (size_t)kst * (64 * F * 2);
#pragma unroll
        for (int i = 0; i < 4; ++i) glds16t<NT_X>(vx[i], xs, slot + (32 * lw + 8 * i) * 128);
#pragma unroll
        for (int i = 0; i < 4; ++i) glds16t<NT_W1>(vw[i], wsrc, slot + 16384 + (16 * lw + 4 * i) * 256);
        kst = kst + 1 == G1_NST ? 0 : kst + 1;
    };
#pragma unroll
    for (int st = 0; st < 3; ++st) issue(st);
    WAIT_VM(16); BAR(); issue(3); issue(4);
#pragma unroll 1
    for (int i = 1; i < 6; ++i) { WAIT_VM(24); BAR(); issue(i - 1); }
    STAMP(1);
    if (TYPE_A) {
        WAIT_VM(24); BAR(); w2_issue(w2s, 0 + lw); w2_issue(w2s, 4 + lw);
        WAIT_VM(24); BAR(); w2_issue(w2s, 8 + lw); w2_issue(w2s, 12 + lw);
        WAIT_VM(24); BAR(); w2_issue(w2s, 16 + lw); w2_issue(w2s, 20 + lw);
        WAIT_VM(24); BAR(); w2_issue(w2s, 28 + lw);
    } else {
        WAIT_VM(24); BAR(); WAIT_VM(16); BAR(); WAIT_VM(8); BAR(); WAIT_VM(0); BAR();
    }
}
__device__ __forceinline__ void g1_store(__amdgpu_buffer_rsrc_t rs, int stg, int ksg, int lane, v4u hb) {
    const v4u o = {silu2(hb.x), silu2(hb.y), silu2(hb.z), silu2(hb.w)};
    st16_sc1(o, rs, WS_H + (stg * 48 + ksg) * 1024 + lane * 16);
}
struct ConvIn { v4u v[2], vp[2], x[2]; unsigned vq[2]; float Dd; };
__device__ __forceinline__ void conv_load_h(const Params& p, ConvIn& c, int ch, int hf, int lane) {
    const char* hsrc = (const char*)p.ssm + (size_t)ch * (S * 2);
#pragma unroll
    for (int j = 0; j < 2; ++j) {
        const int o = (2 * hf + j) * 1024 + lane * 16;
        c.v[j] = gld16<NT_SSM>(hsrc + o);
        c.vp[j] = gld16<NT_SSM>(hsrc + (o < 4 ? 0 : o - 4));
        c.vq[j] = *(const GAS unsigned*)(hsrc + (o < 8 ? 0 : o - 8));
    }
    c.Dd = bf_lo((unsigned)p.Dv[ch]);
}
__device__ __forceinline__ void conv_load_x(ConvIn& c, __amdgpu_buffer_rsrc_t rs, int ch, int hf, int lane) {
#pragma unroll
    for (int j = 0; j < 2; ++j) c.x[j] = __builtin_amdgcn_raw_buffer_load_b128(rs, (2 * hf + j) * 1024 + lane * 16, WS_KV + ch * (S * 2), AUX_SC1);
}
constexpr int CV_SYNC = 4128;
template <bool TYPE_A>
__device__ __forceinline__ void g1_compute(const Params& p, LAS char* L, __amdgpu_buffer_rsrc_t rs, int panel, int ct, int w, int lane, ConvIn& cin, int ch STAMP_ARG) {
    const int wf = w & 1, wsx = w >> 1;
    const int g = lane >> 4, q = (lane & 15) >> 2, pp = lane & 3, s = lane & 15;
    const int phi = q | ((g & 1) << 2);
    unsigned aw[4], ax[2];
#pragma unroll
    for (int ft = 0; ft < 4; ++ft) aw[ft] = 16384 + (8 * g + q) * 256 + (((4 * wf + ft) ^ phi) * 32) + 8 * pp;
#pragma unroll
    for (int ks = 0; ks < 2; ++ks) ax[ks] = (64 * wsx + s) * 128 + (((4 * ks + g) ^ ((s >> 1) & 7)) * 16);
    f32x4 acc[4][4];
#pragma unroll
    for (int a = 0; a < 4; ++a)
#pragma unroll
        for (int b = 0; b < 4; ++b) acc[a][b] = (f32x4){0.f, 0.f, 0.f, 0.f};
    v4u A[2][4], B[2][4];
#pragma unroll
    for (int a = 0; a < 4; ++a) { A[1][a] = (v4u){0u, 0u, 0u, 0u}; B[1][a] = (v4u){0u, 0u, 0u, 0u}; }
    auto rd = [&](int set, LAS char* base, int ks) {
#pragma unroll
        for (int ft = 0; ft < 4; ++ft) {
            const v2u lo = tr_read(base + aw[ft] + ks * 8192), hi = tr_read(base + aw[ft] + ks * 8192 + 1024);
            A[set][ft] = (v4u){lo.x, lo.y, hi.x, hi.y};
            if (ft == 0) {
#pragma unroll
                for (int sx = 0; sx < 4; ++sx) B[set][sx] = lds_rd16(base + ax[ks] + sx * 2048);
            }
        }
    };
    auto mm = [&](int set) {
#pragma unroll
        for (int ft = 0; ft < 4; ++ft)
#pragma unroll
            for (int sx = 0; sx < 4; ++sx) acc[ft][sx] = mfma16(A[set][ft], B[set][sx], acc[ft][sx]);
    };
    unsigned slot = 0;
#define X_G1REP 1
#pragma unroll 1
    for (int i = 0; i < G1_NST * X_G1REP; ++i) {
        LAS char* base = L + slot;
        slot = slot + G1_SLOT == G1_NS * G1_SLOT ? 0 : slot + G1_SLOT;
        BAR();
        if (i == 0) STAMP(1);
        rd(0, base, 0);
        mm(1);
        interleave<16, 12, 1>();
        SCHED();
        rd(1, base, 1);
        mm(0);
        interleave<16, 12, 1>();
        SCHED();
        WAIT_LGKM0();
    }
    mm(1);
    STAMP(2);
    if (!TYPE_A) conv_load_h(p, cin, ch, w & 1, lane);
    SCHED();
#pragma unroll
    for (int j = 0; j < 4; ++j) {
        const f32x4 a = acc[2 * (j & 1)][2 + (j >> 1)], b = acc[2 * (j & 1) + 1][2 + (j >> 1)];
        *(LAS v4u*)(L + G1_STG + w * 4096 + j * 1024 + lane * 16) = (v4u){pk_bf16(a[0], a[1]), pk_bf16(a[2], a[3]), pk_bf16(b[0], b[1]), pk_bf16(b[2], b[3])};
    }
    if (!TYPE_A && lane < 8) *(LAS unsigned*)(L + w * CV_PAIR + CV_SYNC + 4 * lane) = 0u;
    WAIT_LGKM0();
    BAR();
    if (!TYPE_A) conv_load_x(cin, rs, ch, w & 1, lane);
    SCHED();
#pragma unroll
    for (int j = 0; j < 4; ++j) {
        const f32x4 a = acc[2 * (j & 1)][j >> 1], b = acc[2 * (j & 1) + 1][j >> 1];
        const v4u o = {silu2f((f32x2){a[0], a[1]}), silu2f((f32x2){a[2], a[3]}), silu2f((f32x2){b[0], b[1]}), silu2f((f32x2){b[2], b[3]})};
        st16_sc1(o, rs, WS_H + ((panel * 8 + wsx * 4 + (j >> 1)) * 48 + ct * 4 + wf * 2 + (j & 1)) * 1024 + lane * 16);
    }
}
__device__ __forceinline__ void g1_loader_epilogue(LAS char* L, __amdgpu_buffer_rsrc_t rs, int panel, int ct, int lw, int lane) {
    const int wf = lw & 1, wsx = lw >> 1;
    v4u hb[4];
#pragma unroll
    for (int j = 0; j < 4; ++j) hb[j] = *(LAS v4u*)(L + G1_STG + lw * 4096 + j * 1024 + lane * 16);
#pragma unroll
    for (int j = 0; j < 4; ++j) g1_store(rs, panel * 8 + wsx * 4 + 2 + (j >> 1), ct * 4 + wf * 2 + (j & 1), lane, hb[j]);
}

__device__ __forceinline__ void prepass(const Params& p, LAS char* L, __amdgpu_buffer_rsrc_t rs, int wg, int tid STAMP_ARG) {
    const int s0 = 64 * (wg >> 1), c0 = 320 * (wg & 1);
    {
        const int w = __builtin_amdgcn_readfirstlane(tid >> 6);
        const int item = w < 4 ? tid : 256 + (tid & 63);
        const int cc = item % 40, rg = item / 40;
        const int ch = c0 + 8 * cc, r0 = s0 + 8 * rg;
        v4u kk[11], vv[8], sw[4];
#pragma unroll
        for (int j = 0; j < 4; ++j) sw[j] = *(const GAS v4u*)((const char*)p.shk + (size_t)ch * 8 + j * 16);
#pragma unroll
        for (int j = 0; j < 11; ++j) {
            const int row = r0 - 3 + j;
            kk[j] = gld16<NT_KV>((const char*)p.k + (size_t)(row < 0 ? 0 : row) * (D * 2) + ch * 2);
        }
#pragma unroll
        for (int j = 0; j < 8; ++j) vv[j] = gld16<NT_KV>((const char*)p.v + (size_t)(r0 + j) * (D * 2) + ch * 2);
        if (r0 == 0) { kk[0] = (v4u){0u, 0u, 0u, 0u}; kk[1] = kk[0]; kk[2] = kk[0]; }
#pragma unroll
        for (int e = 0; e < 4; ++e) {
            if (w >= 4 && w - 4 != e) continue;
            const f32x2 W0 = {bf_lo(sw[e].x), bf_lo(sw[e].z)}, W1 = {bf_hi(sw[e].x), bf_hi(sw[e].z)};
            const f32x2 W2 = {bf_lo(sw[e].y), bf_lo(sw[e].w)}, W3 = {bf_hi(sw[e].y), bf_hi(sw[e].w)};
            f32x2 kf[11], o[8];
#pragma unroll
            for (int j = 0; j < 11; ++j) kf[j] = bf_2(kk[j][e]);
#pragma unroll
            for (int j = 0; j < 8; ++j) {
                const f32x2 ksh = kf[j] * W3 + kf[j + 1] * W2 + kf[j + 2] * W1 + kf[j + 3] * W0;
                o[j] = ksh * bf_2(vv[j][e]);
            }
            const v4u p0 = {pk_bf16(o[0].x, o[1].x), pk_bf16(o[2].x, o[3].x), pk_bf16(o[4].x, o[5].x), pk_bf16(o[6].x, o[7].x)};
            const v4u p1 = {pk_bf16(o[0].y, o[1].y), pk_bf16(o[2].y, o[3].y), pk_bf16(o[4].y, o[5].y), pk_bf16(o[6].y, o[7].y)};
            *(LAS v4u*)(L + (8 * cc + 2 * e) * 128 + ((rg ^ (cc & 7)) * 16)) = p0;
            *(LAS v4u*)(L + (8 * cc + 2 * e + 1) * 128 + ((rg ^ (cc & 7)) * 16)) = p1;
        }
    }
    STAMP(1);
    WAIT_LGKM0();
    BAR();
#pragma unroll
    for (int j = 0; j < 5; ++j) {
        const int n = tid + NTHR * j, ch = n >> 3, pc = n & 7;
        const v4u val = *(LAS v4u*)(L + ch * 128 + ((pc ^ ((ch >> 3) & 7)) * 16));
        st16_sc1(val, rs, WS_KV + (c0 + ch) * (S * 2) + s0 * 2 + pc * 16);
    }
}
struct GateIn { v4u gw[5], q[5]; };
__device__ __forceinline__ void gate_load(const Params& p, GateIn& gi, int row0, int lane) {
    const int i = lane & 15, row = row0 + (lane >> 4);
#pragma unroll
    for (int j = 0; j < 5; ++j) {
        gi.gw[j] = *(const GAS v4u*)((const char*)p.gw + (i + 16 * j) * 16);
        gi.q[j] = *(const GAS v4u*)((const char*)p.q + (size_t)row * (D * 2) + (i + 16 * j) * 16);
    }
}
__device__ __forceinline__ void gate_finish(const GateIn& gi, __amdgpu_buffer_rsrc_t rs, int row0, int lane) {
    f32x2 a2 = {0.f, 0.f};
#pragma unroll
    for (int j = 0; j < 5; ++j)
#pragma unroll
        for (int e = 0; e < 4; ++e) a2 += bf_2(gi.q[j][e]) * bf_2(gi.gw[j][e]);
    const float z = rbf(row_sum16(a2.x + a2.y));
    const float gv = rbf(__builtin_amdgcn_rcpf(1.f + __builtin_amdgcn_exp2f(z * -1.44269504f)));
    if ((lane & 15) == 0) __builtin_amdgcn_raw_buffer_store_b32(__float_as_uint(gv), rs, WS_G + (row0 + (lane >> 4)) * 4, 0, AUX_SC1);
}

__device__ __forceinline__ void pair_sync(LAS char* words, int hf) {
    WAIT_LGKM0();
    *(volatile LAS unsigned*)(words + 4 * hf) = 1u;
#pragma unroll 1
    for (unsigned spins = 0; spins < (1u << 22); ++spins) { if (__builtin_amdgcn_readfirstlane(*(volatile LAS unsigned*)(words + 4 * (1 - hf))) != 0u) break; }
    asm volatile("" ::: "memory");
}
template <int hf>
__device__ __forceinline__ void conv_half(const Params& p, LAS char* Lw, LAS char* Lx, __amdgpu_buffer_rsrc_t rs, ConvIn& c, int ch, int chn, int round, int lane STAMP_ARG) {
    const int g = lane >> 4, q = (lane & 15) >> 2, pp = lane & 3, n = lane & 15;
    if (hf == 0) {
        if (lane < 30) *(LAS v4u*)(Lw + CV_X + lane * 16) = (v4u){0u, 0u, 0u, 0u};
        if (lane < 8) *(LAS v4u*)(Lw + (lane >> 1) * CV_COPY + (lane & 1) * 16) = (v4u){0u, 0u, 0u, 0u};
        if (lane == 0) { c.vp[0] = (v4u){0u, c.v[0].x, c.v[0].y, c.v[0].z}; c.vq[0] = 0u; }
    }
#pragma unroll
    for (int j = 0; j < 2; ++j) {
        const v4u v = c.v[j], vp = c.vp[j];
        const unsigned a0 = __builtin_amdgcn_alignbit(v.x, vp.x, 16), a1 = __builtin_amdgcn_alignbit(v.y, v.x, 16), a2 = __builtin_amdgcn_alignbit(v.z, v.y, 16), a3 = __builtin_amdgcn_alignbit(v.w, v.z, 16);
        const unsigned am = __builtin_amdgcn_alignbit(vp.x, c.vq[j], 16);
        LAS char* d = Lw + 32 + (2 * hf + j) * 1024 + lane * 16;
        *(LAS v4u*)(d) = v;
        *(LAS v4u*)(d + CV_COPY) = (v4u){a0, a1, a2, a3};
        *(LAS v4u*)(d + 2 * CV_COPY) = vp;
        *(LAS v4u*)(d + 3 * CV_COPY) = (v4u){am, a0, a1, a2};
        *(LAS v4u*)(Lw + CV_X + 480 + (2 * hf + j) * 1024 + lane * 16) = c.x[j];
    }
    const float Dd = c.Dd;
    pair_sync(Lw + CV_SYNC + 16 * round, hf);
    if (chn >= 0) { conv_load_h(p, c, chn, hf, lane); conv_load_x(c, rs, chn, hf, lane); asm volatile("" ::: "memory"); }
    if (round == 0) STAMP(8); else STAMP(12);
    const unsigned la = (unsigned)(size_t)Lw + q * CV_COPY + 32 - 16 * g + 8 * pp + 24 + 256 * hf;
    const unsigned lb = (unsigned)(size_t)Lw + CV_X + 32 * n + 16 * g + 448 - 256 * hf - 192;
    f32x4 acc[8];
#pragma unroll
    for (int t = 0; t < 8; ++t) acc[t] = (f32x4){0.f, 0.f, 0.f, 0.f};
    v2u Alo[2][8], Ahi[2][8];
    v4u B[2][8];
#define CV_RD(set, rs_, k) do { \
        if ((k) % 3 == 0) A_RDTR(Alo[set][(k) / 3], la, 64 * (rs_) + 512 * ((k) / 3) + 8); \
        else if ((k) % 3 == 1) A_RDTR(Ahi[set][(k) / 3], la, 64 * (rs_) + 512 * ((k) / 3)); \
        else A_RD128(B[set][(k) / 3], lb, 192 - 64 * (rs_) + 512 * ((k) / 3)); } while (0)
    auto step = [&](auto SET, auto NEXT) {
        constexpr int set = decltype(SET)::value, nx = decltype(NEXT)::value;
        int k = 0;
#pragma unroll
        for (int l = 0; l < 8; ++l)
#pragma unroll
            for (int i = 0; i + l < 8; ++i) {
                const v4u a = {Alo[set][i].x, Alo[set][i].y, Ahi[set][i].x, Ahi[set][i].y};
                A_MFMA(acc[i + l], a, B[set][l]);
                if (nx >= 0 && k < 24) { CV_RD(1 - set, (nx >= 0 ? nx : 0), k); }
                ++k;
            }
        if (nx >= 0) A_LGKM(0);
    };
#pragma unroll
    for (int k = 0; k < 24; ++k) CV_RD(0, 0, k);
    A_LGKM(0);
    step(std::integral_constant<int, 0>{}, std::integral_constant<int, 1>{});
    step(std::integral_constant<int, 1>{}, std::integral_constant<int, 2>{});
    step(std::integral_constant<int, 0>{}, std::integral_constant<int, 3>{});
    step(std::integral_constant<int, 1>{}, std::integral_constant<int, -1>{});
#undef CV_RD
    A_MFMA_DRAIN();
    if (round == 0) STAMP(9); else STAMP(13);
#pragma unroll
    for (int t = 0; t < 4; ++t) *(LAS f32x4*)(Lx + hf * 4096 + t * 1024 + lane * 16) = acc[4 * (1 - hf) + t];
    v2u xv[4];
#pragma unroll
    for (int t = 0; t < 4; ++t) xv[t] = *(LAS v2u*)(Lw + CV_X + 480 + 2 * (1024 * hf + 256 * t + 16 * n + 4 * g));
    pair_sync(Lw + CV_SYNC + 16 * round + 8, hf);
    f32x4 o[4];
#pragma unroll
    for (int t = 0; t < 4; ++t) o[t] = *(LAS f32x4*)(Lx + (1 - hf) * 4096 + t * 1024 + lane * 16);
    v2u ov[4];
#pragma unroll
    for (int t = 0; t < 4; ++t) {
        const f32x4 y = acc[4 * hf + t] + o[t];
        ov[t] = (v2u){pk2(bf_2(xv[t].x) * Dd + (f32x2){y[0], y[1]}), pk2(bf_2(xv[t].y) * Dd + (f32x2){y[2], y[3]})};
    }
    LAS char* Ly = Lx + (1 - hf) * 4096;
#pragma unroll
    for (int t = 0; t < 4; ++t) *(LAS v2u*)(Ly + 2 * (256 * t + 16 * n + 4 * g)) = ov[t];
    WAIT_LGKM0();
#pragma unroll
    for (int j = 0; j < 2; ++j) {
        const v4u val = *(LAS v4u*)(Ly + j * 1024 + lane * 16);
        st16_sc1(val, rs, WS_Y + ch * (S * 2) + hf * 2048 + j * 1024 + lane * 16);
    }
    if (round == 0) STAMP(10); else STAMP(14);
}

__device__ __forceinline__ void conv_rounds(const Params& p, LAS char* L, __amdgpu_buffer_rsrc_t rs, ConvIn& c, int ch, int chn, int pr, int hf, int lane STAMP_ARG) {
    LAS char* Lw = L + pr * CV_PAIR; LAS char* Lx = L + CV_EX + pr * 8192;
#pragma unroll 1
    for (int round = 0; round < 2; ++round) {
        if (hf) conv_half<1>(p, Lw, Lx, rs, c, ch, chn, round, lane STAMP_PASS); else conv_half<0>(p, Lw, Lx, rs, c, ch, chn, round, lane STAMP_PASS);
        if (chn < 0) break;
        ch = chn; chn = -1;
    }
}

__device__ __forceinline__ void g2_loader(const Params& p, LAS char* L, __amdgpu_buffer_rsrc_t rs, unsigned tag, const W2Src& w2s, int panel, int ct, int lw, int lane STAMP_ARG) {
    gu32* ctl = (gu32*)(p.ws + WS_CTL);
    const int grow = panel * 128 + (ct & 7) * 16 + 4 * lw;
    if (lw == 0) poll_flags(ctl, W_H + panel * 12, 12, tag, lane);
    w2_issue(w2s, 24 + lw);
    GateIn gi;
    gate_load(p, gi, grow, lane);
    asm volatile("" ::: "memory");
    w2_repack(w2s, L, 0 + lw, lane); w2_repack(w2s, L, 4 + lw, lane);
    STAMP(5);
    BAR();
    w2_repack(w2s, L, 8 + lw, lane); w2_repack(w2s, L, 12 + lw, lane);
    if (ct < 8) gate_finish(gi, rs, grow, lane);
    else WAIT_VM(0);
    BAR();
    w2_issue(w2s, 40 + lw); w2_issue(w2s, 44 + lw);
    w2_repack(w2s, L, 16 + lw, lane); w2_repack(w2s, L, 20 + lw, lane);
    WAIT_VM(8);
    BAR();
    if (ct < 8 && lw == 0 && lane == 0) set_flag(ctl, W_G + panel * 8 + ct, tag);
#pragma unroll
    for (int i = 0; i < 4; ++i) {
        const int r = lane >> 3, c = lane & 7, row = 32 * lw + 8 * i + r;
        glds16((unsigned)(row * (D * 2) + ((c ^ ((row >> 1) & 7)) * 16)), (const char*)p.q + (size_t)panel * 128 * D * 2 + ct * 128, G2_Q + (32 * lw + 8 * i) * 128);
    }
#pragma unroll 1
    for (int b = 24; b < 40; b += 4) w2_repack(w2s, L, b + lw, lane);
    BAR();
    WAIT_VM(4);
    w2_repack(w2s, L, 40 + lw, lane); w2_repack(w2s, L, 44 + lw, lane);
    BAR();
    const int y0 = ct < 7 ? (64 * ct) / 7 : 64 + (64 * ct - 448) / 6, y1 = ct < 7 ? (64 * ct + 63) / 7 : 64 + (64 * ct + 63 - 448) / 6;
    poll_flags(ctl, W_Y + y0, y1 - y0 + 1, tag, lane);
    poll_flags(ctl, W_G + panel * 8, 8, tag, lane);
    STAMP(6);
    v4u yv[4];
#pragma unroll
    for (int i = 0; i < 4; ++i) {
        const int r = lane >> 4, c = lane & 15, d = 16 * lw + 4 * i + r;
        yv[i] = __builtin_amdgcn_raw_buffer_load_b128(rs, d * (S * 2) + c * 16, WS_Y + ct * 64 * S * 2 + panel * 256, AUX_SC1);
    }
    const unsigned gv = __builtin_amdgcn_raw_buffer_load_b32(rs, (lw * 32 + (lane & 31)) * 4, WS_G + panel * 512, AUX_SC1);
#pragma unroll
    for (int i = 0; i < 4; ++i) {
        const int r = lane >> 4, c = lane & 15, d = 16 * lw + 4 * i + r;
        *(LAS v4u*)(L + G2_Y + d * 256 + ((c ^ ((d & 7) << 1)) * 16)) = yv[i];
    }
    if (lane < 32) *(LAS unsigned*)(L + G2_G + (lw * 32 + lane) * 4) = gv;
    WAIT_VM(0);
    WAIT_LGKM0();
    BAR();
}
__device__ __forceinline__ void g2_compute(const Params& p, LAS char* L, __amdgpu_buffer_rsrc_t rs, int panel, int ct, int off2, int w, int lane STAMP_ARG) {
    const int g = lane >> 4, q = (lane & 15) >> 2, pp = lane & 3, s = lane & 15;
    const unsigned hbase = WS_H + (panel * 8 + 2 * w) * (48 * 1024);
    int kld = off2;
    f32x4 acc[4][2];
#pragma unroll
    for (int a = 0; a < 4; ++a) { acc[a][0] = (f32x4){0.f, 0.f, 0.f, 0.f}; acc[a][1] = (f32x4){0.f, 0.f, 0.f, 0.f}; }
    v4u H[G2_HD][2][2];
    auto ldh = [&](int slot) {
#pragma unroll
        for (int ks = 0; ks < 2; ++ks)
#pragma unroll
            for (int sx = 0; sx < 2; ++sx) H[slot][ks][sx] = __builtin_amdgcn_raw_buffer_load_b128(rs, lane * 16 + sx * (48 * 1024) + ks * 1024, hbase + kld * 2048, H_AUX);
        kld = kld + 1 == G2_NST ? 0 : kld + 1;
    };
    BAR();
    STAMP(5);
#pragma unroll
    for (int st = 0; st < G2_HD - 1; ++st) ldh(st);
#pragma unroll
    for (int a = 0; a < 4; ++a) H[G2_HD - 1][a >> 1][a & 1] = (v4u){0u, 0u, 0u, 0u};
    SCHED();
    v4u A[2][4];
#pragma unroll
    for (int a = 0; a < 4; ++a) A[1][a] = (v4u){0u, 0u, 0u, 0u};
    auto half = [&](int set, int slot, int ks, unsigned addr, int off) {
        A_LGKM(3);
        A_MFMA(acc[0][0], A[set][0], H[slot][ks][0]); A_RD128(A[1 - set][0], addr, off);
        A_MFMA(acc[0][1], A[set][0], H[slot][ks][1]); A_RD128(A[1 - set][1], addr, off + 1024);
        A_LGKM(4);
        A_MFMA(acc[1][0], A[set][1], H[slot][ks][0]); A_RD128(A[1 - set][2], addr, off + 2048);
        A_MFMA(acc[1][1], A[set][1], H[slot][ks][1]); A_RD128(A[1 - set][3], addr, off + 3072);
        A_LGKM(5);
        A_MFMA(acc[2][0], A[set][2], H[slot][ks][0]);
        A_MFMA(acc[2][1], A[set][2], H[slot][ks][1]);
        A_LGKM(4);
        A_MFMA(acc[3][0], A[set][3], H[slot][ks][0]);
        A_MFMA(acc[3][1], A[set][3], H[slot][ks][1]);
    };
    const unsigned lbase = (unsigned)(size_t)L + lane * 16;
    unsigned sb = 0;
    auto stage = [&](int u, bool load, bool drain) {
        const unsigned addr = lbase + sb;
        half(1, (u + G2_HD - 1) % G2_HD, 1, addr, 0);
        if (load) { ldh((u + G2_HD - 1) % G2_HD); asm volatile("" ::: "memory"); }
        half(0, u, 0, addr, 4096);
        if (drain) WAIT_LGKM0();
        sb = sb + 8192 == G2_RING ? 0 : sb + 8192;
    };
#pragma unroll 1
    for (int jj = 0; jj < 2; ++jj) {
#pragma unroll
        for (int u = 0; u < 8; ++u) {
            if (u == 4 || (u == 0 && jj == 1)) BAR();
            if (u == 0) { if (jj == 0) STAMP(14); else STAMP(12); }
            stage(u, true, (u & 3) == 3);
        }
    }
    BAR();
    STAMP(13);
#pragma unroll
    for (int u = 0; u < 8; ++u) stage(u, u == 0, false);
    WAIT_LGKM0();
#pragma unroll
    for (int dt = 0; dt < 4; ++dt) { A_MFMA(acc[dt][0], A[1][dt], H[G2_HD - 1][1][0]); A_MFMA(acc[dt][1], A[1][dt], H[G2_HD - 1][1][1]); }
    A_MFMA_DRAIN();
    STAMP(6);
    BAR();
    STAMP(7);
    {
        float gg[2]; v2u yv[2][4], qv[2][4];
#pragma unroll
        for (int sx = 0; sx < 2; ++sx) {
            const int row = 32 * w + 16 * sx + s;
            gg[sx] = *(LAS float*)(L + G2_G + row * 4);
#pragma unroll
            for (int dt = 0; dt < 4; ++dt) {
                const int d = 16 * dt + 4 * g + q;
                yv[sx][dt] = tr_read(L + G2_Y + d * 256 + (((2 * w + sx) ^ (d & 7)) * 32) + 8 * pp);
                qv[sx][dt] = *(LAS v2u*)(L + G2_Q + row * 128 + (((2 * dt + (g >> 1)) ^ ((row >> 1) & 7)) * 16) + 8 * (g & 1));
            }
        }
        SCHED();
#pragma unroll
        for (int sx = 0; sx < 2; ++sx) {
            const int row = 32 * w + 16 * sx + s;
            const float omg = 1.f - gg[sx];
#pragma unroll
            for (int dt = 0; dt < 4; ++dt) {
                const f32x4 a = acc[dt][sx];
                const f32x2 o01 = bf_2(qv[sx][dt].x) * bf_2(yv[sx][dt].x) * gg[sx] + (f32x2){a[0], a[1]} * omg;
                const f32x2 o23 = bf_2(qv[sx][dt].y) * bf_2(yv[sx][dt].y) * gg[sx] + (f32x2){a[2], a[3]} * omg;
                *(LAS v2u*)(L + G2_Q + row * 128 + (((2 * dt + (g >> 1)) ^ ((row >> 1) & 7)) * 16) + 8 * (g & 1)) = (v2u){pk2(o01), pk2(o23)};
            }
        }
    }
    WAIT_LGKM0();
#pragma unroll
    for (int i = 0; i < 4; ++i) {
        const int row = 32 * w + 8 * i + (lane >> 3), c = lane & 7;
        const v4u val = *(LAS v4u*)(L + G2_Q + row * 128 + c * 16);
        *(GAS v4u*)((char*)p.out + (size_t)(panel * 128 + row) * (D * 2) + ct * 128 + ((c ^ ((row >> 1) & 7)) * 16)) = val;
    }
    STAMP(11);
}

__global__ void __launch_bounds__(NTHR, 2) k_fused(Params p) {
    extern __shared__ __attribute__((aligned(16))) char smem[];
    LAS char* L = (LAS char*)smem;
    const int tid = threadIdx.x, lane = tid & 63, w = __builtin_amdgcn_readfirstlane(tid >> 6);
    const int x = blockIdx.x & 7, idx = blockIdx.x >> 3, xr = x >> 1, xc = x & 1;
    STAMP(0);
    gu32* ctl = (gu32*)(p.ws + WS_CTL);
    unsigned epoch_v = __hip_atomic_load(ctl + W_EPOCH * FL, RLX_AGENT);
#define GET_TAG() asm volatile("" : "+v"(epoch_v)); const unsigned tag = __builtin_amdgcn_readfirstlane(epoch_v) + 1u
    const __amdgpu_buffer_rsrc_t rs = __builtin_amdgcn_make_buffer_rsrc(p.ws, 0, WS_END, 0x00020000);
    if (idx < 24) {
        const int panel = 4 * xr + idx / 6, ct = 6 * xc + idx % 6;
        const int off1 = 5 * ((idx / 6 + idx % 6) & 1);
        const bool typeA = idx < 20;
        const int panel2 = 4 * xr + idx / 5, ct2 = 5 * xc + idx % 5;
        const int off2 = 12 * ((idx / 5 + idx % 5) & 1);
        if (typeA) {
            if (w < 4) {
                ConvIn cin;
                g1_compute<true>(p, L, rs, panel, ct, w, lane, cin, 0 STAMP_PASS);
                STAMP(3);
                WAIT_VM(0);
                GET_TAG();
                STAMP(4);
                BAR();
                if (tid == 0) set_flag(ctl, W_H + panel * 12 + ct, tag);
                g2_compute(p, L, rs, panel2, ct2, off2, w, lane STAMP_PASS);
            } else {
                const int lw = w - 4;
                W2Src w2s;
                w2_init(w2s, p, ct2, off2, lane);
                g1_loader<true>(p, panel, ct, off1, w2s, lw, lane STAMP_PASS);
                BAR();
                g1_loader_epilogue(L, rs, panel, ct, lw, lane);
                w2_issue(w2s, 32 + lw); w2_issue(w2s, 36 + lw);
                WAIT_VM(8);
                GET_TAG();
                BAR();
                g2_loader(p, L, rs, tag, w2s, panel2, ct2, lw, lane STAMP_PASS);
            }
        } else {
            const int bw = x * 4 + idx - 20, pr = w >> 1, hf = w & 1;
            const int ch = 448 + 6 * bw + pr, chn = pr < 2 ? ch + 4 : -1;
            ConvIn cin;
            if (w < 4) {
                g1_compute<false>(p, L, rs, panel, ct, w, lane, cin, ch STAMP_PASS);
                STAMP(3);
            } else {
                W2Src w2s;
                g1_loader<false>(p, panel, ct, off1, w2s, w - 4, lane STAMP_PASS);
                conv_load_h(p, cin, ch, hf, lane);
                { GET_TAG(); if (w == 7) poll_flags(ctl, W_KV, 64, tag, lane); }
                BAR();
                conv_load_x(cin, rs, ch, hf, lane);
                g1_loader_epilogue(L, rs, panel, ct, w - 4, lane);
            }
            WAIT_VM(0);
            GET_TAG();
            STAMP(4);
            BAR();
            if (tid == 0) set_flag(ctl, W_H + panel * 12 + ct, tag);
            conv_rounds(p, L, rs, cin, ch, chn, pr, hf, lane STAMP_PASS);
            WAIT_VM(0);
            BAR();
            if (tid == 0) set_flag(ctl, W_Y + 64 + bw, tag);
            STAMP(16);
        }
    } else {
        const int cw = x * 8 + (idx - 24), pr = w >> 1, hf = w & 1;
        const int ch = 7 * cw + pr, chn = pr < 3 ? ch + 4 : -1;
        ConvIn cin;
        conv_load_h(p, cin, ch, hf, lane);
        prepass(p, L, rs, cw, tid STAMP_PASS);
        STAMP(2);
        WAIT_VM(0);
        GET_TAG();
        BAR();
        if (tid == 0) set_flag(ctl, W_KV + cw, tag);
        if (hf == 0 && lane < 8) *(LAS unsigned*)(L + pr * CV_PAIR + CV_SYNC + 4 * lane) = 0u;
        STAMP(3);
        if (w == 1) poll_flags(ctl, W_KV, 64, tag, lane);
        WAIT_LGKM0();
        BAR();
        STAMP(4);
        conv_load_x(cin, rs, ch, hf, lane);
        conv_rounds(p, L, rs, cin, ch, chn, pr, hf, lane STAMP_PASS);
        WAIT_VM(0);
        BAR();
        if (tid == 0) set_flag(ctl, W_Y + cw, tag);
        STAMP(16);
        if (w == 7 && blockIdx.x == 255) {
#pragma unroll 1
            for (int pn = 0; pn < 3; ++pn) poll_flags(ctl, W_H + pn * 64, 64, tag, lane);
            if (lane == 0) set_flag(ctl, W_EPOCH, tag);
        }
    }
    STAMP_FLUSH();
}

constexpr size_t WS_DBG2 = 64u << 20;
__global__ void k_marker(char* ws) { if (threadIdx.x == 0) { *(unsigned long long*)(ws + WS_DBG2 + (1 << 20)) = __builtin_amdgcn_s_memrealtime(); } }
__global__ void __launch_bounds__(512) k_dummy(char* ws) {
    const unsigned long long t = __builtin_amdgcn_s_memrealtime();

    if (threadIdx.x == 0) ((unsigned*)(ws + WS_DBG2))[blockIdx.x] = (unsigned)t;
}
__global__ void k_probe(char* ws) {
    const unsigned* d = (const unsigned*)(ws + WS_DBG2);
    const unsigned tm = *(const unsigned*)(ws + WS_DBG2 + (1 << 20));
    int mi = 2000000000;
    for (int e = threadIdx.x; e < 256; e += 64) mi = min(mi, (int)(d[e] - tm));
    for (int o = 32; o >= 1; o >>= 1) mi = min(mi, __shfl_xor(mi, o));
    const unsigned long long dur = (unsigned long long)(mi) * 10ull;
    const unsigned long long t0 = __builtin_amdgcn_s_memrealtime();
    while (__builtin_amdgcn_s_memrealtime() - t0 < dur && __builtin_amdgcn_s_memrealtime() - t0 < 100000ull) __builtin_amdgcn_s_sleep(1);
}
extern "C" void kernel_launch(void* const* d_in, const int* in_sizes, int n_in, void* d_out, int out_size, void* d_ws, size_t ws_size, hipStream_t stream) {
    Params p;
    p.q = (const u16*)d_in[0]; p.k = (const u16*)d_in[1]; p.v = (const u16*)d_in[2]; p.shk = (const u16*)d_in[3];
    p.ssm = (const u16*)d_in[4]; p.Dv = (const u16*)d_in[5]; p.w1 = (const u16*)d_in[7]; p.w2 = (const u16*)d_in[8]; p.gw = (const u16*)d_in[9];
    p.out = (u16*)d_out; p.ws = (char*)d_ws;
    (void)hipFuncSetAttribute((const void*)k_fused, hipFuncAttributeMaxDynamicSharedMemorySize, LDS_BYTES);
    (void)hipFuncSetAttribute((const void*)k_dummy, hipFuncAttributeMaxDynamicSharedMemorySize, 163840);
    k_marker<<<1, 64, 0, stream>>>(p.ws);
    k_dummy<<<256, 512, 0, stream>>>(p.ws);
    k_probe<<<1, 64, 0, stream>>>(p.ws);
    k_fused<<<256, NTHR, LDS_BYTES, stream>>>(p);
}
```

```cpp
#include <hip/hip_runtime.h>
#include <stdint.h>
#include <type_traits>

typedef unsigned short u16;
typedef unsigned v4u __attribute__((ext_vector_type(4)));
typedef unsigned v2u __attribute__((ext_vector_type(2)));
typedef float f32x4 __attribute__((ext_vector_type(4)));
typedef float f32x2 __attribute__((ext_vector_type(2)));
typedef short bf16x8 __attribute__((ext_vector_type(8)));
typedef short s16x4 __attribute__((ext_vector_type(4)));
typedef __bf16 bf16x2_t __attribute__((ext_vector_type(2)));
#define LAS __attribute__((address_space(3)))
#define GAS __attribute__((address_space(1)))
typedef GAS unsigned gu32;
typedef GAS unsigned long long gu64;
#define RLX_AGENT __ATOMIC_RELAXED, __HIP_MEMORY_SCOPE_AGENT

constexpr int S = 2048, D = 640, F = 1536;
constexpr int NTHR = 512;
constexpr unsigned WS_H = 0;
constexpr unsigned WS_KV = WS_H + S * F * 2;
constexpr unsigned WS_Y = WS_KV + S * D * 2;
constexpr unsigned WS_G = WS_Y + S * D * 2;
constexpr unsigned WS_CTL = WS_G + S * 4;
constexpr int FL = 32;
constexpr int W_EPOCH = 0, W_H = 1, W_KV = W_H + 192, W_G = W_KV + 64, W_Y = W_G + 128, W_LINES = W_Y + 96;
constexpr unsigned WS_END = WS_CTL + W_LINES * 128;
constexpr int AUX_SC1 = 16;
constexpr int H_AUX = 16;
constexpr unsigned kSpinCap = 1u << 18;

struct Params {
    const u16 *q, *k, *v, *shk, *ssm, *Dv, *w1, *w2, *gw;
    u16* out;
    char* ws;
};

__device__ __forceinline__ float bf_lo(unsigned u) { return __uint_as_float(u << 16); }
__device__ __forceinline__ float bf_hi(unsigned u) { return __uint_as_float(u & 0xffff0000u); }
__device__ __forceinline__ f32x2 bf_2(unsigned u) { return (f32x2){bf_lo(u), bf_hi(u)}; }
__device__ __forceinline__ unsigned pk2(f32x2 v) { return __builtin_bit_cast(unsigned, __builtin_convertvector(v, bf16x2_t)); }
__device__ __forceinline__ unsigned pk_bf16(float lo, float hi) { return pk2((f32x2){lo, hi}); }
__device__ __forceinline__ float rbf(float x) { return bf_lo(pk_bf16(x, x)); }
__device__ __forceinline__ f32x4 mfma16(v4u a, v4u b, f32x4 c) { return __builtin_amdgcn_mfma_f32_16x16x32_bf16(__builtin_bit_cast(bf16x8, a), __builtin_bit_cast(bf16x8, b), c, 0, 0, 0); }
__device__ __forceinline__ v2u tr_read(LAS char* p) { return __builtin_bit_cast(v2u, __builtin_amdgcn_ds_read_tr16_b64_v4i16((LAS s16x4*)p)); }
__device__ __forceinline__ v4u lds_rd16(LAS char* p) { return *(LAS v4u*)p; }
__device__ __forceinline__ void glds16(unsigned voff, const void* sbase, unsigned lds_dst) {
    asm volatile("s_mov_b32 m0, %2\n\ts_nop 0\n\tglobal_load_lds_dwordx4 %0, %1" :: "v"(voff), "s"(sbase), "s"(lds_dst) : "memory");
}
template <bool NT> __device__ __forceinline__ void glds16t(unsigned voff, const void* sbase, unsigned lds_dst) {
    if (NT) asm volatile("s_mov_b32 m0, %2\n\ts_nop 0\n\tglobal_load_lds_dwordx4 %0, %1 nt" :: "v"(voff), "s"(sbase), "s"(lds_dst) : "memory");
    else glds16(voff, sbase, lds_dst);
}
#define NT_X 0
#define NT_W1 0
#define NT_W2 0
#define NT_KV 0
#define NT_SSM 0
template <bool NT> __device__ __forceinline__ v4u gld16(const void* ptr) {
    if (NT) return __builtin_nontemporal_load((const GAS v4u*)ptr);
    return *(const GAS v4u*)ptr;
}
#define WAIT_VM(n) asm volatile("s_waitcnt vmcnt(%0)" :: "n"(n) : "memory")
#define WAIT_LGKM0() asm volatile("s_waitcnt lgkmcnt(0)" ::: "memory")
#define BAR() do { asm volatile("" ::: "memory"); __builtin_amdgcn_s_barrier(); asm volatile("" ::: "memory"); } while (0)
#define SCHED() __builtin_amdgcn_sched_barrier(0)
#define A_MFMA(acc, a, b) asm volatile("v_mfma_f32_16x16x32_bf16 %0, %1, %2, %0" : "+v"(acc) : "v"(a), "v"(b))
#define A_RD128(dst, addr, off) asm volatile("ds_read_b128 %0, %1 offset:%2" : "=v"(dst) : "v"(addr), "n"(off))
#define A_RDTR(dst, addr, off) asm volatile("ds_read_b64_tr_b16 %0, %1 offset:%2" : "=v"(dst) : "v"(addr), "n"(off))
#define A_LGKM(n) asm volatile("s_waitcnt lgkmcnt(%0)" :: "n"(n))
#define A_MFMA_DRAIN() asm volatile("s_nop 15\n\ts_nop 7" ::: "memory")
template <int NM, int NR, int PER> __device__ __forceinline__ void interleave() {
#pragma unroll
    for (int i = 0; i < NM; ++i) {
        __builtin_amdgcn_sched_group_barrier(0x8, 1, 0);
        if (i * PER < NR) __builtin_amdgcn_sched_group_barrier(0x100, PER, 0);
    }
}
#define STAMP(id) do {} while (0)
#define STAMP_ARG
#define STAMP_PASS
#define STAMP_FLUSH() do {} while (0)

__device__ __forceinline__ void st16_sc1(v4u v, __amdgpu_buffer_rsrc_t rs, unsigned off) { __builtin_amdgcn_raw_buffer_store_b128(v, rs, off, 0, AUX_SC1); }
__device__ __forceinline__ void poll_flags(gu32* ctl, int line, int n, unsigned tag, int lane) {
    gu32* f = ctl + (line + (lane < n ? lane : 0)) * FL;
#pragma unroll 1
    for (unsigned spins = 0; spins < kSpinCap; ++spins) {
        const unsigned v = __hip_atomic_load(f, RLX_AGENT);
        if (__all(v == tag)) break;
        __builtin_amdgcn_s_sleep(4);
    }
    __builtin_amdgcn_fence(__ATOMIC_ACQUIRE, "wavefront");
}
__device__ __forceinline__ void set_flag(gu32* ctl, int line, unsigned tag) { __hip_atomic_store(ctl + line * FL, tag, RLX_AGENT); }
__device__ __forceinline__ unsigned silu2f(f32x2 h) {
    const f32x2 t = h * -1.44269504f;
    const f32x2 den = (f32x2){__builtin_amdgcn_exp2f(t.x), __builtin_amdgcn_exp2f(t.y)} + 1.f;
    const f32x2 r = (f32x2){__builtin_amdgcn_rcpf(den.x), __builtin_amdgcn_rcpf(den.y)};
    return pk2(h * r);
}
__device__ __forceinline__ unsigned silu2(unsigned hb) { return silu2f(bf_2(hb)); }
__device__ __forceinline__ float row_sum16(float v) {
    v += __builtin_bit_cast(float, __builtin_amdgcn_update_dpp(0, __builtin_bit_cast(int, v), 0x128, 0xf, 0xf, false));
    v += __builtin_bit_cast(float, __builtin_amdgcn_update_dpp(0, __builtin_bit_cast(int, v), 0x124, 0xf, 0xf, false));
    v += __builtin_bit_cast(float, __builtin_amdgcn_update_dpp(0, __builtin_bit_cast(int, v), 0x122, 0xf, 0xf, false));
    v += __builtin_bit_cast(float, __builtin_amdgcn_update_dpp(0, __builtin_bit_cast(int, v), 0x121, 0xf, 0xf, false));
    return v;
}

constexpr int G1_SLOT = 32768, G1_NS = 5, G1_NST = D / 64, G1_STG = 3 * G1_SLOT;
constexpr int G2_NST = F / 64, G2_RING = 40 * 4096, G2_HD = 8;
constexpr int G2_Q = 8 * 4096, G2_Y = 12 * 4096, G2_G = 16 * 4096;
constexpr int CV_COPY = 4160, CV_X = 4 * CV_COPY, CV_PAIR = CV_X + 4608;
constexpr int CV_EX = 4 * CV_PAIR;
constexpr int LDS_BYTES = 163840;
static_assert(CV_EX + 4 * 8192 <= LDS_BYTES, "LDS map");

struct W2Src { unsigned vw[4]; const char* base; int kb0; unsigned aw[4]; };
__device__ __forceinline__ void w2_init(W2Src& s, const Params& p, int ct, int off2, int lane) {
#pragma unroll
    for (int i = 0; i < 4; ++i) {
        const int r = lane >> 3, c = lane & 7, k = 8 * i + r;
        s.vw[i] = (unsigned)(k * (D * 2) + ((c ^ (((k >> 1) & 3) << 1)) * 16));
    }
    const int g = lane >> 4, q = (lane & 15) >> 2, pp = lane & 3, sw = ((g & 1) << 1) | (q >> 1);
#pragma unroll
    for (int dt = 0; dt < 4; ++dt) s.aw[dt] = (4 * g + q) * 128 + ((dt ^ sw) * 32) + 8 * pp;
    s.base = (const char*)p.w2 + ct * 128; s.kb0 = 2 * off2;
}
__device__ __forceinline__ void w2_issue(const W2Src& s, int b) {
    const int kb = s.kb0 + b >= 48 ? s.kb0 + b - 48 : s.kb0 + b;
    const char* src = s.base + (size_t)kb * (32 * D * 2);
    const unsigned slot = (unsigned)(b >= 40 ? b - 40 : b) * 4096;
#pragma unroll
    for (int i = 0; i < 4; ++i) glds16t<NT_W2>(s.vw[i], src, slot + i * 1024);
}
__device__ __forceinline__ void w2_repack(const W2Src& s, LAS char* L, int b, int lane) {
    LAS char* blk = L + (b >= 40 ? b - 40 : b) * 4096;
    v4u f[4];
#pragma unroll
    for (int dt = 0; dt < 4; ++dt) { const v2u lo = tr_read(blk + s.aw[dt]), hi = tr_read(blk + s.aw[dt] + 2048); f[dt] = (v4u){lo.x, lo.y, hi.x, hi.y}; }
    WAIT_LGKM0();
#pragma unroll
    for (int dt = 0; dt < 4; ++dt) *(LAS v4u*)(blk + dt * 1024 + lane * 16) = f[dt];
    WAIT_LGKM0();
}

template <bool TYPE_A>
__device__ __forceinline__ void g1_loader(const Params& p, int panel, int ct, int off1, const W2Src& w2s, int lw, int lane STAMP_ARG) {
    unsigned vx[4], vw[4];
#pragma unroll
    for (int i = 0; i < 4; ++i) {
        const int r = lane >> 3, c = lane & 7, row = 32 * lw + 8 * i + r;
        vx[i] = (unsigned)(row * (D * 2) + ((c ^ ((row >> 1) & 7)) * 16));
        const int r2 = lane >> 4, c2 = lane & 15, k = 16 * lw + 4 * i + r2, phi = (k & 3) | (((k >> 3) & 1) << 2);
        vw[i] = (unsigned)(k * (F * 2) + ((c2 ^ (phi << 1)) * 16));
    }
    const char* xb = (const char*)p.q + (size_t)panel * 128 * D * 2;
    const char* wb = (const char*)p.w1 + (size_t)ct * 256;
    int kst = off1;
    auto issue = [&](int slot_i) {
        const unsigned slot = (unsigned)slot_i * G1_SLOT;
        const char* xs = xb + kst * 128; const char* wsrc = wb + (size_t)kst * (64 * F * 2);
#pragma unroll
        for (int i = 0; i < 4; ++i) glds16t<NT_X>(vx[i], xs, slot + (32 * lw + 8 * i) * 128);
#pragma unroll
        for (int i = 0; i < 4; ++i) glds16t<NT_W1>(vw[i], wsrc, slot + 16384 + (16 * lw + 4 * i) * 256);
        kst = kst + 1 == G1_NST ? 0 : kst + 1;
    };
#pragma unroll
    for (int st = 0; st < 3; ++st) issue(st);
    WAIT_VM(16); BAR(); issue(3); issue(4);
#pragma unroll 1
    for (int i = 1; i < 6; ++i) { WAIT_VM(24); BAR(); issue(i - 1); }
    STAMP(1);
    if (TYPE_A) {
        WAIT_VM(24); BAR(); w2_issue(w2s, 0 + lw); w2_issue(w2s, 4 + lw);
        WAIT_VM(24); BAR(); w2_issue(w2s, 8 + lw); w2_issue(w2s, 12 + lw);
        WAIT_VM(24); BAR(); w2_issue(w2s, 16 + lw); w2_issue(w2s, 20 + lw);
        WAIT_VM(24); BAR(); w2_issue(w2s, 28 + lw);
    } else {
        WAIT_VM(24); BAR(); WAIT_VM(16); BAR(); WAIT_VM(8); BAR(); WAIT_VM(0); BAR();
    }
}
__device__ __forceinline__ void g1_store(__amdgpu_buffer_rsrc_t rs, int stg, int ksg, int lane, v4u hb) {
    const v4u o = {silu2(hb.x), silu2(hb.y), silu2(hb.z), silu2(hb.w)};
    st16_sc1(o, rs, WS_H + (stg * 48 + ksg) * 1024 + lane * 16);
}
struct ConvIn { v4u v[2], vp[2], x[2]; unsigned vq[2]; float Dd; };
__device__ __forceinline__ void conv_load_h(const Params& p, ConvIn& c, int ch, int hf, int lane) {
    const char* hsrc = (const char*)p.ssm + (size_t)ch * (S * 2);
#pragma unroll
    for (int j = 0; j < 2; ++j) {
        const int o = (2 * hf + j) * 1024 + lane * 16;
        c.v[j] = gld16<NT_SSM>(hsrc + o);
        c.vp[j] = gld16<NT_SSM>(hsrc + (o < 4 ? 0 : o - 4));
        c.vq[j] = *(const GAS unsigned*)(hsrc + (o < 8 ? 0 : o - 8));
    }
    c.Dd = bf_lo((unsigned)p.Dv[ch]);
}
__device__ __forceinline__ void conv_load_x(ConvIn& c, __amdgpu_buffer_rsrc_t rs, int ch, int hf, int lane) {
#pragma unroll
    for (int j = 0; j < 2; ++j) c.x[j] = __builtin_amdgcn_raw_buffer_load_b128(rs, (2 * hf + j) * 1024 + lane * 16, WS_KV + ch * (S * 2), AUX_SC1);
}
constexpr int CV_SYNC = 4128;
template <bool TYPE_A>
__device__ __forceinline__ void g1_compute(const Params& p, LAS char* L, __amdgpu_buffer_rsrc_t rs, int panel, int ct, int w, int lane, ConvIn& cin, int ch STAMP_ARG) {
    const int wf = w & 1, wsx = w >> 1;
    const int g = lane >> 4, q = (lane & 15) >> 2, pp = lane & 3, s = lane & 15;
    const int phi = q | ((g & 1) << 2);
    unsigned aw[4], ax[2];
#pragma unroll
    for (int ft = 0; ft < 4; ++ft) aw[ft] = 16384 + (8 * g + q) * 256 + (((4 * wf + ft) ^ phi) * 32) + 8 * pp;
#pragma unroll
    for (int ks = 0; ks < 2; ++ks) ax[ks] = (64 * wsx + s) * 128 + (((4 * ks + g) ^ ((s >> 1) & 7)) * 16);
    f32x4 acc[4][4];
#pragma unroll
    for (int a = 0; a < 4; ++a)
#pragma unroll
        for (int b = 0; b < 4; ++b) acc[a][b] = (f32x4){0.f, 0.f, 0.f, 0.f};
    v4u A[2][4], B[2][4];
#pragma unroll
    for (int a = 0; a < 4; ++a) { A[1][a] = (v4u){0u, 0u, 0u, 0u}; B[1][a] = (v4u){0u, 0u, 0u, 0u}; }
    auto rd = [&](int set, LAS char* base, int ks) {
#pragma unroll
        for (int ft = 0; ft < 4; ++ft) {
            const v2u lo = tr_read(base + aw[ft] + ks * 8192), hi = tr_read(base + aw[ft] + ks * 8192 + 1024);
            A[set][ft] = (v4u){lo.x, lo.y, hi.x, hi.y};
            if (ft == 0) {
#pragma unroll
                for (int sx = 0; sx < 4; ++sx) B[set][sx] = lds_rd16(base + ax[ks] + sx * 2048);
            }
        }
    };
    auto mm = [&](int set) {
#pragma unroll
        for (int ft = 0; ft < 4; ++ft)
#pragma unroll
            for (int sx = 0; sx < 4; ++sx) acc[ft][sx] = mfma16(A[set][ft], B[set][sx], acc[ft][sx]);
    };
    unsigned slot = 0;
#define X_G1REP 1
#pragma unroll 1
    for (int i = 0; i < G1_NST * X_G1REP; ++i) {
        LAS char* base = L + slot;
        slot = slot + G1_SLOT == G1_NS * G1_SLOT ? 0 : slot + G1_SLOT;
        BAR();
        if (i == 0) STAMP(1);
        rd(0, base, 0);
        mm(1);
        interleave<16, 12, 1>();
        SCHED();
        rd(1, base, 1);
        mm(0);
        interleave<16, 12, 1>();
        SCHED();
        WAIT_LGKM0();
    }
    mm(1);
    STAMP(2);
    if (!TYPE_A) conv_load_h(p, cin, ch, w & 1, lane);
    SCHED();
#pragma unroll
    for (int j = 0; j < 4; ++j) {
        const f32x4 a = acc[2 * (j & 1)][2 + (j >> 1)], b = acc[2 * (j & 1) + 1][2 + (j >> 1)];
        *(LAS v4u*)(L + G1_STG + w * 4096 + j * 1024 + lane * 16) = (v4u){pk_bf16(a[0], a[1]), pk_bf16(a[2], a[3]), pk_bf16(b[0], b[1]), pk_bf16(b[2], b[3])};
    }
    if (!TYPE_A && lane < 8) *(LAS unsigned*)(L + w * CV_PAIR + CV_SYNC + 4 * lane) = 0u;
    WAIT_LGKM0();
    BAR();
    if (!TYPE_A) conv_load_x(cin, rs, ch, w & 1, lane);
    SCHED();
#pragma unroll
    for (int j = 0; j < 4; ++j) {
        const f32x4 a = acc[2 * (j & 1)][j >> 1], b = acc[2 * (j & 1) + 1][j >> 1];
        const v4u o = {silu2f((f32x2){a[0], a[1]}), silu2f((f32x2){a[2], a[3]}), silu2f((f32x2){b[0], b[1]}), silu2f((f32x2){b[2], b[3]})};
        st16_sc1(o, rs, WS_H + ((panel * 8 + wsx * 4 + (j >> 1)) * 48 + ct * 4 + wf * 2 + (j & 1)) * 1024 + lane * 16);
    }
}
__device__ __forceinline__ void g1_loader_epilogue(LAS char* L, __amdgpu_buffer_rsrc_t rs, int panel, int ct, int lw, int lane) {
    const int wf = lw & 1, wsx = lw >> 1;
    v4u hb[4];
#pragma unroll
    for (int j = 0; j < 4; ++j) hb[j] = *(LAS v4u*)(L + G1_STG + lw * 4096 + j * 1024 + lane * 16);
#pragma unroll
    for (int j = 0; j < 4; ++j) g1_store(rs, panel * 8 + wsx * 4 + 2 + (j >> 1), ct * 4 + wf * 2 + (j & 1), lane, hb[j]);
}

__device__ __forceinline__ void prepass(const Params& p, LAS char* L, __amdgpu_buffer_rsrc_t rs, int wg, int tid STAMP_ARG) {
    const int s0 = 64 * (wg >> 1), c0 = 320 * (wg & 1);
    {
        const int w = __builtin_amdgcn_readfirstlane(tid >> 6);
        const int item = w < 4 ? tid : 256 + (tid & 63);
        const int cc = item % 40, rg = item / 40;
        const int ch = c0 + 8 * cc, r0 = s0 + 8 * rg;
        v4u kk[11], vv[8], sw[4];
#pragma unroll
        for (int j = 0; j < 4; ++j) sw[j] = *(const GAS v4u*)((const char*)p.shk + (size_t)ch * 8 + j * 16);
#pragma unroll
        for (int j = 0; j < 11; ++j) {
            const int row = r0 - 3 + j;
            kk[j] = gld16<NT_KV>((const char*)p.k + (size_t)(row < 0 ? 0 : row) * (D * 2) + ch * 2);
        }
#pragma unroll
        for (int j = 0; j < 8; ++j) vv[j] = gld16<NT_KV>((const char*)p.v + (size_t)(r0 + j) * (D * 2) + ch * 2);
        if (r0 == 0) { kk[0] = (v4u){0u, 0u, 0u, 0u}; kk[1] = kk[0]; kk[2] = kk[0]; }
#pragma unroll
        for (int e = 0; e < 4; ++e) {
            if (w >= 4 && w - 4 != e) continue;
            const f32x2 W0 = {bf_lo(sw[e].x), bf_lo(sw[e].z)}, W1 = {bf_hi(sw[e].x), bf_hi(sw[e].z)};
            const f32x2 W2 = {bf_lo(sw[e].y), bf_lo(sw[e].w)}, W3 = {bf_hi(sw[e].y), bf_hi(sw[e].w)};
            f32x2 kf[11], o[8];
#pragma unroll
            for (int j = 0; j < 11; ++j) kf[j] = bf_2(kk[j][e]);
#pragma unroll
            for (int j = 0; j < 8; ++j) {
                const f32x2 ksh = kf[j] * W3 + kf[j + 1] * W2 + kf[j + 2] * W1 + kf[j + 3] * W0;
                o[j] = ksh * bf_2(vv[j][e]);
            }
            const v4u p0 = {pk_bf16(o[0].x, o[1].x), pk_bf16(o[2].x, o[3].x), pk_bf16(o[4].x, o[5].x), pk_bf16(o[6].x, o[7].x)};
            const v4u p1 = {pk_bf16(o[0].y, o[1].y), pk_bf16(o[2].y, o[3].y), pk_bf16(o[4].y, o[5].y), pk_bf16(o[6].y, o[7].y)};
            *(LAS v4u*)(L + (8 * cc + 2 * e) * 128 + ((rg ^ (cc & 7)) * 16)) = p0;
            *(LAS v4u*)(L + (8 * cc + 2 * e + 1) * 128 + ((rg ^ (cc & 7)) * 16)) = p1;
        }
    }
    STAMP(1);
    WAIT_LGKM0();
    BAR();
#pragma unroll
    for (int j = 0; j < 5; ++j) {
        const int n = tid + NTHR * j, ch = n >> 3, pc = n & 7;
        const v4u val = *(LAS v4u*)(L + ch * 128 + ((pc ^ ((ch >> 3) & 7)) * 16));
        st16_sc1(val, rs, WS_KV + (c0 + ch) * (S * 2) + s0 * 2 + pc * 16);
    }
}
struct GateIn { v4u gw[5], q[5]; };
__device__ __forceinline__ void gate_load(const Params& p, GateIn& gi, int row0, int lane) {
    const int i = lane & 15, row = row0 + (lane >> 4);
#pragma unroll
    for (int j = 0; j < 5; ++j) {
        gi.gw[j] = *(const GAS v4u*)((const char*)p.gw + (i + 16 * j) * 16);
        gi.q[j] = *(const GAS v4u*)((const char*)p.q + (size_t)row * (D * 2) + (i + 16 * j) * 16);
    }
}
__device__ __forceinline__ void gate_finish(const GateIn& gi, __amdgpu_buffer_rsrc_t rs, int row0, int lane) {
    f32x2 a2 = {0.f, 0.f};
#pragma unroll
    for (int j = 0; j < 5; ++j)
#pragma unroll
        for (int e = 0; e < 4; ++e) a2 += bf_2(gi.q[j][e]) * bf_2(gi.gw[j][e]);
    const float z = rbf(row_sum16(a2.x + a2.y));
    const float gv = rbf(__builtin_amdgcn_rcpf(1.f + __builtin_amdgcn_exp2f(z * -1.44269504f)));
    if ((lane & 15) == 0) __builtin_amdgcn_raw_buffer_store_b32(__float_as_uint(gv), rs, WS_G + (row0 + (lane >> 4)) * 4, 0, AUX_SC1);
}

__device__ __forceinline__ void pair_sync(LAS char* words, int hf) {
    WAIT_LGKM0();
    *(volatile LAS unsigned*)(words + 4 * hf) = 1u;
#pragma unroll 1
    for (unsigned spins = 0; spins < (1u << 22); ++spins) { if (__builtin_amdgcn_readfirstlane(*(volatile LAS unsigned*)(words + 4 * (1 - hf))) != 0u) break; }
    asm volatile("" ::: "memory");
}
template <int hf>
__device__ __forceinline__ void conv_half(const Params& p, LAS char* Lw, LAS char* Lx, __amdgpu_buffer_rsrc_t rs, ConvIn& c, int ch, int chn, int round, int lane STAMP_ARG) {
    const int g = lane >> 4, q = (lane & 15) >> 2, pp = lane & 3, n = lane & 15;
    if (hf == 0) {
        if (lane < 30) *(LAS v4u*)(Lw + CV_X + lane * 16) = (v4u){0u, 0u, 0u, 0u};
        if (lane < 8) *(LAS v4u*)(Lw + (lane >> 1) * CV_COPY + (lane & 1) * 16) = (v4u){0u, 0u, 0u, 0u};
        if (lane == 0) { c.vp[0] = (v4u){0u, c.v[0].x, c.v[0].y, c.v[0].z}; c.vq[0] = 0u; }
    }
#pragma unroll
    for (int j = 0; j < 2; ++j) {
        const v4u v = c.v[j], vp = c.vp[j];
        const unsigned a0 = __builtin_amdgcn_alignbit(v.x, vp.x, 16), a1 = __builtin_amdgcn_alignbit(v.y, v.x, 16), a2 = __builtin_amdgcn_alignbit(v.z, v.y, 16), a3 = __builtin_amdgcn_alignbit(v.w, v.z, 16);
        const unsigned am = __builtin_amdgcn_alignbit(vp.x, c.vq[j], 16);
        LAS char* d = Lw + 32 + (2 * hf + j) * 1024 + lane * 16;
        *(LAS v4u*)(d) = v;
        *(LAS v4u*)(d + CV_COPY) = (v4u){a0, a1, a2, a3};
        *(LAS v4u*)(d + 2 * CV_COPY) = vp;
        *(LAS v4u*)(d + 3 * CV_COPY) = (v4u){am, a0, a1, a2};
        *(LAS v4u*)(Lw + CV_X + 480 + (2 * hf + j) * 1024 + lane * 16) = c.x[j];
    }
    const float Dd = c.Dd;
    pair_sync(Lw + CV_SYNC + 16 * round, hf);
    if (chn >= 0) { conv_load_h(p, c, chn, hf, lane); conv_load_x(c, rs, chn, hf, lane); asm volatile("" ::: "memory"); }
    if (round == 0) STAMP(8); else STAMP(12);
    const unsigned la = (unsigned)(size_t)Lw + q * CV_COPY + 32 - 16 * g + 8 * pp + 24 + 256 * hf;
    const unsigned lb = (unsigned)(size_t)Lw + CV_X + 32 * n + 16 * g + 448 - 256 * hf - 192;
    f32x4 acc[8];
#pragma unroll
    for (int t = 0; t < 8; ++t) acc[t] = (f32x4){0.f, 0.f, 0.f, 0.f};
    v2u Alo[2][8], Ahi[2][8];
    v4u B[2][8];
#define CV_RD(set, rs_, k) do { \
        if ((k) % 3 == 0) A_RDTR(Alo[set][(k) / 3], la, 64 * (rs_) + 512 * ((k) / 3) + 8); \
        else if ((k) % 3 == 1) A_RDTR(Ahi[set][(k) / 3], la, 64 * (rs_) + 512 * ((k) / 3)); \
        else A_RD128(B[set][(k) / 3], lb, 192 - 64 * (rs_) + 512 * ((k) / 3)); } while (0)
    auto step = [&](auto SET, auto NEXT) {
        constexpr int set = decltype(SET)::value, nx = decltype(NEXT)::value;
        int k = 0;
#pragma unroll
        for (int l = 0; l < 8; ++l)
#pragma unroll
            for (int i = 0; i + l < 8; ++i) {
                const v4u a = {Alo[set][i].x, Alo[set][i].y, Ahi[set][i].x, Ahi[set][i].y};
                A_MFMA(acc[i + l], a, B[set][l]);
                if (nx >= 0 && k < 24) { CV_RD(1 - set, (nx >= 0 ? nx : 0), k); }
                ++k;
            }
        if (nx >= 0) A_LGKM(0);
    };
#pragma unroll
    for (int k = 0; k < 24; ++k) CV_RD(0, 0, k);
    A_LGKM(0);
    step(std::integral_constant<int, 0>{}, std::integral_constant<int, 1>{});
    step(std::integral_constant<int, 1>{}, std::integral_constant<int, 2>{});
    step(std::integral_constant<int, 0>{}, std::integral_constant<int, 3>{});
    step(std::integral_constant<int, 1>{}, std::integral_constant<int, -1>{});
#undef CV_RD
    A_MFMA_DRAIN();
    if (round == 0) STAMP(9); else STAMP(13);
#pragma unroll
    for (int t = 0; t < 4; ++t) *(LAS f32x4*)(Lx + hf * 4096 + t * 1024 + lane * 16) = acc[4 * (1 - hf) + t];
    v2u xv[4];
#pragma unroll
    for (int t = 0; t < 4; ++t) xv[t] = *(LAS v2u*)(Lw + CV_X + 480 + 2 * (1024 * hf + 256 * t + 16 * n + 4 * g));
    pair_sync(Lw + CV_SYNC + 16 * round + 8, hf);
    f32x4 o[4];
#pragma unroll
    for (int t = 0; t < 4; ++t) o[t] = *(LAS f32x4*)(Lx + (1 - hf) * 4096 + t * 1024 + lane * 16);
    v2u ov[4];
#pragma unroll
    for (int t = 0; t < 4; ++t) {
        const f32x4 y = acc[4 * hf + t] + o[t];
        ov[t] = (v2u){pk2(bf_2(xv[t].x) * Dd + (f32x2){y[0], y[1]}), pk2(bf_2(xv[t].y) * Dd + (f32x2){y[2], y[3]})};
    }
    LAS char* Ly = Lx + (1 - hf) * 4096;
#pragma unroll
    for (int t = 0; t < 4; ++t) *(LAS v2u*)(Ly + 2 * (256 * t + 16 * n + 4 * g)) = ov[t];
    WAIT_LGKM0();
#pragma unroll
    for (int j = 0; j < 2; ++j) {
        const v4u val = *(LAS v4u*)(Ly + j * 1024 + lane * 16);
        st16_sc1(val, rs, WS_Y + ch * (S * 2) + hf * 2048 + j * 1024 + lane * 16);
    }
    if (round == 0) STAMP(10); else STAMP(14);
}

__device__ __forceinline__ void conv_rounds(const Params& p, LAS char* L, __amdgpu_buffer_rsrc_t rs, ConvIn& c, int ch, int chn, int pr, int hf, int lane STAMP_ARG) {
    LAS char* Lw = L + pr * CV_PAIR; LAS char* Lx = L + CV_EX + pr * 8192;
#pragma unroll 1
    for (int round = 0; round < 2; ++round) {
        if (hf) conv_half<1>(p, Lw, Lx, rs, c, ch, chn, round, lane STAMP_PASS); else conv_half<0>(p, Lw, Lx, rs, c, ch, chn, round, lane STAMP_PASS);
        if (chn < 0) break;
        ch = chn; chn = -1;
    }
}

__device__ __forceinline__ void g2_loader(const Params& p, LAS char* L, __amdgpu_buffer_rsrc_t rs, unsigned tag, const W2Src& w2s, int panel, int ct, int lw, int lane STAMP_ARG) {
    gu32* ctl = (gu32*)(p.ws + WS_CTL);
    const int grow = panel * 128 + (ct & 7) * 16 + 4 * lw;
    if (lw == 0) poll_flags(ctl, W_H + panel * 12, 12, tag, lane);
    w2_issue(w2s, 24 + lw);
    GateIn gi;
    gate_load(p, gi, grow, lane);
    asm volatile("" ::: "memory");
    w2_repack(w2s, L, 0 + lw, lane); w2_repack(w2s, L, 4 + lw, lane);
    STAMP(5);
    BAR();
    w2_repack(w2s, L, 8 + lw, lane); w2_repack(w2s, L, 12 + lw, lane);
    if (ct < 8) gate_finish(gi, rs, grow, lane);
    else WAIT_VM(0);
    BAR();
    w2_issue(w2s, 40 + lw); w2_issue(w2s, 44 + lw);
    w2_repack(w2s, L, 16 + lw, lane); w2_repack(w2s, L, 20 + lw, lane);
    WAIT_VM(8);
    BAR();
    if (ct < 8 && lw == 0 && lane == 0) set_flag(ctl, W_G + panel * 8 + ct, tag);
#pragma unroll
    for (int i = 0; i < 4; ++i) {
        const int r = lane >> 3, c = lane & 7, row = 32 * lw + 8 * i + r;
        glds16((unsigned)(row * (D * 2) + ((c ^ ((row >> 1) & 7)) * 16)), (const char*)p.q + (size_t)panel * 128 * D * 2 + ct * 128, G2_Q + (32 * lw + 8 * i) * 128);
    }
#pragma unroll 1
    for (int b = 24; b < 40; b += 4) w2_repack(w2s, L, b + lw, lane);
    BAR();
    WAIT_VM(4);
    w2_repack(w2s, L, 40 + lw, lane); w2_repack(w2s, L, 44 + lw, lane);
    BAR();
    const int y0 = ct < 7 ? (64 * ct) / 7 : 64 + (64 * ct - 448) / 6, y1 = ct < 7 ? (64 * ct + 63) / 7 : 64 + (64 * ct + 63 - 448) / 6;
    poll_flags(ctl, W_Y + y0, y1 - y0 + 1, tag, lane);
    poll_flags(ctl, W_G + panel * 8, 8, tag, lane);
    STAMP(6);
    v4u yv[4];
#pragma unroll
    for (int i = 0; i < 4; ++i) {
        const int r = lane >> 4, c = lane & 15, d = 16 * lw + 4 * i + r;
        yv[i] = __builtin_amdgcn_raw_buffer_load_b128(rs, d * (S * 2) + c * 16, WS_Y + ct * 64 * S * 2 + panel * 256, AUX_SC1);
    }
    const unsigned gv = __builtin_amdgcn_raw_buffer_load_b32(rs, (lw * 32 + (lane & 31)) * 4, WS_G + panel * 512, AUX_SC1);
#pragma unroll
    for (int i = 0; i < 4; ++i) {
        const int r = lane >> 4, c = lane & 15, d = 16 * lw + 4 * i + r;
        *(LAS v4u*)(L + G2_Y + d * 256 + ((c ^ ((d & 7) << 1)) * 16)) = yv[i];
    }
    if (lane < 32) *(LAS unsigned*)(L + G2_G + (lw * 32 + lane) * 4) = gv;
    WAIT_VM(0);
    WAIT_LGKM0();
    BAR();
}
__device__ __forceinline__ void g2_compute(const Params& p, LAS char* L, __amdgpu_buffer_rsrc_t rs, int panel, int ct, int off2, int w, int lane STAMP_ARG) {
    const int g = lane >> 4, q = (lane & 15) >> 2, pp = lane & 3, s = lane & 15;
    const unsigned hbase = WS_H + (panel * 8 + 2 * w) * (48 * 1024);
    int kld = off2;
    f32x4 acc[4][2];
#pragma unroll
    for (int a = 0; a < 4; ++a) { acc[a][0] = (f32x4){0.f, 0.f, 0.f, 0.f}; acc[a][1] = (f32x4){0.f, 0.f, 0.f, 0.f}; }
    v4u H[G2_HD][2][2];
    auto ldh = [&](int slot) {
#pragma unroll
        for (int ks = 0; ks < 2; ++ks)
#pragma unroll
            for (int sx = 0; sx < 2; ++sx) H[slot][ks][sx] = __builtin_amdgcn_raw_buffer_load_b128(rs, lane * 16 + sx * (48 * 1024) + ks * 1024, hbase + kld * 2048, H_AUX);
        kld = kld + 1 == G2_NST ? 0 : kld + 1;
    };
    BAR();
    STAMP(5);
#pragma unroll
    for (int st = 0; st < G2_HD - 1; ++st) ldh(st);
#pragma unroll
    for (int a = 0; a < 4; ++a) H[G2_HD - 1][a >> 1][a & 1] = (v4u){0u, 0u, 0u, 0u};
    SCHED();
    v4u A[2][4];
#pragma unroll
    for (int a = 0; a < 4; ++a) A[1][a] = (v4u){0u, 0u, 0u, 0u};
    auto half = [&](int set, int slot, int ks, unsigned addr, int off) {
        A_LGKM(3);
        A_MFMA(acc[0][0], A[set][0], H[slot][ks][0]); A_RD128(A[1 - set][0], addr, off);
        A_MFMA(acc[0][1], A[set][0], H[slot][ks][1]); A_RD128(A[1 - set][1], addr, off + 1024);
        A_LGKM(4);
        A_MFMA(acc[1][0], A[set][1], H[slot][ks][0]); A_RD128(A[1 - set][2], addr, off + 2048);
        A_MFMA(acc[1][1], A[set][1], H[slot][ks][1]); A_RD128(A[1 - set][3], addr, off + 3072);
        A_LGKM(5);
        A_MFMA(acc[2][0], A[set][2], H[slot][ks][0]);
        A_MFMA(acc[2][1], A[set][2], H[slot][ks][1]);
        A_LGKM(4);
        A_MFMA(acc[3][0], A[set][3], H[slot][ks][0]);
        A_MFMA(acc[3][1], A[set][3], H[slot][ks][1]);
    };
    const unsigned lbase = (unsigned)(size_t)L + lane * 16;
    unsigned sb = 0;
    auto stage = [&](int u, bool load, bool drain) {
        const unsigned addr = lbase + sb;
        half(1, (u + G2_HD - 1) % G2_HD, 1, addr, 0);
        if (load) { ldh((u + G2_HD - 1) % G2_HD); asm volatile("" ::: "memory"); }
        half(0, u, 0, addr, 4096);
        if (drain) WAIT_LGKM0();
        sb = sb + 8192 == G2_RING ? 0 : sb + 8192;
    };
#pragma unroll 1
    for (int jj = 0; jj < 2; ++jj) {
#pragma unroll
        for (int u = 0; u < 8; ++u) {
            if (u == 4 || (u == 0 && jj == 1)) BAR();
            if (u == 0) { if (jj == 0) STAMP(14); else STAMP(12); }
            stage(u, true, (u & 3) == 3);
        }
    }
    BAR();
    STAMP(13);
#pragma unroll
    for (int u = 0; u < 8; ++u) stage(u, u == 0, false);
    WAIT_LGKM0();
#pragma unroll
    for (int dt = 0; dt < 4; ++dt) { A_MFMA(acc[dt][0], A[1][dt], H[G2_HD - 1][1][0]); A_MFMA(acc[dt][1], A[1][dt], H[G2_HD - 1][1][1]); }
    A_MFMA_DRAIN();
    STAMP(6);
    BAR();
    STAMP(7);
    {
        float gg[2]; v2u yv[2][4], qv[2][4];
#pragma unroll
        for (int sx = 0; sx < 2; ++sx) {
            const int row = 32 * w + 16 * sx + s;
            gg[sx] = *(LAS float*)(L + G2_G + row * 4);
#pragma unroll
            for (int dt = 0; dt < 4; ++dt) {
                const int d = 16 * dt + 4 * g + q;
                yv[sx][dt] = tr_read(L + G2_Y + d * 256 + (((2 * w + sx) ^ (d & 7)) * 32) + 8 * pp);
                qv[sx][dt] = *(LAS v2u*)(L + G2_Q + row * 128 + (((2 * dt + (g >> 1)) ^ ((row >> 1) & 7)) * 16) + 8 * (g & 1));
            }
        }
        SCHED();
#pragma unroll
        for (int sx = 0; sx < 2; ++sx) {
            const int row = 32 * w + 16 * sx + s;
            const float omg = 1.f - gg[sx];
#pragma unroll
            for (int dt = 0; dt < 4; ++dt) {
                const f32x4 a = acc[dt][sx];
                const f32x2 o01 = bf_2(qv[sx][dt].x) * bf_2(yv[sx][dt].x) * gg[sx] + (f32x2){a[0], a[1]} * omg;
                const f32x2 o23 = bf_2(qv[sx][dt].y) * bf_2(yv[sx][dt].y) * gg[sx] + (f32x2){a[2], a[3]} * omg;
                *(LAS v2u*)(L + G2_Q + row * 128 + (((2 * dt + (g >> 1)) ^ ((row >> 1) & 7)) * 16) + 8 * (g & 1)) = (v2u){pk2(o01), pk2(o23)};
            }
        }
    }
    WAIT_LGKM0();
#pragma unroll
    for (int i = 0; i < 4; ++i) {
        const int row = 32 * w + 8 * i + (lane >> 3), c = lane & 7;
        const v4u val = *(LAS v4u*)(L + G2_Q + row * 128 + c * 16);
        *(GAS v4u*)((char*)p.out + (size_t)(panel * 128 + row) * (D * 2) + ct * 128 + ((c ^ ((row >> 1) & 7)) * 16)) = val;
    }
    STAMP(11);
}

__global__ void __launch_bounds__(NTHR, 2) k_fused(Params p) {
    extern __shared__ __attribute__((aligned(16))) char smem[];
    LAS char* L = (LAS char*)smem;
    const int tid = threadIdx.x, lane = tid & 63, w = __builtin_amdgcn_readfirstlane(tid >> 6);
    const int x = blockIdx.x & 7, idx = blockIdx.x >> 3, xr = x >> 1, xc = x & 1;
    const unsigned long long tq0 = __builtin_amdgcn_s_memrealtime();
    gu32* ctl = (gu32*)(p.ws + WS_CTL);
    unsigned epoch_v = __hip_atomic_load(ctl + W_EPOCH * FL, RLX_AGENT);
#define GET_TAG() asm volatile("" : "+v"(epoch_v)); const unsigned tag = __builtin_amdgcn_readfirstlane(epoch_v) + 1u
    const __amdgpu_buffer_rsrc_t rs = __builtin_amdgcn_make_buffer_rsrc(p.ws, 0, WS_END, 0x00020000);
    if (idx < 24) {
        const int panel = 4 * xr + idx / 6, ct = 6 * xc + idx % 6;
        const int off1 = 5 * ((idx / 6 + idx % 6) & 1);
        const bool typeA = idx < 20;
        const int panel2 = 4 * xr + idx / 5, ct2 = 5 * xc + idx % 5;
        const int off2 = 12 * ((idx / 5 + idx % 5) & 1);
        if (typeA) {
            if (w < 4) {
                ConvIn cin;
                g1_compute<true>(p, L, rs, panel, ct, w, lane, cin, 0 STAMP_PASS);
                STAMP(3);
                WAIT_VM(0);
                GET_TAG();
                STAMP(4);
                BAR();
                if (tid == 0) set_flag(ctl, W_H + panel * 12 + ct, tag);
                g2_compute(p, L, rs, panel2, ct2, off2, w, lane STAMP_PASS);
            } else {
                const int lw = w - 4;
                W2Src w2s;
                w2_init(w2s, p, ct2, off2, lane);
                g1_loader<true>(p, panel, ct, off1, w2s, lw, lane STAMP_PASS);
                BAR();
                g1_loader_epilogue(L, rs, panel, ct, lw, lane);
                w2_issue(w2s, 32 + lw); w2_issue(w2s, 36 + lw);
                WAIT_VM(8);
                GET_TAG();
                BAR();
                g2_loader(p, L, rs, tag, w2s, panel2, ct2, lw, lane STAMP_PASS);
            }
        } else {
            const int bw = x * 4 + idx - 20, pr = w >> 1, hf = w & 1;
            const int ch = 448 + 6 * bw + pr, chn = pr < 2 ? ch + 4 : -1;
            ConvIn cin;
            if (w < 4) {
                g1_compute<false>(p, L, rs, panel, ct, w, lane, cin, ch STAMP_PASS);
                STAMP(3);
            } else {
                W2Src w2s;
                g1_loader<false>(p, panel, ct, off1, w2s, w - 4, lane STAMP_PASS);
                conv_load_h(p, cin, ch, hf, lane);
                { GET_TAG(); if (w == 7) poll_flags(ctl, W_KV, 64, tag, lane); }
                BAR();
                conv_load_x(cin, rs, ch, hf, lane);
                g1_loader_epilogue(L, rs, panel, ct, w - 4, lane);
            }
            WAIT_VM(0);
            GET_TAG();
            STAMP(4);
            BAR();
            if (tid == 0) set_flag(ctl, W_H + panel * 12 + ct, tag);
            conv_rounds(p, L, rs, cin, ch, chn, pr, hf, lane STAMP_PASS);
            WAIT_VM(0);
            BAR();
            if (tid == 0) set_flag(ctl, W_Y + 64 + bw, tag);
            STAMP(16);
        }
    } else {
        const int cw = x * 8 + (idx - 24), pr = w >> 1, hf = w & 1;
        const int ch = 7 * cw + pr, chn = pr < 3 ? ch + 4 : -1;
        ConvIn cin;
        conv_load_h(p, cin, ch, hf, lane);
        prepass(p, L, rs, cw, tid STAMP_PASS);
        STAMP(2);
        WAIT_VM(0);
        GET_TAG();
        BAR();
        if (tid == 0) set_flag(ctl, W_KV + cw, tag);
        if (hf == 0 && lane < 8) *(LAS unsigned*)(L + pr * CV_PAIR + CV_SYNC + 4 * lane) = 0u;
        STAMP(3);
        if (w == 1) poll_flags(ctl, W_KV, 64, tag, lane);
        WAIT_LGKM0();
        BAR();
        STAMP(4);
        conv_load_x(cin, rs, ch, hf, lane);
        conv_rounds(p, L, rs, cin, ch, chn, pr, hf, lane STAMP_PASS);
        WAIT_VM(0);
        BAR();
        if (tid == 0) set_flag(ctl, W_Y + cw, tag);
        STAMP(16);
        if (w == 7 && blockIdx.x == 255) {
#pragma unroll 1
            for (int pn = 0; pn < 3; ++pn) poll_flags(ctl, W_H + pn * 64, 64, tag, lane);
            if (lane == 0) set_flag(ctl, W_EPOCH, tag);
        }
    }
    if (blockIdx.x == 0 && w == 0) {
        const unsigned long long t1 = __builtin_amdgcn_s_memrealtime(), dur = (t1 - tq0) * 0ull;
        while (__builtin_amdgcn_s_memrealtime() - t1 < dur && __builtin_amdgcn_s_memrealtime() - t1 < 100000ull) __builtin_amdgcn_s_sleep(1);
    }
}

extern "C" void kernel_launch(void* const* d_in, const int* in_sizes, int n_in, void* d_out, int out_size, void* d_ws, size_t ws_size, hipStream_t stream) {
    Params p;
    p.q = (const u16*)d_in[0]; p.k = (const u16*)d_in[1]; p.v = (const u16*)d_in[2]; p.shk = (const u16*)d_in[3];
    p.ssm = (const u16*)d_in[4]; p.Dv = (const u16*)d_in[5]; p.w1 = (const u16*)d_in[7]; p.w2 = (const u16*)d_in[8]; p.gw = (const u16*)d_in[9];
    p.out = (u16*)d_out; p.ws = (char*)d_ws;
    (void)hipFuncSetAttribute((const void*)k_fused, hipFuncAttributeMaxDynamicSharedMemorySize, LDS_BYTES);
    k_fused<<<256, NTHR, LDS_BYTES, stream>>>(p);
}
```

```cpp
#include <hip/hip_runtime.h>
#include <stdint.h>
#include <type_traits>

typedef unsigned short u16;
typedef unsigned v4u __attribute__((ext_vector_type(4)));
typedef unsigned v2u __attribute__((ext_vector_type(2)));
typedef float f32x4 __attribute__((ext_vector_type(4)));
typedef float f32x2 __attribute__((ext_vector_type(2)));
typedef short bf16x8 __attribute__((ext_vector_type(8)));
typedef short s16x4 __attribute__((ext_vector_type(4)));
typedef __bf16 bf16x2_t __attribute__((ext_vector_type(2)));
#define LAS __attribute__((address_space(3)))
#define GAS __attribute__((address_space(1)))
typedef GAS unsigned gu32;
typedef GAS unsigned long long gu64;
#define RLX_AGENT __ATOMIC_RELAXED, __HIP_MEMORY_SCOPE_AGENT

constexpr int S = 2048, D = 640, F = 1536;
constexpr int NTHR = 512;
constexpr unsigned WS_H = 0;
constexpr unsigned WS_KV = WS_H + S * F * 2;
constexpr unsigned WS_Y = WS_KV + S * D * 2;
constexpr unsigned WS_G = WS_Y + S * D * 2;
constexpr unsigned WS_CTL = WS_G + S * 4;
constexpr int FL = 32;
constexpr int W_EPOCH = 0, W_H = 1, W_KV = W_H + 192, W_G = W_KV + 64, W_Y = W_G + 128, W_LINES = W_Y + 96;
constexpr unsigned WS_END = WS_CTL + W_LINES * 128;
constexpr int AUX_SC1 = 16;
constexpr int H_AUX = 16;
constexpr unsigned kSpinCap = 1u << 18;

struct Params {
    const u16 *q, *k, *v, *shk, *ssm, *Dv, *w1, *w2, *gw;
    u16* out;
    char* ws;
};

__device__ __forceinline__ float bf_lo(unsigned u) { return __uint_as_float(u << 16); }
__device__ __forceinline__ float bf_hi(unsigned u) { return __uint_as_float(u & 0xffff0000u); }
__device__ __forceinline__ f32x2 bf_2(unsigned u) { return (f32x2){bf_lo(u), bf_hi(u)}; }
__device__ __forceinline__ unsigned pk2(f32x2 v) { return __builtin_bit_cast(unsigned, __builtin_convertvector(v, bf16x2_t)); }
__device__ __forceinline__ unsigned pk_bf16(float lo, float hi) { return pk2((f32x2){lo, hi}); }
__device__ __forceinline__ float rbf(float x) { return bf_lo(pk_bf16(x, x)); }
__device__ __forceinline__ f32x4 mfma16(v4u a, v4u b, f32x4 c) { return __builtin_amdgcn_mfma_f32_16x16x32_bf16(__builtin_bit_cast(bf16x8, a), __builtin_bit_cast(bf16x8, b), c, 0, 0, 0); }
__device__ __forceinline__ v2u tr_read(LAS char* p) { return __builtin_bit_cast(v2u, __builtin_amdgcn_ds_read_tr16_b64_v4i16((LAS s16x4*)p)); }
__device__ __forceinline__ v4u lds_rd16(LAS char* p) { return *(LAS v4u*)p; }
__device__ __forceinline__ void glds16(unsigned voff, const void* sbase, unsigned lds_dst) {
    asm volatile("s_mov_b32 m0, %2\n\ts_nop 0\n\tglobal_load_lds_dwordx4 %0, %1" :: "v"(voff), "s"(sbase), "s"(lds_dst) : "memory");
}
template <bool NT> __device__ __forceinline__ void glds16t(unsigned voff, const void* sbase, unsigned lds_dst) {
    if (NT) asm volatile("s_mov_b32 m0, %2\n\ts_nop 0\n\tglobal_load_lds_dwordx4 %0, %1 nt" :: "v"(voff), "s"(sbase), "s"(lds_dst) : "memory");
    else glds16(voff, sbase, lds_dst);
}
#define NT_X 0
#define NT_W1 0
#define NT_W2 0
#define NT_KV 0
#define NT_SSM 0
template <bool NT> __device__ __forceinline__ v4u gld16(const void* ptr) {
    if (NT) return __builtin_nontemporal_load((const GAS v4u*)ptr);
    return *(const GAS v4u*)ptr;
}
#define WAIT_VM(n) asm volatile("s_waitcnt vmcnt(%0)" :: "n"(n) : "memory")
#define WAIT_LGKM0() asm volatile("s_waitcnt lgkmcnt(0)" ::: "memory")
#define BAR() do { asm volatile("" ::: "memory"); __builtin_amdgcn_s_barrier(); asm volatile("" ::: "memory"); } while (0)
#define SCHED() __builtin_amdgcn_sched_barrier(0)
#define A_MFMA(acc, a, b) asm volatile("v_mfma_f32_16x16x32_bf16 %0, %1, %2, %0" : "+v"(acc) : "v"(a), "v"(b))
#define A_RD128(dst, addr, off) asm volatile("ds_read_b128 %0, %1 offset:%2" : "=v"(dst) : "v"(addr), "n"(off))
#define A_RDTR(dst, addr, off) asm volatile("ds_read_b64_tr_b16 %0, %1 offset:%2" : "=v"(dst) : "v"(addr), "n"(off))
#define A_LGKM(n) asm volatile("s_waitcnt lgkmcnt(%0)" :: "n"(n))
#define A_MFMA_DRAIN() asm volatile("s_nop 15\n\ts_nop 7" ::: "memory")
template <int NM, int NR, int PER> __device__ __forceinline__ void interleave() {
#pragma unroll
    for (int i = 0; i < NM; ++i) {
        __builtin_amdgcn_sched_group_barrier(0x8, 1, 0);
        if (i * PER < NR) __builtin_amdgcn_sched_group_barrier(0x100, PER, 0);
    }
}
#define STAMP(id) do {} while (0)
#define STAMP_ARG
#define STAMP_PASS
#define STAMP_FLUSH() do {} while (0)

__device__ __forceinline__ void st16_sc1(v4u v, __amdgpu_buffer_rsrc_t rs, unsigned off) { __builtin_amdgcn_raw_buffer_store_b128(v, rs, off, 0, AUX_SC1); }
__device__ __forceinline__ void poll_flags(gu32* ctl, int line, int n, unsigned tag, int lane) {
    gu32* f = ctl + (line + (lane < n ? lane : 0)) * FL;
#pragma unroll 1
    for (unsigned spins = 0; spins < kSpinCap; ++spins) {
        const unsigned v = __hip_atomic_load(f, RLX_AGENT);
        if (__all(v == tag)) break;
        __builtin_amdgcn_s_sleep(4);
    }
    __builtin_amdgcn_fence(__ATOMIC_ACQUIRE, "wavefront");
}
__device__ __forceinline__ void set_flag(gu32* ctl, int line, unsigned tag) { __hip_atomic_store(ctl + line * FL, tag, RLX_AGENT); }
__device__ __forceinline__ unsigned silu2f(f32x2 h) {
    const f32x2 t = h * -1.44269504f;
    const f32x2 den = (f32x2){__builtin_amdgcn_exp2f(t.x), __builtin_amdgcn_exp2f(t.y)} + 1.f;
    const f32x2 r = (f32x2){__builtin_amdgcn_rcpf(den.x), __builtin_amdgcn_rcpf(den.y)};
    return pk2(h * r);
}
__device__ __forceinline__ unsigned silu2(unsigned hb) { return silu2f(bf_2(hb)); }
__device__ __forceinline__ float row_sum16(float v) {
    v += __builtin_bit_cast(float, __builtin_amdgcn_update_dpp(0, __builtin_bit_cast(int, v), 0x128, 0xf, 0xf, false));
    v += __builtin_bit_cast(float, __builtin_amdgcn_update_dpp(0, __builtin_bit_cast(int, v), 0x124, 0xf, 0xf, false));
    v += __builtin_bit_cast(float, __builtin_amdgcn_update_dpp(0, __builtin_bit_cast(int, v), 0x122, 0xf, 0xf, false));
    v += __builtin_bit_cast(float, __builtin_amdgcn_update_dpp(0, __builtin_bit_cast(int, v), 0x121, 0xf, 0xf, false));
    return v;
}

constexpr int G1_SLOT = 32768, G1_NS = 4, G1_NST = D / 64, G1_STG = 4 * G1_SLOT;
constexpr int G2_NST = F / 64, G2_RING = 40 * 4096, G2_ROT = 16, G2_HD = 4;
constexpr int G2_Q = (8 + G2_ROT) * 4096, G2_Y = (12 + G2_ROT) * 4096, G2_G = (16 + G2_ROT) * 4096;
constexpr int CV_COPY = 4160, CV_X = 4 * CV_COPY, CV_PAIR = CV_X + 4608;
constexpr int LDS_BYTES = 163840;
static_assert(7 * CV_PAIR <= LDS_BYTES, "LDS map");

struct W2Src { unsigned vw[4]; const char* base; int kb0; unsigned aw[4]; };
__device__ __forceinline__ void w2_init(W2Src& s, const Params& p, int ct, int off2, int lane) {
#pragma unroll
    for (int i = 0; i < 4; ++i) {
        const int r = lane >> 3, c = lane & 7, k = 8 * i + r;
        s.vw[i] = (unsigned)(k * (D * 2) + ((c ^ (((k >> 1) & 3) << 1)) * 16));
    }
    const int g = lane >> 4, q = (lane & 15) >> 2, pp = lane & 3, sw = ((g & 1) << 1) | (q >> 1);
#pragma unroll
    for (int dt = 0; dt < 4; ++dt) s.aw[dt] = (4 * g + q) * 128 + ((dt ^ sw) * 32) + 8 * pp;
    s.base = (const char*)p.w2 + ct * 128; s.kb0 = 2 * off2;
}
__device__ __forceinline__ void w2_issue(const W2Src& s, int b) {
    const int kb = s.kb0 + b >= 48 ? s.kb0 + b - 48 : s.kb0 + b;
    const char* src = s.base + (size_t)kb * (32 * D * 2);
    const unsigned slot = (unsigned)((b + G2_ROT) % 40) * 4096;
#pragma unroll
    for (int i = 0; i < 4; ++i) glds16t<NT_W2>(s.vw[i], src, slot + i * 1024);
}
__device__ __forceinline__ void w2_repack(const W2Src& s, LAS char* L, int b, int lane) {
    LAS char* blk = L + ((b + G2_ROT) % 40) * 4096;
    v4u f[4];
#pragma unroll
    for (int dt = 0; dt < 4; ++dt) { const v2u lo = tr_read(blk + s.aw[dt]), hi = tr_read(blk + s.aw[dt] + 2048); f[dt] = (v4u){lo.x, lo.y, hi.x, hi.y}; }
    WAIT_LGKM0();
#pragma unroll
    for (int dt = 0; dt < 4; ++dt) *(LAS v4u*)(blk + dt * 1024 + lane * 16) = f[dt];
    WAIT_LGKM0();
}

__device__ __forceinline__ void w2_repack2(const W2Src& s, LAS char* L, int b, int lane) {
    LAS char* blk[2] = {L + ((b + G2_ROT) % 40) * 4096, L + ((b + 4 + G2_ROT) % 40) * 4096};
    v4u f[2][4];
#pragma unroll
    for (int h = 0; h < 2; ++h)
#pragma unroll
        for (int dt = 0; dt < 4; ++dt) { const v2u lo = tr_read(blk[h] + s.aw[dt]), hi = tr_read(blk[h] + s.aw[dt] + 2048); f[h][dt] = (v4u){lo.x, lo.y, hi.x, hi.y}; }
    WAIT_LGKM0();
#pragma unroll
    for (int h = 0; h < 2; ++h)
#pragma unroll
        for (int dt = 0; dt < 4; ++dt) *(LAS v4u*)(blk[h] + dt * 1024 + lane * 16) = f[h][dt];
    WAIT_LGKM0();
}
template <bool TYPE_A>
__device__ __forceinline__ void g1_loader(const Params& p, int panel, int ct, int off1, const W2Src& w2s, int lw, int lane STAMP_ARG) {
    unsigned vx[4], vw[4];
#pragma unroll
    for (int i = 0; i < 4; ++i) {
        const int r = lane >> 3, c = lane & 7, row = 32 * lw + 8 * i + r;
        vx[i] = (unsigned)(row * (D * 2) + ((c ^ ((row >> 1) & 7)) * 16));
        const int r2 = lane >> 4, c2 = lane & 15, k = 16 * lw + 4 * i + r2, phi = (k & 3) | (((k >> 3) & 1) << 2);
        vw[i] = (unsigned)(k * (F * 2) + ((c2 ^ (phi << 1)) * 16));
    }
    const char* xb = (const char*)p.q + (size_t)panel * 128 * D * 2;
    const char* wb = (const char*)p.w1 + (size_t)ct * 256;
    int kst = off1;
    auto issue_x = [&](int st) {
        const char* xs = xb + kst * 128;
#pragma unroll
        for (int i = 0; i < 4; ++i) glds16t<NT_X>(vx[i], xs, (unsigned)(st & 3) * G1_SLOT + (32 * lw + 8 * i) * 128);
    };
    auto issue_w = [&](int st) {
        const char* wsrc = wb + (size_t)kst * (64 * F * 2);
#pragma unroll
        for (int i = 0; i < 4; ++i) glds16t<NT_W1>(vw[i], wsrc, (unsigned)(st & 3) * G1_SLOT + 16384 + (16 * lw + 4 * i) * 256);
        kst = kst + 1 == G1_NST ? 0 : kst + 1;
    };
    issue_x(0); issue_w(0); issue_x(1);
    WAIT_VM(8); issue_w(1);
    WAIT_VM(8); issue_x(2);
    BAR();
#pragma unroll 1
    for (int i = 0; i < 7; ++i) { WAIT_VM(8); issue_w(i + 2); WAIT_VM(8); issue_x(i + 3); BAR(); }
    STAMP(1);
    WAIT_VM(8); issue_w(9);
    if (TYPE_A) {
        WAIT_VM(8); w2_issue(w2s, 20 + lw); BAR();
        WAIT_VM(8); w2_issue(w2s, 0 + lw); WAIT_VM(8); w2_issue(w2s, 4 + lw); BAR();
        WAIT_VM(8); w2_issue(w2s, 8 + lw); WAIT_VM(8); w2_issue(w2s, 12 + lw);
    } else {
        WAIT_VM(8); BAR(); WAIT_VM(0); BAR();
    }
}
__device__ __forceinline__ void g1_store(__amdgpu_buffer_rsrc_t rs, int stg, int ksg, int lane, v4u hb) {
    const v4u o = {silu2(hb.x), silu2(hb.y), silu2(hb.z), silu2(hb.w)};
    st16_sc1(o, rs, WS_H + (stg * 48 + ksg) * 1024 + lane * 16);
}
struct ConvIn { v4u v[4], vp[4], x[4]; unsigned vq[4]; float Dd; };
__device__ __forceinline__ void conv_load_h(const Params& p, ConvIn& c, int ch, int lane) {
    const char* hsrc = (const char*)p.ssm + (size_t)ch * (S * 2);
#pragma unroll
    for (int j = 0; j < 4; ++j) {
        const int o = j * 1024 + lane * 16;
        c.v[j] = gld16<NT_SSM>(hsrc + o);
        c.vp[j] = gld16<NT_SSM>(hsrc + (o < 4 ? 0 : o - 4));
        c.vq[j] = *(const GAS unsigned*)(hsrc + (o < 8 ? 0 : o - 8));
    }
    c.Dd = bf_lo((unsigned)p.Dv[ch]);
}
__device__ __forceinline__ void conv_load_x(ConvIn& c, __amdgpu_buffer_rsrc_t rs, int ch, int lane) {
#pragma unroll
    for (int j = 0; j < 4; ++j) c.x[j] = __builtin_amdgcn_raw_buffer_load_b128(rs, j * 1024 + lane * 16, WS_KV + ch * (S * 2), AUX_SC1);
}
template <bool TYPE_A>
__device__ __forceinline__ void g1_compute(const Params& p, LAS char* L, __amdgpu_buffer_rsrc_t rs, int panel, int ct, int w, int lane, ConvIn& cin, int ch STAMP_ARG) {
    const int wf = w & 1, wsx = w >> 1;
    const int g = lane >> 4, q = (lane & 15) >> 2, pp = lane & 3, s = lane & 15;
    const int phi = q | ((g & 1) << 2);
    unsigned aw[4], ax[2];
#pragma unroll
    for (int ft = 0; ft < 4; ++ft) aw[ft] = 16384 + (8 * g + q) * 256 + (((4 * wf + ft) ^ phi) * 32) + 8 * pp;
#pragma unroll
    for (int ks = 0; ks < 2; ++ks) ax[ks] = (64 * wsx + s) * 128 + (((4 * ks + g) ^ ((s >> 1) & 7)) * 16);
    f32x4 acc[4][4];
#pragma unroll
    for (int a = 0; a < 4; ++a)
#pragma unroll
        for (int b = 0; b < 4; ++b) acc[a][b] = (f32x4){0.f, 0.f, 0.f, 0.f};
    v4u A[2][4], B[2][4];
#pragma unroll
    for (int a = 0; a < 4; ++a) { A[1][a] = (v4u){0u, 0u, 0u, 0u}; B[1][a] = (v4u){0u, 0u, 0u, 0u}; }
    auto rd = [&](int set, LAS char* base, int ks) {
#pragma unroll
        for (int ft = 0; ft < 4; ++ft) {
            const v2u lo = tr_read(base + aw[ft] + ks * 8192), hi = tr_read(base + aw[ft] + ks * 8192 + 1024);
            A[set][ft] = (v4u){lo.x, lo.y, hi.x, hi.y};
            if (ft == 0) {
#pragma unroll
                for (int sx = 0; sx < 4; ++sx) B[set][sx] = lds_rd16(base + ax[ks] + sx * 2048);
            }
        }
    };
    auto mm = [&](int set) {
#pragma unroll
        for (int ft = 0; ft < 4; ++ft)
#pragma unroll
            for (int sx = 0; sx < 4; ++sx) acc[ft][sx] = mfma16(A[set][ft], B[set][sx], acc[ft][sx]);
    };
    unsigned slot = 0;
#define X_G1REP 1
#pragma unroll 1
    for (int i = 0; i < G1_NST * X_G1REP; ++i) {
        LAS char* base = L + slot;
        slot = (slot + G1_SLOT) & (G1_NS * G1_SLOT - 1);
        BAR();
        if (i == 0) STAMP(1);
        rd(0, base, 0);
        mm(1);
        interleave<16, 12, 1>();
        SCHED();
        rd(1, base, 1);
        mm(0);
        interleave<16, 12, 1>();
        SCHED();
        WAIT_LGKM0();
    }
    mm(1);
    STAMP(2);
    if (!TYPE_A) conv_load_h(p, cin, ch, lane);
    SCHED();
#pragma unroll
    for (int j = 0; j < 4; ++j) {
        const f32x4 a = acc[2 * (j & 1)][2 + (j >> 1)], b = acc[2 * (j & 1) + 1][2 + (j >> 1)];
        *(LAS v4u*)(L + G1_STG + w * 4096 + j * 1024 + lane * 16) = (v4u){pk_bf16(a[0], a[1]), pk_bf16(a[2], a[3]), pk_bf16(b[0], b[1]), pk_bf16(b[2], b[3])};
    }
    WAIT_LGKM0();
    BAR();
    SCHED();
#pragma unroll
    for (int j = 0; j < 4; ++j) {
        const f32x4 a = acc[2 * (j & 1)][j >> 1], b = acc[2 * (j & 1) + 1][j >> 1];
        const v4u o = {silu2f((f32x2){a[0], a[1]}), silu2f((f32x2){a[2], a[3]}), silu2f((f32x2){b[0], b[1]}), silu2f((f32x2){b[2], b[3]})};
        st16_sc1(o, rs, WS_H + ((panel * 8 + wsx * 4 + (j >> 1)) * 48 + ct * 4 + wf * 2 + (j & 1)) * 1024 + lane * 16);
    }
}
template <bool TYPE_A>
__device__ __forceinline__ void g1_loader_epilogue(LAS char* L, __amdgpu_buffer_rsrc_t rs, int panel, int ct, const W2Src& w2s, int lw, int lane) {
    const int wf = lw & 1, wsx = lw >> 1;
    v4u hb[4];
#pragma unroll
    for (int j = 0; j < 4; ++j) hb[j] = *(LAS v4u*)(L + G1_STG + lw * 4096 + j * 1024 + lane * 16);
#pragma unroll
    for (int j = 0; j < 4; ++j) g1_store(rs, panel * 8 + wsx * 4 + 2 + (j >> 1), ct * 4 + wf * 2 + (j & 1), lane, hb[j]);
    if (TYPE_A) {
#pragma unroll
        for (int j = 0; j < 4; ++j) { WAIT_VM(8); w2_issue(w2s, 24 + 4 * j + lw); }
    }
}

__device__ __forceinline__ void prepass(const Params& p, LAS char* L, __amdgpu_buffer_rsrc_t rs, int wg, int tid STAMP_ARG) {
    const int s0 = 64 * (wg >> 1), c0 = 320 * (wg & 1);
    {
        const int w = __builtin_amdgcn_readfirstlane(tid >> 6);
        const int item = w < 4 ? tid : 256 + (tid & 63);
        const int cc = item % 40, rg = item / 40;
        const int ch = c0 + 8 * cc, r0 = s0 + 8 * rg;
        v4u kk[11], vv[8], sw[4];
#pragma unroll
        for (int j = 0; j < 4; ++j) sw[j] = *(const GAS v4u*)((const char*)p.shk + (size_t)ch * 8 + j * 16);
#pragma unroll
        for (int j = 0; j < 11; ++j) {
            const int row = r0 - 3 + j;
            kk[j] = gld16<NT_KV>((const char*)p.k + (size_t)(row < 0 ? 0 : row) * (D * 2) + ch * 2);
        }
#pragma unroll
        for (int j = 0; j < 8; ++j) vv[j] = gld16<NT_KV>((const char*)p.v + (size_t)(r0 + j) * (D * 2) + ch * 2);
        if (r0 == 0) { kk[0] = (v4u){0u, 0u, 0u, 0u}; kk[1] = kk[0]; kk[2] = kk[0]; }
#pragma unroll
        for (int e = 0; e < 4; ++e) {
            if (w >= 4 && w - 4 != e) continue;
            const f32x2 W0 = {bf_lo(sw[e].x), bf_lo(sw[e].z)}, W1 = {bf_hi(sw[e].x), bf_hi(sw[e].z)};
            const f32x2 W2 = {bf_lo(sw[e].y), bf_lo(sw[e].w)}, W3 = {bf_hi(sw[e].y), bf_hi(sw[e].w)};
            f32x2 kf[11], o[8];
#pragma unroll
            for (int j = 0; j < 11; ++j) kf[j] = bf_2(kk[j][e]);
#pragma unroll
            for (int j = 0; j < 8; ++j) {
                const f32x2 ksh = kf[j] * W3 + kf[j + 1] * W2 + kf[j + 2] * W1 + kf[j + 3] * W0;
                o[j] = ksh * bf_2(vv[j][e]);
            }
            const v4u p0 = {pk_bf16(o[0].x, o[1].x), pk_bf16(o[2].x, o[3].x), pk_bf16(o[4].x, o[5].x), pk_bf16(o[6].x, o[7].x)};
            const v4u p1 = {pk_bf16(o[0].y, o[1].y), pk_bf16(o[2].y, o[3].y), pk_bf16(o[4].y, o[5].y), pk_bf16(o[6].y, o[7].y)};
            *(LAS v4u*)(L + (8 * cc + 2 * e) * 128 + ((rg ^ (cc & 7)) * 16)) = p0;
            *(LAS v4u*)(L + (8 * cc + 2 * e + 1) * 128 + ((rg ^ (cc & 7)) * 16)) = p1;
        }
    }
    STAMP(1);
    WAIT_LGKM0();
    BAR();
#pragma unroll
    for (int j = 0; j < 5; ++j) {
        const int n = tid + NTHR * j, ch = n >> 3, pc = n & 7;
        const v4u val = *(LAS v4u*)(L + ch * 128 + ((pc ^ ((ch >> 3) & 7)) * 16));
        st16_sc1(val, rs, WS_KV + (c0 + ch) * (S * 2) + s0 * 2 + pc * 16);
    }
}
struct GateIn { v4u gw[5], q[5]; };
__device__ __forceinline__ void gate_load(const Params& p, GateIn& gi, int row0, int lane) {
    const int i = lane & 15, row = row0 + (lane >> 4);
#pragma unroll
    for (int j = 0; j < 5; ++j) {
        gi.gw[j] = *(const GAS v4u*)((const char*)p.gw + (i + 16 * j) * 16);
        gi.q[j] = *(const GAS v4u*)((const char*)p.q + (size_t)row * (D * 2) + (i + 16 * j) * 16);
    }
}
__device__ __forceinline__ void gate_finish(const GateIn& gi, __amdgpu_buffer_rsrc_t rs, int row0, int lane) {
    f32x2 a2 = {0.f, 0.f};
#pragma unroll
    for (int j = 0; j < 5; ++j)
#pragma unroll
        for (int e = 0; e < 4; ++e) a2 += bf_2(gi.q[j][e]) * bf_2(gi.gw[j][e]);
    const float z = rbf(row_sum16(a2.x + a2.y));
    const float gv = rbf(__builtin_amdgcn_rcpf(1.f + __builtin_amdgcn_exp2f(z * -1.44269504f)));
    if ((lane & 15) == 0) __builtin_amdgcn_raw_buffer_store_b32(__float_as_uint(gv), rs, WS_G + (row0 + (lane >> 4)) * 4, 0, AUX_SC1);
}

__device__ __forceinline__ void conv_channel(const Params& p, LAS char* Lw, __amdgpu_buffer_rsrc_t rs, ConvIn& c, int ch, int chn, int lane STAMP_ARG) {
    const int g = lane >> 4, q = (lane & 15) >> 2, pp = lane & 3, n = lane & 15;
    __builtin_amdgcn_s_setprio(2);
    if (lane < 30) *(LAS v4u*)(Lw + CV_X + lane * 16) = (v4u){0u, 0u, 0u, 0u};
    if (lane < 8) *(LAS v4u*)(Lw + (lane >> 1) * CV_COPY + (lane & 1) * 16) = (v4u){0u, 0u, 0u, 0u};
    if (lane == 0) { c.vp[0] = (v4u){0u, c.v[0].x, c.v[0].y, c.v[0].z}; c.vq[0] = 0u; }
#pragma unroll
    for (int j = 0; j < 4; ++j) {
        const v4u v = c.v[j], vp = c.vp[j];
        const unsigned a0 = __builtin_amdgcn_alignbit(v.x, vp.x, 16), a1 = __builtin_amdgcn_alignbit(v.y, v.x, 16), a2 = __builtin_amdgcn_alignbit(v.z, v.y, 16), a3 = __builtin_amdgcn_alignbit(v.w, v.z, 16);
        const unsigned am = __builtin_amdgcn_alignbit(vp.x, c.vq[j], 16);
        LAS char* d = Lw + 32 + j * 1024 + lane * 16;
        *(LAS v4u*)(d) = v;
        *(LAS v4u*)(d + CV_COPY) = (v4u){a0, a1, a2, a3};
        *(LAS v4u*)(d + 2 * CV_COPY) = vp;
        *(LAS v4u*)(d + 3 * CV_COPY) = (v4u){am, a0, a1, a2};
    }
#pragma unroll
    for (int j = 0; j < 4; ++j) *(LAS v4u*)(Lw + CV_X + 480 + j * 1024 + lane * 16) = c.x[j];
    const float Dd = c.Dd;
    WAIT_LGKM0();
    __builtin_amdgcn_s_setprio(0);
    if (chn >= 0) { conv_load_h(p, c, chn, lane); conv_load_x(c, rs, chn, lane); asm volatile("" ::: "memory"); }
    STAMP(8);
    unsigned la = (unsigned)(size_t)Lw + q * CV_COPY + 32 - 16 * g + 8 * pp + 24;
    unsigned lb = (unsigned)(size_t)Lw + CV_X + 32 * n + 16 * g + 448 - 128;
    f32x4 acc[8];
#pragma unroll
    for (int t = 0; t < 8; ++t) acc[t] = (f32x4){0.f, 0.f, 0.f, 0.f};
    v2u Alo[2][8], Ahi[2][8];
    v4u B[2][8];
#define CV_RD(set, ro, k) do { \
        if ((k) % 3 == 0) A_RDTR(Alo[set][(k) / 3], la, (ro) + 512 * ((k) / 3) + 8); \
        else if ((k) % 3 == 1) A_RDTR(Ahi[set][(k) / 3], la, (ro) + 512 * ((k) / 3)); \
        else A_RD128(B[set][(k) / 3], lb, 128 - (ro) + 512 * ((k) / 3)); } while (0)
    auto step = [&](auto SET, auto RO) {
        constexpr int set = decltype(SET)::value, ro = decltype(RO)::value;
        int k = 0;
#pragma unroll
        for (int l = 0; l < 8; ++l)
#pragma unroll
            for (int i = 0; i + l < 8; ++i) {
                const v4u a = {Alo[set][i].x, Alo[set][i].y, Ahi[set][i].x, Ahi[set][i].y};
                A_MFMA(acc[i + l], a, B[set][l]);
                if (k < 24) { CV_RD(1 - set, ro, k); }
                ++k;
            }
        A_LGKM(0);
    };
#pragma unroll
    for (int k = 0; k < 24; ++k) CV_RD(0, 0, k);
    A_LGKM(0);
#pragma unroll 1
    for (int rr = 0; rr < 4; ++rr) {
        step(std::integral_constant<int, 0>{}, std::integral_constant<int, 64>{});
        step(std::integral_constant<int, 1>{}, std::integral_constant<int, 128>{});
        la += 128; lb -= 128;
    }
#undef CV_RD
    A_MFMA_DRAIN();
    STAMP(9);
    v2u xv[8];
#pragma unroll
    for (int t = 0; t < 8; ++t) xv[t] = *(LAS v2u*)(Lw + CV_X + 480 + 2 * (256 * t + 16 * n + 4 * g));
#pragma unroll
    for (int t = 0; t < 8; ++t) {
        const f32x4 y = acc[t];
        const v2u o = {pk2(bf_2(xv[t].x) * Dd + (f32x2){y[0], y[1]}), pk2(bf_2(xv[t].y) * Dd + (f32x2){y[2], y[3]})};
        *(LAS v2u*)(Lw + 512 * t + 32 * n + 8 * (g ^ (n >> 2))) = o;
    }
    WAIT_LGKM0();
    v4u val[4];
#pragma unroll
    for (int j = 0; j < 4; ++j) {
        const int nn = (lane >> 1) & 15, cc = nn >> 2;
        val[j] = *(LAS v4u*)(Lw + 1024 * j + 512 * (lane >> 5) + 32 * nn + 16 * ((lane & 1) ^ (cc >> 1)));
        if (cc & 1) val[j] = (v4u){val[j].z, val[j].w, val[j].x, val[j].y};
    }
#pragma unroll
    for (int j = 0; j < 4; ++j) st16_sc1(val[j], rs, WS_Y + ch * (S * 2) + j * 1024 + lane * 16);
    STAMP(10);
}

__device__ __forceinline__ void g2_loader(const Params& p, LAS char* L, __amdgpu_buffer_rsrc_t rs, unsigned tag, const W2Src& w2s, int panel, int ct, int lw, int lane STAMP_ARG) {
    gu32* ctl = (gu32*)(p.ws + WS_CTL);
    const int grow = panel * 128 + (ct & 7) * 16 + 4 * lw;
    STAMP(2);
    w2_issue(w2s, 16 + lw);
    w2_repack2(w2s, L, 0 + lw, lane); w2_repack2(w2s, L, 8 + lw, lane);
    GateIn gi;
    gate_load(p, gi, grow, lane);
    asm volatile("" ::: "memory");
    w2_repack2(w2s, L, 24 + lw, lane);
    STAMP(3);
    if (lw == 0) poll_flags(ctl, W_H + panel * 12, 12, tag, lane);
    if (ct < 8) gate_finish(gi, rs, grow, lane);
    else WAIT_VM(0);
    STAMP(5);
    BAR();
    w2_repack2(w2s, L, 16 + lw, lane); w2_repack2(w2s, L, 32 + lw, lane);
    STAMP(9);
    BAR();
    STAMP(10);
    w2_issue(w2s, 40 + lw); w2_issue(w2s, 44 + lw);
    WAIT_VM(8);
    BAR();
    if (ct < 8 && lw == 0 && lane == 0) set_flag(ctl, W_G + panel * 8 + ct, tag);
#pragma unroll
    for (int i = 0; i < 4; ++i) {
        const int r = lane >> 3, c = lane & 7, row = 32 * lw + 8 * i + r;
        glds16((unsigned)(row * (D * 2) + ((c ^ ((row >> 1) & 7)) * 16)), (const char*)p.q + (size_t)panel * 128 * D * 2 + ct * 128, G2_Q + (32 * lw + 8 * i) * 128);
    }
    WAIT_VM(4);
    w2_repack2(w2s, L, 40 + lw, lane);
    BAR();
    poll_flags(ctl, W_Y + (ct < 8 ? 8 * ct : 64 + 16 * (ct - 8)), ct < 8 ? 8 : 16, tag, lane);
    poll_flags(ctl, W_G + panel * 8, 8, tag, lane);
    STAMP(6);
    v4u yv[4];
#pragma unroll
    for (int i = 0; i < 4; ++i) {
        const int r = lane >> 4, c = lane & 15, d = 16 * lw + 4 * i + r;
        yv[i] = __builtin_amdgcn_raw_buffer_load_b128(rs, d * (S * 2) + c * 16, WS_Y + ct * 64 * S * 2 + panel * 256, AUX_SC1);
    }
    const unsigned gv = __builtin_amdgcn_raw_buffer_load_b32(rs, (lw * 32 + (lane & 31)) * 4, WS_G + panel * 512, AUX_SC1);
#pragma unroll
    for (int i = 0; i < 4; ++i) {
        const int r = lane >> 4, c = lane & 15, d = 16 * lw + 4 * i + r;
        *(LAS v4u*)(L + G2_Y + d * 256 + ((c ^ ((d & 7) << 1)) * 16)) = yv[i];
    }
    if (lane < 32) *(LAS unsigned*)(L + G2_G + (lw * 32 + lane) * 4) = gv;
    WAIT_VM(0);
    WAIT_LGKM0();
    BAR();
}
__device__ __forceinline__ void g2_compute(const Params& p, LAS char* L, __amdgpu_buffer_rsrc_t rs, int panel, int ct, int off2, int w, int lane STAMP_ARG) {
    const int g = lane >> 4, q = (lane & 15) >> 2, pp = lane & 3, s = lane & 15;
    const unsigned hbase = WS_H + (panel * 8 + 2 * w) * (48 * 1024);
    int kld = off2;
    f32x4 acc[4][2];
#pragma unroll
    for (int a = 0; a < 4; ++a) { acc[a][0] = (f32x4){0.f, 0.f, 0.f, 0.f}; acc[a][1] = (f32x4){0.f, 0.f, 0.f, 0.f}; }
    v4u H[G2_HD][2][2];
    auto ldh1 = [&](int slot, int ks, int sx) {
        H[slot][ks][sx] = __builtin_amdgcn_raw_buffer_load_b128(rs, lane * 16 + sx * (48 * 1024) + ks * 1024,
            hbase + kld * 2048, H_AUX);
        asm volatile("" ::: "memory");
    };
    auto ldh = [&](int slot) {
#pragma unroll
        for (int ks = 0; ks < 2; ++ks)
#pragma unroll
            for (int sx = 0; sx < 2; ++sx) ldh1(slot, ks, sx);
        kld = kld + 1 == G2_NST ? 0 : kld + 1;
    };
    BAR();
    STAMP(5);
#pragma unroll
    for (int st = 0; st < G2_HD - 1; ++st) ldh(st);
#pragma unroll
    for (int a = 0; a < 4; ++a) H[G2_HD - 1][a >> 1][a & 1] = (v4u){0u, 0u, 0u, 0u};
    SCHED();
    v4u A[2][4];
#pragma unroll
    for (int a = 0; a < 4; ++a) A[1][a] = (v4u){0u, 0u, 0u, 0u};
    auto half = [&](int set, int slot, int ks, unsigned addr, int off, bool load, int up, int lk) {
        A_LGKM(3);
        A_MFMA(acc[0][0], A[set][0], H[slot][ks][0]); A_RD128(A[1 - set][0], addr, off);
        A_MFMA(acc[0][1], A[set][0], H[slot][ks][1]); A_RD128(A[1 - set][1], addr, off + 1024);
        if (load) ldh1(up, lk, 0);
        A_LGKM(4);
        A_MFMA(acc[1][0], A[set][1], H[slot][ks][0]); A_RD128(A[1 - set][2], addr, off + 2048);
        A_MFMA(acc[1][1], A[set][1], H[slot][ks][1]); A_RD128(A[1 - set][3], addr, off + 3072);
        A_LGKM(5);
        A_MFMA(acc[2][0], A[set][2], H[slot][ks][0]);
        A_MFMA(acc[2][1], A[set][2], H[slot][ks][1]);
        if (load) ldh1(up, lk, 1);
        A_LGKM(4);
        A_MFMA(acc[3][0], A[set][3], H[slot][ks][0]);
        A_MFMA(acc[3][1], A[set][3], H[slot][ks][1]);
    };
    const unsigned lbase = (unsigned)(size_t)L + lane * 16;
    unsigned sb = G2_ROT * 4096;
    auto stage = [&](int u, bool load, bool drain) {
        const unsigned addr = lbase + sb;
        const int up = (u + G2_HD - 1) % G2_HD;
        half(1, up, 1, addr, 0, load, up, 0);
        half(0, u, 0, addr, 4096, load, up, 1);
        if (load) kld = kld + 1 == G2_NST ? 0 : kld + 1;
        if (drain) WAIT_LGKM0();
        sb = sb + 8192 == G2_RING ? 0 : sb + 8192;
    };
#pragma unroll 1
    for (int jj = 0; jj < 5; ++jj) {
        if (jj >= 1 && jj <= 3) BAR();
        if (jj == 0) STAMP(14); else if (jj == 2) STAMP(12); else if (jj == 4) STAMP(13);
#pragma unroll
        for (int u = 0; u < 4; ++u) stage(u, true, u == 3);
    }
#pragma unroll
    for (int u = 0; u < 4; ++u) stage(u, u == 0, false);
    WAIT_LGKM0();
#pragma unroll
    for (int dt = 0; dt < 4; ++dt) { A_MFMA(acc[dt][0], A[1][dt], H[G2_HD - 1][1][0]); A_MFMA(acc[dt][1], A[1][dt], H[G2_HD - 1][1][1]); }
    A_MFMA_DRAIN();
    STAMP(6);
    BAR();
    STAMP(7);
    {
        float gg[2]; v2u yv[2][4], qv[2][4];
#pragma unroll
        for (int sx = 0; sx < 2; ++sx) {
            const int row = 32 * w + 16 * sx + s;
            gg[sx] = *(LAS float*)(L + G2_G + row * 4);
#pragma unroll
            for (int dt = 0; dt < 4; ++dt) {
                const int d = 16 * dt + 4 * g + q;
                yv[sx][dt] = tr_read(L + G2_Y + d * 256 + (((2 * w + sx) ^ (d & 7)) * 32) + 8 * pp);
                qv[sx][dt] = *(LAS v2u*)(L + G2_Q + row * 128 + (((2 * dt + (g >> 1)) ^ ((row >> 1) & 7)) * 16) + 8 * (g & 1));
            }
        }
        SCHED();
#pragma unroll
        for (int sx = 0; sx < 2; ++sx) {
            const int row = 32 * w + 16 * sx + s;
            const float omg = 1.f - gg[sx];
#pragma unroll
            for (int dt = 0; dt < 4; ++dt) {
                const f32x4 a = acc[dt][sx];
                const f32x2 o01 = bf_2(qv[sx][dt].x) * bf_2(yv[sx][dt].x) * gg[sx] + (f32x2){a[0], a[1]} * omg;
                const f32x2 o23 = bf_2(qv[sx][dt].y) * bf_2(yv[sx][dt].y) * gg[sx] + (f32x2){a[2], a[3]} * omg;
                *(LAS v2u*)(L + G2_Q + row * 128 + (((2 * dt + (g >> 1)) ^ ((row >> 1) & 7)) * 16) + 8 * (g & 1)) = (v2u){pk2(o01), pk2(o23)};
            }
        }
    }
    WAIT_LGKM0();
#pragma unroll
    for (int i = 0; i < 4; ++i) {
        const int row = 32 * w + 8 * i + (lane >> 3), c = lane & 7;
        const v4u val = *(LAS v4u*)(L + G2_Q + row * 128 + c * 16);
        *(GAS v4u*)((char*)p.out + (size_t)(panel * 128 + row) * (D * 2) + ct * 128 + ((c ^ ((row >> 1) & 7)) * 16)) = val;
    }
    STAMP(11);
}

__global__ void __launch_bounds__(NTHR, 2) k_fused(Params p) {
    extern __shared__ __attribute__((aligned(16))) char smem[];
    LAS char* L = (LAS char*)smem;
    const int tid = threadIdx.x, lane = tid & 63, w = __builtin_amdgcn_readfirstlane(tid >> 6);
    const int x = blockIdx.x & 7, idx = blockIdx.x >> 3, xr = x >> 1, xc = x & 1;
    STAMP(0);
    gu32* ctl = (gu32*)(p.ws + WS_CTL);
    unsigned epoch_v = __hip_atomic_load(ctl + W_EPOCH * FL, RLX_AGENT);
#define GET_TAG() asm volatile("" : "+v"(epoch_v)); const unsigned tag = __builtin_amdgcn_readfirstlane(epoch_v) + 1u
    const __amdgpu_buffer_rsrc_t rs = __builtin_amdgcn_make_buffer_rsrc(p.ws, 0, WS_END, 0x00020000);
    if (idx < 24) {
        const int panel = 4 * xr + idx / 6, ct = 6 * xc + idx % 6;
        const int off1 = 5 * ((idx / 6 + idx % 6) & 1);
        const bool typeA = idx < 20;
        const int panel2 = 4 * xr + idx / 5, ct2 = 5 * xc + idx % 5;
        const int off2 = ((idx % 5) * 24 / 5 + (idx / 5) * 6) % 24;
        if (typeA) {
            if (w < 4) {
                ConvIn cin;
                g1_compute<true>(p, L, rs, panel, ct, w, lane, cin, 0 STAMP_PASS);
                STAMP(3);
                WAIT_VM(0);
                GET_TAG();
                STAMP(4);
                BAR();
                if (tid == 0) set_flag(ctl, W_H + panel * 12 + ct, tag);
                g2_compute(p, L, rs, panel2, ct2, off2, w, lane STAMP_PASS);
            } else {
                const int lw = w - 4;
                __builtin_amdgcn_s_setprio(2);
                W2Src w2s;
                w2_init(w2s, p, ct2, off2, lane);
                g1_loader<true>(p, panel, ct, off1, w2s, lw, lane STAMP_PASS);
                __builtin_amdgcn_s_setprio(0);
                BAR();
                g1_loader_epilogue<true>(L, rs, panel, ct, w2s, lw, lane);
                WAIT_VM(8);
                GET_TAG();
                __builtin_amdgcn_s_setprio(2);
                BAR();
                g2_loader(p, L, rs, tag, w2s, panel2, ct2, lw, lane STAMP_PASS);
            }
        } else {
            const int bw = x * 4 + idx - 20;
            if (w < 4) {
                const int ch = 512 + 4 * bw + w;
                ConvIn cin;
                g1_compute<false>(p, L, rs, panel, ct, w, lane, cin, ch STAMP_PASS);
                STAMP(3);
                WAIT_VM(0);
                GET_TAG();
                STAMP(4);
                BAR();
                if (tid == 0) set_flag(ctl, W_H + panel * 12 + ct, tag);
                BAR();
                conv_load_x(cin, rs, ch, lane);
                conv_channel(p, L + w * CV_PAIR, rs, cin, ch, -1, lane STAMP_PASS);
                WAIT_VM(0);
                BAR();
                if (tid == 0) set_flag(ctl, W_Y + 64 + bw, tag);
                STAMP(16);
            } else {
                W2Src w2s;
                __builtin_amdgcn_s_setprio(2);
                g1_loader<false>(p, panel, ct, off1, w2s, w - 4, lane STAMP_PASS);
                __builtin_amdgcn_s_setprio(0);
                BAR();
                g1_loader_epilogue<false>(L, rs, panel, ct, w2s, w - 4, lane);
                WAIT_VM(0);
                GET_TAG();
                BAR();
                if (w == 7) poll_flags(ctl, W_KV, 64, tag, lane);
                BAR();
                BAR();
            }
        }
    } else {
        const int cw = x * 8 + (idx - 24);
        const int ch = 8 * cw + (w < 7 ? w : 0);
        ConvIn cin;
        conv_load_h(p, cin, ch, lane);
        prepass(p, L, rs, cw, tid STAMP_PASS);
        STAMP(2);
        WAIT_VM(0);
        GET_TAG();
        BAR();
        if (tid == 0) set_flag(ctl, W_KV + cw, tag);
        STAMP(3);
        if (w == 7) poll_flags(ctl, W_KV, 64, tag, lane);
        BAR();
        STAMP(4);
        if (w < 7) {
            conv_load_x(cin, rs, ch, lane);
            int c0 = ch, c1 = w == 3 ? 8 * cw + 7 : -1;
#pragma unroll 1
            for (int round = 0; round < 2; ++round) {
                conv_channel(p, L + w * CV_PAIR, rs, cin, c0, c1, lane STAMP_PASS);
                if (c1 < 0) break;
                c0 = c1; c1 = -1;
            }
        }
        WAIT_VM(0);
        BAR();
        if (tid == 0) set_flag(ctl, W_Y + cw, tag);
        STAMP(16);
        if (w == 7 && blockIdx.x == 255) {
#pragma unroll 1
            for (int pn = 0; pn < 3; ++pn) poll_flags(ctl, W_H + pn * 64, 64, tag, lane);
            if (lane == 0) set_flag(ctl, W_EPOCH, tag);
        }
    }
    STAMP_FLUSH();
}

extern "C" void kernel_launch(void* const* d_in, const int* in_sizes, int n_in, void* d_out, int out_size, void* d_ws, size_t ws_size, hipStream_t stream) {
    Params p;
    p.q = (const u16*)d_in[0]; p.k = (const u16*)d_in[1]; p.v = (const u16*)d_in[2]; p.shk = (const u16*)d_in[3];
    p.ssm = (const u16*)d_in[4]; p.Dv = (const u16*)d_in[5]; p.w1 = (const u16*)d_in[7]; p.w2 = (const u16*)d_in[8]; p.gw = (const u16*)d_in[9];
    p.out = (u16*)d_out; p.ws = (char*)d_ws;
    (void)hipFuncSetAttribute((const void*)k_fused, hipFuncAttributeMaxDynamicSharedMemorySize, LDS_BYTES);
    k_fused<<<256, NTHR, LDS_BYTES, stream>>>(p);
}
```

```cpp
#include <hip/hip_runtime.h>
#include <stdint.h>
#include <type_traits>

typedef unsigned short u16;
typedef unsigned v4u __attribute__((ext_vector_type(4)));
typedef unsigned v2u __attribute__((ext_vector_type(2)));
typedef float f32x4 __attribute__((ext_vector_type(4)));
typedef float f32x2 __attribute__((ext_vector_type(2)));
typedef short bf16x8 __attribute__((ext_vector_type(8)));
typedef short s16x4 __attribute__((ext_vector_type(4)));
typedef __bf16 bf16x2_t __attribute__((ext_vector_type(2)));
#define LAS __attribute__((address_space(3)))
#define GAS __attribute__((address_space(1)))
typedef GAS unsigned gu32;
typedef GAS unsigned long long gu64;
#define RLX_AGENT __ATOMIC_RELAXED, __HIP_MEMORY_SCOPE_AGENT

constexpr int S = 2048, D = 640, F = 1536;
constexpr int NTHR = 512;
constexpr unsigned WS_H = 0;
constexpr unsigned WS_KV = WS_H + S * F * 2;
constexpr unsigned WS_Y = WS_KV + S * D * 2;
constexpr unsigned WS_G = WS_Y + S * D * 2;
constexpr unsigned WS_CTL = WS_G + S * 4;
constexpr int FL = 32;
constexpr int W_EPOCH = 0, W_H = 1, W_KV = W_H + 768, W_G = W_KV + 64, W_Y = W_G + 128, W_LINES = W_Y + 96;
constexpr unsigned WS_END = WS_CTL + W_LINES * 128;
constexpr int AUX_SC1 = 16;
constexpr int H_AUX = 16;
constexpr unsigned kSpinCap = 1u << 18;

struct Params {
    const u16 *q, *k, *v, *shk, *ssm, *Dv, *w1, *w2, *gw;
    u16* out;
    char* ws;
};

__device__ __forceinline__ float bf_lo(unsigned u) { return __uint_as_float(u << 16); }
__device__ __forceinline__ float bf_hi(unsigned u) { return __uint_as_float(u & 0xffff0000u); }
__device__ __forceinline__ f32x2 bf_2(unsigned u) { return (f32x2){bf_lo(u), bf_hi(u)}; }
__device__ __forceinline__ unsigned pk2(f32x2 v) { return __builtin_bit_cast(unsigned, __builtin_convertvector(v, bf16x2_t)); }
__device__ __forceinline__ unsigned pk_bf16(float lo, float hi) { return pk2((f32x2){lo, hi}); }
__device__ __forceinline__ float rbf(float x) { return bf_lo(pk_bf16(x, x)); }
__device__ __forceinline__ f32x4 mfma16(v4u a, v4u b, f32x4 c) { return __builtin_amdgcn_mfma_f32_16x16x32_bf16(__builtin_bit_cast(bf16x8, a), __builtin_bit_cast(bf16x8, b), c, 0, 0, 0); }
__device__ __forceinline__ v2u tr_read(LAS char* p) { return __builtin_bit_cast(v2u, __builtin_amdgcn_ds_read_tr16_b64_v4i16((LAS s16x4*)p)); }
__device__ __forceinline__ v4u lds_rd16(LAS char* p) { return *(LAS v4u*)p; }
__device__ __forceinline__ void glds16(unsigned voff, const void* sbase, unsigned lds_dst) {
    asm volatile("s_mov_b32 m0, %2\n\ts_nop 0\n\tglobal_load_lds_dwordx4 %0, %1" :: "v"(voff), "s"(sbase), "s"(lds_dst) : "memory");
}
template <bool NT> __device__ __forceinline__ void glds16t(unsigned voff, const void* sbase, unsigned lds_dst) {
    if (NT) asm volatile("s_mov_b32 m0, %2\n\ts_nop 0\n\tglobal_load_lds_dwordx4 %0, %1 nt" :: "v"(voff), "s"(sbase), "s"(lds_dst) : "memory");
    else glds16(voff, sbase, lds_dst);
}
#define NT_X 0
#define NT_W1 0
#define NT_W2 0
#define NT_KV 0
#define NT_SSM 0
template <bool NT> __device__ __forceinline__ v4u gld16(const void* ptr) {
    if (NT) return __builtin_nontemporal_load((const GAS v4u*)ptr);
    return *(const GAS v4u*)ptr;
}
#define WAIT_VM(n) asm volatile("s_waitcnt vmcnt(%0)" :: "n"(n) : "memory")
#define WAIT_LGKM0() asm volatile("s_waitcnt lgkmcnt(0)" ::: "memory")
#define BAR() do { asm volatile("" ::: "memory"); __builtin_amdgcn_s_barrier(); asm volatile("" ::: "memory"); } while (0)
#define SCHED() __builtin_amdgcn_sched_barrier(0)
#define A_MFMA(acc, a, b) asm volatile("v_mfma_f32_16x16x32_bf16 %0, %1, %2, %0" : "+v"(acc) : "v"(a), "v"(b))
#define A_RD128(dst, addr, off) asm volatile("ds_read_b128 %0, %1 offset:%2" : "=v"(dst) : "v"(addr), "n"(off))
#define A_RDTR(dst, addr, off) asm volatile("ds_read_b64_tr_b16 %0, %1 offset:%2" : "=v"(dst) : "v"(addr), "n"(off))
#define A_LGKM(n) asm volatile("s_waitcnt lgkmcnt(%0)" :: "n"(n))
#define A_MFMA_DRAIN() asm volatile("s_nop 15\n\ts_nop 7" ::: "memory")
template <int NM, int NR, int PER> __device__ __forceinline__ void interleave() {
#pragma unroll
    for (int i = 0; i < NM; ++i) {
        __builtin_amdgcn_sched_group_barrier(0x8, 1, 0);
        if (i * PER < NR) __builtin_amdgcn_sched_group_barrier(0x100, PER, 0);
    }
}
#define STAMP(id) do {} while (0)
#define STAMP_ARG
#define STAMP_PASS
#define STAMP_FLUSH() do {} while (0)

__device__ __forceinline__ void st16_sc1(v4u v, __amdgpu_buffer_rsrc_t rs, unsigned off) { __builtin_amdgcn_raw_buffer_store_b128(v, rs, off, 0, AUX_SC1); }
__device__ __forceinline__ void poll_flags(gu32* ctl, int line, int n, unsigned tag, int lane) {
    gu32* f = ctl + (line + (lane < n ? lane : 0)) * FL;
#pragma unroll 1
    for (unsigned spins = 0; spins < kSpinCap; ++spins) {
        const unsigned v = __hip_atomic_load(f, RLX_AGENT);
        if (__all(v == tag)) break;
        __builtin_amdgcn_s_sleep(4);
    }
    __builtin_amdgcn_fence(__ATOMIC_ACQUIRE, "wavefront");
}
__device__ __forceinline__ void set_flag(gu32* ctl, int line, unsigned tag) { __hip_atomic_store(ctl + line * FL, tag, RLX_AGENT); }
__device__ __forceinline__ unsigned silu2f(f32x2 h) {
    const f32x2 t = h * -1.44269504f;
    const f32x2 den = (f32x2){__builtin_amdgcn_exp2f(t.x), __builtin_amdgcn_exp2f(t.y)} + 1.f;
    const f32x2 r = (f32x2){__builtin_amdgcn_rcpf(den.x), __builtin_amdgcn_rcpf(den.y)};
    return pk2(h * r);
}
__device__ __forceinline__ unsigned silu2(unsigned hb) { return silu2f(bf_2(hb)); }
__device__ __forceinline__ float row_sum16(float v) {
    v += __builtin_bit_cast(float, __builtin_amdgcn_update_dpp(0, __builtin_bit_cast(int, v), 0x128, 0xf, 0xf, false));
    v += __builtin_bit_cast(float, __builtin_amdgcn_update_dpp(0, __builtin_bit_cast(int, v), 0x124, 0xf, 0xf, false));
    v += __builtin_bit_cast(float, __builtin_amdgcn_update_dpp(0, __builtin_bit_cast(int, v), 0x122, 0xf, 0xf, false));
    v += __builtin_bit_cast(float, __builtin_amdgcn_update_dpp(0, __builtin_bit_cast(int, v), 0x121, 0xf, 0xf, false));
    return v;
}

constexpr int G1_SLOT = 32768, G1_NST = D / 64;
__device__ __forceinline__ unsigned g1_slot(int st) { return (unsigned)(1 + ((st + 2) & 3)) * G1_SLOT; }
constexpr int G2_NST = F / 64, G2_RING = 40 * 4096, G2_HD = 4;
constexpr int G2_Q = 8 * 4096, G2_Y = 12 * 4096, G2_G = 16 * 4096;
constexpr int CV_COPY = 4160, CV_X = 4 * CV_COPY, CV_PAIR = CV_X + 4608;
constexpr int LDS_BYTES = 163840;
static_assert(7 * CV_PAIR <= LDS_BYTES, "LDS map");

struct W2Src { unsigned vw[4]; const char* base; int kb0; unsigned aw[4]; };
__device__ __forceinline__ void w2_init(W2Src& s, const Params& p, int ct, int off2, int lane) {
#pragma unroll
    for (int i = 0; i < 4; ++i) {
        const int r = lane >> 3, c = lane & 7, k = 8 * i + r;
        s.vw[i] = (unsigned)(k * (D * 2) + ((c ^ (((k >> 1) & 3) << 1)) * 16));
    }
    const int g = lane >> 4, q = (lane & 15) >> 2, pp = lane & 3, sw = ((g & 1) << 1) | (q >> 1);
#pragma unroll
    for (int dt = 0; dt < 4; ++dt) s.aw[dt] = (4 * g + q) * 128 + ((dt ^ sw) * 32) + 8 * pp;
    s.base = (const char*)p.w2 + ct * 128; s.kb0 = 2 * off2;
}
__device__ __forceinline__ void w2_issue(const W2Src& s, int b) {
    const int kb = s.kb0 + b >= 48 ? s.kb0 + b - 48 : s.kb0 + b;
    const char* src = s.base + (size_t)kb * (32 * D * 2);
    const unsigned slot = (unsigned)(b >= 40 ? b - 40 : b) * 4096;
#pragma unroll
    for (int i = 0; i < 4; ++i) glds16t<NT_W2>(s.vw[i], src, slot + i * 1024);
}
__device__ __forceinline__ void w2_repack2(const W2Src& s, LAS char* L, int b, int lane) {
    LAS char* blk = L + (b >= 40 ? b - 40 : b) * 4096;
    v4u f[2][4];
#pragma unroll
    for (int h = 0; h < 2; ++h)
#pragma unroll
        for (int dt = 0; dt < 4; ++dt) { const v2u lo = tr_read(blk + h * 8192 + s.aw[dt]), hi = tr_read(blk + h * 8192 + s.aw[dt] + 2048); f[h][dt] = (v4u){lo.x, lo.y, hi.x, hi.y}; }
    WAIT_LGKM0();
#pragma unroll
    for (int h = 0; h < 2; ++h)
#pragma unroll
        for (int dt = 0; dt < 4; ++dt) *(LAS v4u*)(blk + h * 8192 + dt * 1024 + lane * 16) = f[h][dt];
    WAIT_LGKM0();
}
template <bool TYPE_A>
__device__ __forceinline__ void g1_stream(const Params& p, int panel, int ct, int off1, const W2Src& w2s, int sw, int lane STAMP_ARG) {
    unsigned vx[8], vw[8];
#pragma unroll
    for (int i = 0; i < 8; ++i) {
        const int r = lane >> 3, c = lane & 7, row = 64 * sw + 8 * i + r;
        vx[i] = (unsigned)(row * (D * 2) + ((c ^ ((row >> 1) & 7)) * 16));
        const int r2 = lane >> 4, c2 = lane & 15, k = 32 * sw + 4 * i + r2, phi = (k & 3) | (((k >> 3) & 1) << 2);
        vw[i] = (unsigned)(k * (F * 2) + ((c2 ^ (phi << 1)) * 16));
    }
    const char* xb = (const char*)p.q + (size_t)panel * 128 * D * 2;
    const char* wb = (const char*)p.w1 + (size_t)ct * 256;
    int kx = off1, kw = off1;
    auto gx = [&](int st, int part) {
#pragma unroll
        for (int i = 4 * part; i < 4 * part + 4; ++i) glds16t<NT_X>(vx[i], xb + kx * 128, g1_slot(st) + (64 * sw + 8 * i) * 128);
        if (part == 1) kx = kx + 1 == G1_NST ? 0 : kx + 1;
    };
    auto gw = [&](int st, int part) {
#pragma unroll
        for (int i = 4 * part; i < 4 * part + 4; ++i) glds16t<NT_W1>(vw[i], wb + (size_t)kw * (64 * F * 2), g1_slot(st) + 16384 + (32 * sw + 4 * i) * 256);
        if (part == 1) kw = kw + 1 == G1_NST ? 0 : kw + 1;
    };
    gx(0, 0); gx(0, 1); gw(0, 0); gw(0, 1); gx(1, 0); gx(1, 1);
    WAIT_VM(20); gw(1, 0); WAIT_VM(20); gw(1, 1); WAIT_VM(20); gx(2, 0); WAIT_VM(20); gx(2, 1);
    BAR();
#pragma unroll 1
    for (int i = 0; i < 7; ++i) {
        WAIT_VM(20); gw(i + 2, 0); WAIT_VM(20); gw(i + 2, 1); WAIT_VM(20); gx(i + 3, 0); WAIT_VM(20); gx(i + 3, 1);
        BAR();
    }
    STAMP(1);
    WAIT_VM(20); gw(9, 0); WAIT_VM(20); gw(9, 1);
    if (TYPE_A) {
        WAIT_VM(20); w2_issue(w2s, 0 + sw); WAIT_VM(20); w2_issue(w2s, 2 + sw); BAR();
#pragma unroll
        for (int k = 2; k < 6; ++k) { WAIT_VM(20); w2_issue(w2s, 2 * k + sw); }
        BAR();
    } else {
        WAIT_VM(16); BAR(); WAIT_VM(0); BAR();
    }
}
struct ConvIn { v4u v[4], vp[4], x[4]; unsigned vq[4]; u16 Dd; };
__device__ __forceinline__ void conv_load_h(const Params& p, ConvIn& c, int ch, int lane) {
    const char* hsrc = (const char*)p.ssm + (size_t)ch * (S * 2);
#pragma unroll
    for (int j = 0; j < 4; ++j) {
        const int o = j * 1024 + lane * 16;
        c.v[j] = gld16<NT_SSM>(hsrc + o);
        c.vp[j] = gld16<NT_SSM>(hsrc + (o < 4 ? 0 : o - 4));
        c.vq[j] = *(const GAS unsigned*)(hsrc + (o < 8 ? 0 : o - 8));
    }
    c.Dd = p.Dv[ch];
}
__device__ __forceinline__ void conv_load_x(ConvIn& c, __amdgpu_buffer_rsrc_t rs, int ch, int lane) {
#pragma unroll
    for (int j = 0; j < 4; ++j) c.x[j] = __builtin_amdgcn_raw_buffer_load_b128(rs, j * 1024 + lane * 16, WS_KV + ch * (S * 2), AUX_SC1);
}
template <bool TYPE_A>
__device__ __forceinline__ void g1_compute(const Params& p, LAS char* L, __amdgpu_buffer_rsrc_t rs, int panel, int ct, int w, int lane, ConvIn& cin, int ch STAMP_ARG) {
    const int wf = w & 1, wsx = w >> 1;
    const int g = lane >> 4, q = (lane & 15) >> 2, pp = lane & 3, s = lane & 15;
    const int phi = q | ((g & 1) << 2);
    unsigned aw[4], ax[2];
#pragma unroll
    for (int ft = 0; ft < 4; ++ft) aw[ft] = 16384 + (8 * g + q) * 256 + (((4 * wf + ft) ^ phi) * 32) + 8 * pp;
#pragma unroll
    for (int ks = 0; ks < 2; ++ks) ax[ks] = (64 * wsx + s) * 128 + (((4 * ks + g) ^ ((s >> 1) & 7)) * 16);
    f32x4 acc[4][4];
#pragma unroll
    for (int a = 0; a < 4; ++a)
#pragma unroll
        for (int b = 0; b < 4; ++b) acc[a][b] = (f32x4){0.f, 0.f, 0.f, 0.f};
    v4u A[2][4], B[2][4];
#pragma unroll
    for (int a = 0; a < 4; ++a) { A[1][a] = (v4u){0u, 0u, 0u, 0u}; B[1][a] = (v4u){0u, 0u, 0u, 0u}; }
    auto rd = [&](int set, LAS char* base, int ks) {
#pragma unroll
        for (int ft = 0; ft < 4; ++ft) {
            const v2u lo = tr_read(base + aw[ft] + ks * 8192), hi = tr_read(base + aw[ft] + ks * 8192 + 1024);
            A[set][ft] = (v4u){lo.x, lo.y, hi.x, hi.y};
            if (ft == 0) {
#pragma unroll
                for (int sx = 0; sx < 4; ++sx) B[set][sx] = lds_rd16(base + ax[ks] + sx * 2048);
            }
        }
    };
    auto mm = [&](int set) {
#pragma unroll
        for (int ft = 0; ft < 4; ++ft)
#pragma unroll
            for (int sx = 0; sx < 4; ++sx) acc[ft][sx] = mfma16(A[set][ft], B[set][sx], acc[ft][sx]);
    };
#pragma unroll 1
    for (int i = 0; i < G1_NST; ++i) {
        LAS char* base = L + g1_slot(i);
        BAR();
        if (i == 0) STAMP(1);
        rd(0, base, 0);
        mm(1);
        interleave<16, 12, 1>();
        SCHED();
        rd(1, base, 1);
        mm(0);
        interleave<16, 12, 1>();
        SCHED();
        WAIT_LGKM0();
    }
    mm(1);
    STAMP(2);
    if (!TYPE_A) conv_load_h(p, cin, ch, lane);
    SCHED();
#pragma unroll
    for (int j = 0; j < 8; ++j) {
        const f32x4 a = acc[2 * (j & 1)][j >> 1], b = acc[2 * (j & 1) + 1][j >> 1];
        const v4u o = {silu2f((f32x2){a[0], a[1]}), silu2f((f32x2){a[2], a[3]}), silu2f((f32x2){b[0], b[1]}), silu2f((f32x2){b[2], b[3]})};
        st16_sc1(o, rs, WS_H + ((panel * 8 + wsx * 4 + (j >> 1)) * 48 + ct * 4 + wf * 2 + (j & 1)) * 1024 + lane * 16);
    }
}

__device__ __forceinline__ void prepass(const Params& p, LAS char* L, __amdgpu_buffer_rsrc_t rs, int wg, int tid STAMP_ARG) {
    const int s0 = 64 * (wg >> 1), c0 = 320 * (wg & 1);
    {
        const int w = __builtin_amdgcn_readfirstlane(tid >> 6);
        const int item = w < 4 ? tid : 256 + (tid & 63);
        const int cc = item % 40, rg = item / 40;
        const int ch = c0 + 8 * cc, r0 = s0 + 8 * rg;
        v4u kk[11], vv[8], sw[4];
#pragma unroll
        for (int j = 0; j < 4; ++j) sw[j] = *(const GAS v4u*)((const char*)p.shk + (size_t)ch * 8 + j * 16);
#pragma unroll
        for (int j = 0; j < 11; ++j) {
            const int row = r0 - 3 + j;
            kk[j] = gld16<NT_KV>((const char*)p.k + (size_t)(row < 0 ? 0 : row) * (D * 2) + ch * 2);
        }
#pragma unroll
        for (int j = 0; j < 8; ++j) vv[j] = gld16<NT_KV>((const char*)p.v + (size_t)(r0 + j) * (D * 2) + ch * 2);
        if (r0 == 0) { kk[0] = (v4u){0u, 0u, 0u, 0u}; kk[1] = kk[0]; kk[2] = kk[0]; }
#pragma unroll
        for (int e = 0; e < 4; ++e) {
            if (w >= 4 && w - 4 != e) continue;
            const f32x2 W0 = {bf_lo(sw[e].x), bf_lo(sw[e].z)}, W1 = {bf_hi(sw[e].x), bf_hi(sw[e].z)};
            const f32x2 W2 = {bf_lo(sw[e].y), bf_lo(sw[e].w)}, W3 = {bf_hi(sw[e].y), bf_hi(sw[e].w)};
            f32x2 kf[11], o[8];
#pragma unroll
            for (int j = 0; j < 11; ++j) kf[j] = bf_2(kk[j][e]);
#pragma unroll
            for (int j = 0; j < 8; ++j) {
                const f32x2 ksh = kf[j] * W3 + kf[j + 1] * W2 + kf[j + 2] * W1 + kf[j + 3] * W0;
                o[j] = ksh * bf_2(vv[j][e]);
            }
            const v4u p0 = {pk_bf16(o[0].x, o[1].x), pk_bf16(o[2].x, o[3].x), pk_bf16(o[4].x, o[5].x), pk_bf16(o[6].x, o[7].x)};
            const v4u p1 = {pk_bf16(o[0].y, o[1].y), pk_bf16(o[2].y, o[3].y), pk_bf16(o[4].y, o[5].y), pk_bf16(o[6].y, o[7].y)};
            *(LAS v4u*)(L + (8 * cc + 2 * e) * 128 + ((rg ^ (cc & 7)) * 16)) = p0;
            *(LAS v4u*)(L + (8 * cc + 2 * e + 1) * 128 + ((rg ^ (cc & 7)) * 16)) = p1;
        }
    }
    STAMP(1);
    WAIT_LGKM0();
    BAR();
#pragma unroll
    for (int j = 0; j < 5; ++j) {
        const int n = tid + NTHR * j, ch = n >> 3, pc = n & 7;
        const v4u val = *(LAS v4u*)(L + ch * 128 + ((pc ^ ((ch >> 3) & 7)) * 16));
        st16_sc1(val, rs, WS_KV + (c0 + ch) * (S * 2) + s0 * 2 + pc * 16);
    }
}
struct GateIn { v4u gw[5], q[5]; };
__device__ __forceinline__ void gate_load(const Params& p, GateIn& gi, int row0, int lane) {
    const int i = lane & 15, row = row0 + (lane >> 4);
#pragma unroll
    for (int j = 0; j < 5; ++j) {
        gi.gw[j] = *(const GAS v4u*)((const char*)p.gw + (i + 16 * j) * 16);
        gi.q[j] = *(const GAS v4u*)((const char*)p.q + (size_t)row * (D * 2) + (i + 16 * j) * 16);
    }
}
__device__ __forceinline__ void gate_finish(const GateIn& gi, __amdgpu_buffer_rsrc_t rs, int row0, int lane) {
    f32x2 a2 = {0.f, 0.f};
#pragma unroll
    for (int j = 0; j < 5; ++j)
#pragma unroll
        for (int e = 0; e < 4; ++e) a2 += bf_2(gi.q[j][e]) * bf_2(gi.gw[j][e]);
    const float z = rbf(row_sum16(a2.x + a2.y));
    const float gv = rbf(__builtin_amdgcn_rcpf(1.f + __builtin_amdgcn_exp2f(z * -1.44269504f)));
    if ((lane & 15) == 0) __builtin_amdgcn_raw_buffer_store_b32(__float_as_uint(gv), rs, WS_G + (row0 + (lane >> 4)) * 4, 0, AUX_SC1);
}

__device__ __forceinline__ void conv_channel(const Params& p, LAS char* Lw, __amdgpu_buffer_rsrc_t rs, ConvIn& c, int ch, int chn, int lane STAMP_ARG) {
    const int g = lane >> 4, q = (lane & 15) >> 2, pp = lane & 3, n = lane & 15;
    __builtin_amdgcn_s_setprio(2);
    if (lane < 30) *(LAS v4u*)(Lw + CV_X + lane * 16) = (v4u){0u, 0u, 0u, 0u};
    if (lane < 8) *(LAS v4u*)(Lw + (lane >> 1) * CV_COPY + (lane & 1) * 16) = (v4u){0u, 0u, 0u, 0u};
    if (lane == 0) { c.vp[0] = (v4u){0u, c.v[0].x, c.v[0].y, c.v[0].z}; c.vq[0] = 0u; }
#pragma unroll
    for (int j = 0; j < 4; ++j) {
        const v4u v = c.v[j], vp = c.vp[j];
        const unsigned a0 = __builtin_amdgcn_alignbit(v.x, vp.x, 16), a1 = __builtin_amdgcn_alignbit(v.y, v.x, 16), a2 = __builtin_amdgcn_alignbit(v.z, v.y, 16), a3 = __builtin_amdgcn_alignbit(v.w, v.z, 16);
        const unsigned am = __builtin_amdgcn_alignbit(vp.x, c.vq[j], 16);
        LAS char* d = Lw + 32 + j * 1024 + lane * 16;
        *(LAS v4u*)(d) = v;
        *(LAS v4u*)(d + CV_COPY) = (v4u){a0, a1, a2, a3};
        *(LAS v4u*)(d + 2 * CV_COPY) = vp;
        *(LAS v4u*)(d + 3 * CV_COPY) = (v4u){am, a0, a1, a2};
    }
#pragma unroll
    for (int j = 0; j < 4; ++j) *(LAS v4u*)(Lw + CV_X + 480 + j * 1024 + lane * 16) = c.x[j];
    const float Dd = bf_lo((unsigned)c.Dd);
    WAIT_LGKM0();
    __builtin_amdgcn_s_setprio(0);
    if (chn >= 0) { conv_load_h(p, c, chn, lane); conv_load_x(c, rs, chn, lane); asm volatile("" ::: "memory"); }
    STAMP(8);
    unsigned la = (unsigned)(size_t)Lw + q * CV_COPY + 32 - 16 * g + 8 * pp + 24;
    unsigned lb = (unsigned)(size_t)Lw + CV_X + 32 * n + 16 * g + 448 - 128;
    f32x4 acc[8];
#pragma unroll
    for (int t = 0; t < 8; ++t) acc[t] = (f32x4){0.f, 0.f, 0.f, 0.f};
    v2u Alo[2][8], Ahi[2][8];
    v4u B[2][8];
#define CV_RD(set, ro, k) do { \
        if ((k) % 3 == 0) A_RDTR(Alo[set][(k) / 3], la, (ro) + 512 * ((k) / 3) + 8); \
        else if ((k) % 3 == 1) A_RDTR(Ahi[set][(k) / 3], la, (ro) + 512 * ((k) / 3)); \
        else A_RD128(B[set][(k) / 3], lb, 128 - (ro) + 512 * ((k) / 3)); } while (0)
    auto step = [&](auto SET, auto RO) {
        constexpr int set = decltype(SET)::value, ro = decltype(RO)::value;
        int k = 0;
#pragma unroll
        for (int l = 0; l < 8; ++l)
#pragma unroll
            for (int i = 0; i + l < 8; ++i) {
                const v4u a = {Alo[set][i].x, Alo[set][i].y, Ahi[set][i].x, Ahi[set][i].y};
                A_MFMA(acc[i + l], a, B[set][l]);
                if (k < 24) { CV_RD(1 - set, ro, k); }
                ++k;
            }
        A_LGKM(0);
    };
#pragma unroll
    for (int k = 0; k < 24; ++k) CV_RD(0, 0, k);
    A_LGKM(0);
#pragma unroll 1
    for (int rr = 0; rr < 4; ++rr) {
        step(std::integral_constant<int, 0>{}, std::integral_constant<int, 64>{});
        step(std::integral_constant<int, 1>{}, std::integral_constant<int, 128>{});
        la += 128; lb -= 128;
    }
#undef CV_RD
    A_MFMA_DRAIN();
    STAMP(9);
    v2u xv[8];
#pragma unroll
    for (int t = 0; t < 8; ++t) xv[t] = *(LAS v2u*)(Lw + CV_X + 480 + 2 * (256 * t + 16 * n + 4 * g));
#pragma unroll
    for (int t = 0; t < 8; ++t) {
        const f32x4 y = acc[t];
        const v2u o = {pk2(bf_2(xv[t].x) * Dd + (f32x2){y[0], y[1]}), pk2(bf_2(xv[t].y) * Dd + (f32x2){y[2], y[3]})};
        *(LAS v2u*)(Lw + 512 * t + 32 * n + 8 * (g ^ (n >> 2))) = o;
    }
    WAIT_LGKM0();
    v4u val[4];
#pragma unroll
    for (int j = 0; j < 4; ++j) {
        const int nn = (lane >> 1) & 15, cc = nn >> 2;
        val[j] = *(LAS v4u*)(Lw + 1024 * j + 512 * (lane >> 5) + 32 * nn + 16 * ((lane & 1) ^ (cc >> 1)));
        if (cc & 1) val[j] = (v4u){val[j].z, val[j].w, val[j].x, val[j].y};
    }
#pragma unroll
    for (int j = 0; j < 4; ++j) st16_sc1(val[j], rs, WS_Y + ch * (S * 2) + j * 1024 + lane * 16);
    STAMP(10);
}

__device__ __forceinline__ void g2_stream(const Params& p, LAS char* L, const W2Src& w2s, int panel, int ct, int sw, int lane STAMP_ARG) {
#pragma unroll 1
    for (int k = 6; k < 16; k += 2) {
        WAIT_VM(20); w2_issue(w2s, 2 * k + sw); WAIT_VM(20); w2_issue(w2s, 2 * k + 2 + sw);
        w2_repack2(w2s, L, 2 * (k - 6) + sw, lane);
    }
    STAMP(5);
    BAR();
#pragma unroll
    for (int k = 16; k < 20; ++k) w2_issue(w2s, 2 * k + sw);
    WAIT_VM(16);
#pragma unroll 1
    for (int k = 10; k < 16; k += 2) w2_repack2(w2s, L, 2 * k + sw, lane);
    STAMP(9);
    BAR();
#pragma unroll
    for (int k = 20; k < 24; ++k) w2_issue(w2s, 2 * k + sw);
    WAIT_VM(16);
    w2_repack2(w2s, L, 32 + sw, lane); w2_repack2(w2s, L, 36 + sw, lane);
    BAR();
#pragma unroll
    for (int i = 0; i < 8; ++i) {
        const int r = lane >> 3, c = lane & 7, row = 64 * sw + 8 * i + r;
        glds16((unsigned)(row * (D * 2) + ((c ^ ((row >> 1) & 7)) * 16)), (const char*)p.q + (size_t)panel * 128 * D * 2 + ct * 128, G2_Q + (64 * sw + 8 * i) * 128);
    }
    WAIT_VM(8);
    w2_repack2(w2s, L, 40 + sw, lane); w2_repack2(w2s, L, 44 + sw, lane);
    BAR();
    WAIT_VM(0);
    BAR();
}
__device__ __forceinline__ void g2_helper(const Params& p, LAS char* L, __amdgpu_buffer_rsrc_t rs, unsigned tag, int panel, int ct, int hw, int lane STAMP_ARG) {
    gu32* ctl = (gu32*)(p.ws + WS_CTL);
    if (ct < 8) {
        GateIn gi[2];
#pragma unroll
        for (int t = 0; t < 2; ++t) gate_load(p, gi[t], panel * 128 + ct * 16 + 8 * hw + 4 * t, lane);
#pragma unroll
        for (int t = 0; t < 2; ++t) gate_finish(gi[t], rs, panel * 128 + ct * 16 + 8 * hw + 4 * t, lane);
    }
    if (hw == 0) poll_flags(ctl, W_H + panel * 48, 48, tag, lane);
    STAMP(5);
    BAR();
    WAIT_VM(0);
    BAR();
    if (ct < 8 && hw == 0 && lane == 0) set_flag(ctl, W_G + panel * 8 + ct, tag);
    BAR();
    BAR();
    poll_flags(ctl, W_Y + (ct < 8 ? 8 * ct : 64 + 16 * (ct - 8)), ct < 8 ? 8 : 16, tag, lane);
    poll_flags(ctl, W_G + panel * 8, 8, tag, lane);
    STAMP(6);
    v4u yv[8];
#pragma unroll
    for (int i = 0; i < 8; ++i) {
        const int r = lane >> 4, c = lane & 15, d = 32 * hw + 4 * i + r;
        yv[i] = __builtin_amdgcn_raw_buffer_load_b128(rs, d * (S * 2) + c * 16, WS_Y + ct * 64 * S * 2 + panel * 256, AUX_SC1);
    }
    const unsigned gv = __builtin_amdgcn_raw_buffer_load_b32(rs, (hw * 64 + lane) * 4, WS_G + panel * 512, AUX_SC1);
#pragma unroll
    for (int i = 0; i < 8; ++i) {
        const int r = lane >> 4, c = lane & 15, d = 32 * hw + 4 * i + r;
        *(LAS v4u*)(L + G2_Y + d * 256 + ((c ^ ((d & 7) << 1)) * 16)) = yv[i];
    }
    *(LAS unsigned*)(L + G2_G + (hw * 64 + lane) * 4) = gv;
    WAIT_LGKM0();
    BAR();
}
__device__ __forceinline__ void g2_compute(const Params& p, LAS char* L, __amdgpu_buffer_rsrc_t rs, int panel, int ct, int off2, int w, int lane STAMP_ARG) {
    const int g = lane >> 4, q = (lane & 15) >> 2, pp = lane & 3, s = lane & 15;
    const unsigned hbase = WS_H + (panel * 8 + 2 * w) * (48 * 1024);
    int kld = off2;
    f32x4 acc[4][2];
#pragma unroll
    for (int a = 0; a < 4; ++a) { acc[a][0] = (f32x4){0.f, 0.f, 0.f, 0.f}; acc[a][1] = (f32x4){0.f, 0.f, 0.f, 0.f}; }
    v4u H[G2_HD][2][2];
    auto ldh1 = [&](int slot, int ks, int sx) {
        H[slot][ks][sx] = __builtin_amdgcn_raw_buffer_load_b128(rs, lane * 16 + sx * (48 * 1024) + ks * 1024,
            hbase + kld * 2048, H_AUX);
        asm volatile("" ::: "memory");
    };
    auto ldh = [&](int slot) {
#pragma unroll
        for (int ks = 0; ks < 2; ++ks)
#pragma unroll
            for (int sx = 0; sx < 2; ++sx) ldh1(slot, ks, sx);
        kld = kld + 1 == G2_NST ? 0 : kld + 1;
    };
    BAR();
    STAMP(5);
#pragma unroll
    for (int st = 0; st < G2_HD - 1; ++st) ldh(st);
#pragma unroll
    for (int a = 0; a < 4; ++a) H[G2_HD - 1][a >> 1][a & 1] = (v4u){0u, 0u, 0u, 0u};
    SCHED();
    v4u A[2][4];
#pragma unroll
    for (int a = 0; a < 4; ++a) A[1][a] = (v4u){0u, 0u, 0u, 0u};
    auto half = [&](int set, int slot, int ks, unsigned addr, int off, bool load, int up, int lk) {
        A_LGKM(3);
        A_MFMA(acc[0][0], A[set][0], H[slot][ks][0]); A_RD128(A[1 - set][0], addr, off);
        A_MFMA(acc[0][1], A[set][0], H[slot][ks][1]); A_RD128(A[1 - set][1], addr, off + 1024);
        if (load) ldh1(up, lk, 0);
        A_LGKM(4);
        A_MFMA(acc[1][0], A[set][1], H[slot][ks][0]); A_RD128(A[1 - set][2], addr, off + 2048);
        A_MFMA(acc[1][1], A[set][1], H[slot][ks][1]); A_RD128(A[1 - set][3], addr, off + 3072);
        A_LGKM(5);
        A_MFMA(acc[2][0], A[set][2], H[slot][ks][0]);
        A_MFMA(acc[2][1], A[set][2], H[slot][ks][1]);
        if (load) ldh1(up, lk, 1);
        A_LGKM(4);
        A_MFMA(acc[3][0], A[set][3], H[slot][ks][0]);
        A_MFMA(acc[3][1], A[set][3], H[slot][ks][1]);
    };
    const unsigned lbase = (unsigned)(size_t)L + lane * 16;
    unsigned sb = 0;
    auto stage = [&](int u, bool load, bool drain) {
        const unsigned addr = lbase + sb;
        const int up = (u + G2_HD - 1) % G2_HD;
        half(1, up, 1, addr, 0, load, up, 0);
        half(0, u, 0, addr, 4096, load, up, 1);
        if (load) kld = kld + 1 == G2_NST ? 0 : kld + 1;
        if (drain) WAIT_LGKM0();
        sb = sb + 8192 == G2_RING ? 0 : sb + 8192;
    };
#pragma unroll 1
    for (int jj = 0; jj < 5; ++jj) {
        if (jj >= 1 && jj <= 3) BAR();
        if (jj == 0) STAMP(14); else if (jj == 2) STAMP(12); else if (jj == 4) STAMP(13);
#pragma unroll
        for (int u = 0; u < 4; ++u) stage(u, true, u == 3);
    }
#pragma unroll
    for (int u = 0; u < 4; ++u) stage(u, u == 0, false);
    WAIT_LGKM0();
#pragma unroll
    for (int dt = 0; dt < 4; ++dt) { A_MFMA(acc[dt][0], A[1][dt], H[G2_HD - 1][1][0]); A_MFMA(acc[dt][1], A[1][dt], H[G2_HD - 1][1][1]); }
    A_MFMA_DRAIN();
    STAMP(6);
    BAR();
    STAMP(7);
    {
        float gg[2]; v2u yv[2][4], qv[2][4];
#pragma unroll
        for (int sx = 0; sx < 2; ++sx) {
            const int row = 32 * w + 16 * sx + s;
            gg[sx] = *(LAS float*)(L + G2_G + row * 4);
#pragma unroll
            for (int dt = 0; dt < 4; ++dt) {
                const int d = 16 * dt + 4 * g + q;
                yv[sx][dt] = tr_read(L + G2_Y + d * 256 + (((2 * w + sx) ^ (d & 7)) * 32) + 8 * pp);
                qv[sx][dt] = *(LAS v2u*)(L + G2_Q + row * 128 + (((2 * dt + (g >> 1)) ^ ((row >> 1) & 7)) * 16) + 8 * (g & 1));
            }
        }
        SCHED();
#pragma unroll
        for (int sx = 0; sx < 2; ++sx) {
            const int row = 32 * w + 16 * sx + s;
            const float omg = 1.f - gg[sx];
#pragma unroll
            for (int dt = 0; dt < 4; ++dt) {
                const f32x4 a = acc[dt][sx];
                const f32x2 o01 = bf_2(qv[sx][dt].x) * bf_2(yv[sx][dt].x) * gg[sx] + (f32x2){a[0], a[1]} * omg;
                const f32x2 o23 = bf_2(qv[sx][dt].y) * bf_2(yv[sx][dt].y) * gg[sx] + (f32x2){a[2], a[3]} * omg;
                *(LAS v2u*)(L + G2_Q + row * 128 + (((2 * dt + (g >> 1)) ^ ((row >> 1) & 7)) * 16) + 8 * (g & 1)) = (v2u){pk2(o01), pk2(o23)};
            }
        }
    }
    WAIT_LGKM0();
#pragma unroll
    for (int i = 0; i < 4; ++i) {
        const int row = 32 * w + 8 * i + (lane >> 3), c = lane & 7;
        const v4u val = *(LAS v4u*)(L + G2_Q + row * 128 + c * 16);
        *(GAS v4u*)((char*)p.out + (size_t)(panel * 128 + row) * (D * 2) + ct * 128 + ((c ^ ((row >> 1) & 7)) * 16)) = val;
    }
    STAMP(11);
}

__global__ void __launch_bounds__(NTHR, 2) k_fused(Params p) {
    extern __shared__ __attribute__((aligned(16))) char smem[];
    LAS char* L = (LAS char*)smem;
    const int tid = threadIdx.x, lane = tid & 63, w = __builtin_amdgcn_readfirstlane(tid >> 6);
    const int x = blockIdx.x & 7, idx = blockIdx.x >> 3, xr = x >> 1, xc = x & 1;
    STAMP(0);
    gu32* ctl = (gu32*)(p.ws + WS_CTL);
    const __amdgpu_buffer_rsrc_t rs = __builtin_amdgcn_make_buffer_rsrc(p.ws, 0, WS_END, 0x00020000);
#define GET_TAG() const unsigned tag = __builtin_amdgcn_readfirstlane(__hip_atomic_load(ctl + W_EPOCH * FL, RLX_AGENT)) + 1u
    if (idx < 24) {
        const int panel = 4 * xr + idx / 6, ct = 6 * xc + idx % 6;
        const int off1 = 5 * ((idx / 6 + idx % 6) & 1);
        const bool typeA = idx < 20;
        const int panel2 = 4 * xr + idx / 5, ct2 = 5 * xc + idx % 5;
        const int off2 = ((idx % 5) * 24 / 5 + (idx / 5) * 6) % 24;
        const int bw = x * 4 + idx - 20;
        if (w < 4) {
            GET_TAG();
            ConvIn cin;
            const int ch = 512 + 4 * bw + w;
            if (typeA) g1_compute<true>(p, L, rs, panel, ct, w, lane, cin, 0 STAMP_PASS);
            else g1_compute<false>(p, L, rs, panel, ct, w, lane, cin, ch STAMP_PASS);
            STAMP(3);
            WAIT_VM(0);
            if (lane == 0) set_flag(ctl, W_H + (panel * 12 + ct) * 4 + w, tag);
            STAMP(4);
            if (typeA) g2_compute(p, L, rs, panel2, ct2, off2, w, lane STAMP_PASS);
            else {
                BAR();
                conv_load_x(cin, rs, ch, lane);
                conv_channel(p, L + w * CV_PAIR, rs, cin, ch, -1, lane STAMP_PASS);
                WAIT_VM(0);
                BAR();
                if (tid == 0) set_flag(ctl, W_Y + 64 + bw, tag);
                STAMP(16);
            }
        } else if (w < 6) {
            __builtin_amdgcn_s_setprio(2);
            W2Src w2s;
            w2_init(w2s, p, ct2, off2, lane);
            if (typeA) {
                g1_stream<true>(p, panel, ct, off1, w2s, w - 4, lane STAMP_PASS);
                g2_stream(p, L, w2s, panel2, ct2, w - 4, lane STAMP_PASS);
            } else {
                g1_stream<false>(p, panel, ct, off1, w2s, w - 4, lane STAMP_PASS);
                BAR();
                BAR();
            }
        } else {
            GET_TAG();
#pragma unroll 1
            for (int i = 0; i < G1_NST; ++i) BAR();
            if (typeA) g2_helper(p, L, rs, tag, panel2, ct2, w - 6, lane STAMP_PASS);
            else {
                if (w == 7) poll_flags(ctl, W_KV, 64, tag, lane);
                BAR();
                BAR();
            }
        }
    } else {
        const int cw = x * 8 + (idx - 24);
        const int ch = 8 * cw + (w < 7 ? w : 0);
        ConvIn cin;
        unsigned epoch_v = __hip_atomic_load(ctl + W_EPOCH * FL, RLX_AGENT);
        conv_load_h(p, cin, ch, lane);
        prepass(p, L, rs, cw, tid STAMP_PASS);
        STAMP(2);
        WAIT_VM(0);
        asm volatile("" : "+v"(epoch_v));
        const unsigned tag = __builtin_amdgcn_readfirstlane(epoch_v) + 1u;
        BAR();
        if (tid == 0) set_flag(ctl, W_KV + cw, tag);
        STAMP(3);
        if (w == 7) poll_flags(ctl, W_KV, 64, tag, lane);
        BAR();
        STAMP(4);
        if (w < 7) {
            conv_load_x(cin, rs, ch, lane);
            int c0 = ch, c1 = w == 3 ? 8 * cw + 7 : -1;
#pragma unroll 1
            for (int round = 0; round < 2; ++round) {
                conv_channel(p, L + w * CV_PAIR, rs, cin, c0, c1, lane STAMP_PASS);
                if (c1 < 0) break;
                c0 = c1; c1 = -1;
            }
        }
        WAIT_VM(0);
        BAR();
        if (tid == 0) set_flag(ctl, W_Y + cw, tag);
        STAMP(16);
        if (w == 7 && blockIdx.x == 255) {
#pragma unroll 1
            for (int pn = 0; pn < 12; ++pn) poll_flags(ctl, W_H + pn * 64, 64, tag, lane);
            if (lane == 0) set_flag(ctl, W_EPOCH, tag);
        }
    }
    STAMP_FLUSH();
}

extern "C" void kernel_launch(void* const* d_in, const int* in_sizes, int n_in, void* d_out, int out_size, void* d_ws, size_t ws_size, hipStream_t stream) {
    Params p;
    p.q = (const u16*)d_in[0]; p.k = (const u16*)d_in[1]; p.v = (const u16*)d_in[2]; p.shk = (const u16*)d_in[3];
    p.ssm = (const u16*)d_in[4]; p.Dv = (const u16*)d_in[5]; p.w1 = (const u16*)d_in[7]; p.w2 = (const u16*)d_in[8]; p.gw = (const u16*)d_in[9];
    p.out = (u16*)d_out; p.ws = (char*)d_ws;
    (void)hipFuncSetAttribute((const void*)k_fused, hipFuncAttributeMaxDynamicSharedMemorySize, LDS_BYTES);
    k_fused<<<256, NTHR, LDS_BYTES, stream>>>(p);
}
```

```cpp
#define X_CODEPF 35072
#define X_CODEPF_WGS 1
#include <hip/hip_runtime.h>
#include <stdint.h>
#include <type_traits>

typedef unsigned short u16;
typedef unsigned v4u __attribute__((ext_vector_type(4)));
typedef unsigned v2u __attribute__((ext_vector_type(2)));
typedef float f32x4 __attribute__((ext_vector_type(4)));
typedef float f32x2 __attribute__((ext_vector_type(2)));
typedef short bf16x8 __attribute__((ext_vector_type(8)));
typedef short s16x4 __attribute__((ext_vector_type(4)));
typedef __bf16 bf16x2_t __attribute__((ext_vector_type(2)));
#define LAS __attribute__((address_space(3)))
#define GAS __attribute__((address_space(1)))
typedef GAS unsigned gu32;
typedef GAS unsigned long long gu64;
#define RLX_AGENT __ATOMIC_RELAXED, __HIP_MEMORY_SCOPE_AGENT

constexpr int S = 2048, D = 640, F = 1536;
constexpr int NTHR = 512;
constexpr unsigned WS_H = 0;
constexpr unsigned WS_KV = WS_H + S * F * 2;
constexpr unsigned WS_Y = WS_KV + S * D * 2;
constexpr unsigned WS_G = WS_Y + S * D * 2;
constexpr unsigned WS_CTL = WS_G + S * 4;
constexpr int FL = 32;
constexpr int W_EPOCH = 0, W_H = 1, W_KV = W_H + 768, W_G = W_KV + 64, W_Y = W_G + 128, W_LINES = W_Y + 96;
constexpr unsigned WS_END = WS_CTL + W_LINES * 128;
constexpr int AUX_SC1 = 16;
constexpr int H_AUX = 16;
constexpr unsigned kSpinCap = 1u << 18;

struct Params {
    const u16 *q, *k, *v, *shk, *ssm, *Dv, *w1, *w2, *gw;
    u16* out;
    char* ws;
};

__device__ __forceinline__ float bf_lo(unsigned u) { return __uint_as_float(u << 16); }
__device__ __forceinline__ float bf_hi(unsigned u) { return __uint_as_float(u & 0xffff0000u); }
__device__ __forceinline__ f32x2 bf_2(unsigned u) { return (f32x2){bf_lo(u), bf_hi(u)}; }
__device__ __forceinline__ unsigned pk2(f32x2 v) { return __builtin_bit_cast(unsigned, __builtin_convertvector(v, bf16x2_t)); }
__device__ __forceinline__ unsigned pk_bf16(float lo, float hi) { return pk2((f32x2){lo, hi}); }
__device__ __forceinline__ float rbf(float x) { return bf_lo(pk_bf16(x, x)); }
__device__ __forceinline__ f32x4 mfma16(v4u a, v4u b, f32x4 c) { return __builtin_amdgcn_mfma_f32_16x16x32_bf16(__builtin_bit_cast(bf16x8, a), __builtin_bit_cast(bf16x8, b), c, 0, 0, 0); }
__device__ __forceinline__ v2u tr_read(LAS char* p) { return __builtin_bit_cast(v2u, __builtin_amdgcn_ds_read_tr16_b64_v4i16((LAS s16x4*)p)); }
__device__ __forceinline__ v4u lds_rd16(LAS char* p) { return *(LAS v4u*)p; }
__device__ __forceinline__ void glds16(unsigned voff, const void* sbase, unsigned lds_dst) {
    asm volatile("s_mov_b32 m0, %2\n\ts_nop 0\n\tglobal_load_lds_dwordx4 %0, %1" :: "v"(voff), "s"(sbase), "s"(lds_dst) : "memory");
}
template <bool NT> __device__ __forceinline__ void glds16t(unsigned voff, const void* sbase, unsigned lds_dst) {
    if (NT) asm volatile("s_mov_b32 m0, %2\n\ts_nop 0\n\tglobal_load_lds_dwordx4 %0, %1 nt" :: "v"(voff), "s"(sbase), "s"(lds_dst) : "memory");
    else glds16(voff, sbase, lds_dst);
}
#define NT_X 0
#define NT_W1 0
#define NT_W2 0
#define NT_KV 0
#define NT_SSM 0
template <bool NT> __device__ __forceinline__ v4u gld16(const void* ptr) {
    if (NT) return __builtin_nontemporal_load((const GAS v4u*)ptr);
    return *(const GAS v4u*)ptr;
}
#define WAIT_VM(n) asm volatile("s_waitcnt vmcnt(%0)" :: "n"(n) : "memory")
#define WAIT_LGKM0() asm volatile("s_waitcnt lgkmcnt(0)" ::: "memory")
#define BAR() do { asm volatile("" ::: "memory"); __builtin_amdgcn_s_barrier(); asm volatile("" ::: "memory"); } while (0)
#define SCHED() __builtin_amdgcn_sched_barrier(0)
#define A_MFMA(acc, a, b) asm volatile("v_mfma_f32_16x16x32_bf16 %0, %1, %2, %0" : "+v"(acc) : "v"(a), "v"(b))
#define A_RD128(dst, addr, off) asm volatile("ds_read_b128 %0, %1 offset:%2" : "=v"(dst) : "v"(addr), "n"(off))
#define A_RDTR(dst, addr, off) asm volatile("ds_read_b64_tr_b16 %0, %1 offset:%2" : "=v"(dst) : "v"(addr), "n"(off))
#define A_LGKM(n) asm volatile("s_waitcnt lgkmcnt(%0)" :: "n"(n))
#define A_MFMA_DRAIN() asm volatile("s_nop 15\n\ts_nop 7" ::: "memory")
template <int NM, int NR, int PER> __device__ __forceinline__ void interleave() {
#pragma unroll
    for (int i = 0; i < NM; ++i) {
        __builtin_amdgcn_sched_group_barrier(0x8, 1, 0);
        if (i * PER < NR) __builtin_amdgcn_sched_group_barrier(0x100, PER, 0);
    }
}
#define STAMP(id) do {} while (0)
#define STAMP_ARG
#define STAMP_PASS
#define STAMP_FLUSH() do {} while (0)

__device__ __forceinline__ void st16_sc1(v4u v, __amdgpu_buffer_rsrc_t rs, unsigned off) { __builtin_amdgcn_raw_buffer_store_b128(v, rs, off, 0, AUX_SC1); }
__device__ __forceinline__ void poll_flags(gu32* ctl, int line, int n, unsigned tag, int lane) {
    gu32* f = ctl + (line + (lane < n ? lane : 0)) * FL;
#pragma unroll 1
    for (unsigned spins = 0; spins < kSpinCap; ++spins) {
        const unsigned v = __hip_atomic_load(f, RLX_AGENT);
        if (__all(v == tag)) break;
        __builtin_amdgcn_s_sleep(4);
    }
    __builtin_amdgcn_fence(__ATOMIC_ACQUIRE, "wavefront");
}
__device__ __forceinline__ void set_flag(gu32* ctl, int line, unsigned tag) { __hip_atomic_store(ctl + line * FL, tag, RLX_AGENT); }
__device__ __forceinline__ unsigned silu2f(f32x2 h) {
    const f32x2 t = h * -1.44269504f;
    const f32x2 den = (f32x2){__builtin_amdgcn_exp2f(t.x), __builtin_amdgcn_exp2f(t.y)} + 1.f;
    const f32x2 r = (f32x2){__builtin_amdgcn_rcpf(den.x), __builtin_amdgcn_rcpf(den.y)};
    return pk2(h * r);
}
__device__ __forceinline__ unsigned silu2(unsigned hb) { return silu2f(bf_2(hb)); }
__device__ __forceinline__ float row_sum16(float v) {
    v += __builtin_bit_cast(float, __builtin_amdgcn_update_dpp(0, __builtin_bit_cast(int, v), 0x128, 0xf, 0xf, false));
    v += __builtin_bit_cast(float, __builtin_amdgcn_update_dpp(0, __builtin_bit_cast(int, v), 0x124, 0xf, 0xf, false));
    v += __builtin_bit_cast(float, __builtin_amdgcn_update_dpp(0, __builtin_bit_cast(int, v), 0x122, 0xf, 0xf, false));
    v += __builtin_bit_cast(float, __builtin_amdgcn_update_dpp(0, __builtin_bit_cast(int, v), 0x121, 0xf, 0xf, false));
    return v;
}

constexpr int G1_SLOT = 32768, G1_NST = D / 64;
__device__ __forceinline__ unsigned g1_slot(int st) { return (unsigned)(1 + ((st + 2) & 3)) * G1_SLOT; }
#define X_HD 4
constexpr int G2_NST = F / 64, G2_RING = 40 * 4096, G2_HD = X_HD;
constexpr int G2_Q = 8 * 4096, G2_Y = 12 * 4096, G2_G = 16 * 4096;
constexpr int CV_COPY = 4160, CV_X = 4 * CV_COPY, CV_PAIR = CV_X + 4608;
constexpr int LDS_BYTES = 163840;
static_assert(7 * CV_PAIR <= LDS_BYTES, "LDS map");

struct W2Src { unsigned vw[4]; const char* base; int kb0; };
__device__ __forceinline__ void w2_init(W2Src& s, const Params& p, int ct, int off2, int lane) {
#pragma unroll
    for (int i = 0; i < 4; ++i) {
        const int r = lane >> 3, c = lane & 7, k = 8 * i + r;
        s.vw[i] = (unsigned)(k * (D * 2) + ((c ^ (((k >> 1) & 3) << 1)) * 16));
    }
    s.base = (const char*)p.w2 + ct * 128; s.kb0 = 2 * off2;
}
__device__ __forceinline__ void w2_issue(const W2Src& s, int b) {
    const int kb = s.kb0 + b >= 48 ? s.kb0 + b - 48 : s.kb0 + b;
    const char* src = s.base + (size_t)kb * (32 * D * 2);
    const unsigned slot = (unsigned)(b >= 40 ? b - 40 : b) * 4096;
#pragma unroll
    for (int i = 0; i < 4; ++i) glds16t<NT_W2>(s.vw[i], src, slot + i * 1024);
}
struct G1Src { unsigned vx[2], vw[2]; const char *xb, *wb; int kx, kw; };
__device__ __forceinline__ void g1s_x(G1Src& g, int st, int part, int sw) {
#pragma unroll
    for (int i = 4 * part; i < 4 * part + 4; ++i) glds16t<NT_X>(g.vx[i & 1], g.xb + g.kx * 128 + i * (8 * D * 2), g1_slot(st) + (64 * sw + 8 * i) * 128);
    if (part == 1) g.kx = g.kx + 1 == G1_NST ? 0 : g.kx + 1;
}
__device__ __forceinline__ void g1s_w(G1Src& g, int st, int part, int sw) {
#pragma unroll
    for (int i = 4 * part; i < 4 * part + 4; ++i) glds16t<NT_W1>(g.vw[(i >> 1) & 1], g.wb + (size_t)g.kw * (64 * F * 2) + i * (4 * F * 2), g1_slot(st) + 16384 + (32 * sw + 4 * i) * 256);
    if (part == 1) g.kw = g.kw + 1 == G1_NST ? 0 : g.kw + 1;
}
__device__ __forceinline__ void g1_stream_start(G1Src& g, const Params& p, int panel, int ct, int off1, int sw, int lane) {
    const int r = lane >> 3, c = lane & 7, r2 = lane >> 4, c2 = lane & 15;
    g.vx[0] = (unsigned)((64 * sw + r) * (D * 2) + ((c ^ (r >> 1)) * 16));
    g.vx[1] = (unsigned)((64 * sw + r) * (D * 2) + ((c ^ ((r >> 1) | 4)) * 16));
    g.vw[0] = (unsigned)((32 * sw + r2) * (F * 2) + ((c2 ^ (r2 << 1)) * 16));
    g.vw[1] = (unsigned)((32 * sw + r2) * (F * 2) + ((c2 ^ ((r2 | 4) << 1)) * 16));
    g.xb = (const char*)p.q + (size_t)panel * 128 * D * 2;
    g.wb = (const char*)p.w1 + (size_t)ct * 256;
    g.kx = off1; g.kw = off1;
    g1s_x(g, 0, 0, sw); g1s_x(g, 0, 1, sw); g1s_w(g, 0, 0, sw); g1s_w(g, 0, 1, sw); g1s_x(g, 1, 0, sw); g1s_x(g, 1, 1, sw);
}
template <bool TYPE_A>
__device__ __forceinline__ void g1_stream(G1Src& g, const W2Src& w2s, int sw STAMP_ARG) {
    WAIT_VM(20); g1s_w(g, 1, 0, sw); WAIT_VM(20); g1s_w(g, 1, 1, sw); WAIT_VM(20); g1s_x(g, 2, 0, sw); WAIT_VM(20); g1s_x(g, 2, 1, sw);
    BAR();
#pragma unroll 1
    for (int i = 0; i < 7; ++i) {
        WAIT_VM(20); g1s_w(g, i + 2, 0, sw); WAIT_VM(20); g1s_w(g, i + 2, 1, sw); WAIT_VM(20); g1s_x(g, i + 3, 0, sw); WAIT_VM(20); g1s_x(g, i + 3, 1, sw);
        BAR();
    }
    STAMP(1);
    WAIT_VM(20); g1s_w(g, 9, 0, sw); WAIT_VM(20); g1s_w(g, 9, 1, sw);
    if (TYPE_A) {
        WAIT_VM(20); w2_issue(w2s, 0 + sw); WAIT_VM(20); w2_issue(w2s, 2 + sw); BAR();
#pragma unroll
        for (int k = 2; k < 6; ++k) { WAIT_VM(20); w2_issue(w2s, 2 * k + sw); }
        BAR();
    } else {
        WAIT_VM(16); BAR(); WAIT_VM(0); BAR();
    }
}
struct ConvIn { v4u v[4], vp[4], x[4]; unsigned vq[4]; u16 Dd; };
__device__ __forceinline__ void conv_load_h(const Params& p, ConvIn& c, int ch, int lane) {
    const char* hsrc = (const char*)p.ssm + (size_t)ch * (S * 2);
#pragma unroll
    for (int j = 0; j < 4; ++j) {
        const int o = j * 1024 + lane * 16;
        c.v[j] = gld16<NT_SSM>(hsrc + o);
        c.vp[j] = gld16<NT_SSM>(hsrc + (o < 4 ? 0 : o - 4));
        c.vq[j] = *(const GAS unsigned*)(hsrc + (o < 8 ? 0 : o - 8));
    }
    c.Dd = p.Dv[ch];
}
__device__ __forceinline__ void conv_load_x(ConvIn& c, __amdgpu_buffer_rsrc_t rs, int ch, int lane) {
#pragma unroll
    for (int j = 0; j < 4; ++j) c.x[j] = __builtin_amdgcn_raw_buffer_load_b128(rs, j * 1024 + lane * 16, WS_KV + ch * (S * 2), AUX_SC1);
}
template <bool TYPE_A>
__device__ __forceinline__ void g1_compute(const Params& p, LAS char* L, __amdgpu_buffer_rsrc_t rs, int panel, int ct, int w, int lane, ConvIn& cin, int ch, LAS char* done STAMP_ARG) {
    const int wf = w & 1, wsx = w >> 1;
    const int g = lane >> 4, q = (lane & 15) >> 2, pp = lane & 3, s = lane & 15;
    const int phi = q | ((g & 1) << 2);
    unsigned aw[4], ax[2];
#pragma unroll
    for (int ft = 0; ft < 4; ++ft) aw[ft] = 16384 + (8 * g + q) * 256 + (((4 * wf + ft) ^ phi) * 32) + 8 * pp;
#pragma unroll
    for (int ks = 0; ks < 2; ++ks) ax[ks] = (64 * wsx + s) * 128 + (((4 * ks + g) ^ ((s >> 1) & 7)) * 16);
    f32x4 acc[4][4];
#pragma unroll
    for (int a = 0; a < 4; ++a)
#pragma unroll
        for (int b = 0; b < 4; ++b) acc[a][b] = (f32x4){0.f, 0.f, 0.f, 0.f};
    v4u A[2][4], B[2][4];
#pragma unroll
    for (int a = 0; a < 4; ++a) { A[1][a] = (v4u){0u, 0u, 0u, 0u}; B[1][a] = (v4u){0u, 0u, 0u, 0u}; }
    auto rd = [&](int set, LAS char* base, int ks) {
#pragma unroll
        for (int ft = 0; ft < 4; ++ft) {
            const v2u lo = tr_read(base + aw[ft] + ks * 8192), hi = tr_read(base + aw[ft] + ks * 8192 + 1024);
            A[set][ft] = (v4u){lo.x, lo.y, hi.x, hi.y};
            if (ft == 0) {
#pragma unroll
                for (int sx = 0; sx < 4; ++sx) B[set][sx] = lds_rd16(base + ax[ks] + sx * 2048);
            }
        }
    };
    auto mm = [&](int set) {
#pragma unroll
        for (int ft = 0; ft < 4; ++ft)
#pragma unroll
            for (int sx = 0; sx < 4; ++sx) acc[ft][sx] = mfma16(A[set][ft], B[set][sx], acc[ft][sx]);
    };
#pragma unroll 1
    for (int i = 0; i < G1_NST; ++i) {
        LAS char* base = L + g1_slot(i);
        BAR();
        if (i == 0) STAMP(1);
        rd(0, base, 0);
        mm(1);
        interleave<16, 12, 1>();
        SCHED();
        rd(1, base, 1);
        mm(0);
        interleave<16, 12, 1>();
        SCHED();
        WAIT_LGKM0();
    }
    mm(1);
    STAMP(2);
    if (!TYPE_A) {
        __builtin_amdgcn_s_setprio(3);
        if (lane == 0) *(LAS unsigned*)(done + 4 * w) = 1u;
    }
    SCHED();
#pragma unroll
    for (int j = 0; j < 8; ++j) {
        const f32x4 a = acc[2 * (j & 1)][j >> 1], b = acc[2 * (j & 1) + 1][j >> 1];
        const v4u o = {silu2f((f32x2){a[0], a[1]}), silu2f((f32x2){a[2], a[3]}), silu2f((f32x2){b[0], b[1]}), silu2f((f32x2){b[2], b[3]})};
        st16_sc1(o, rs, WS_H + ((panel * 8 + wsx * 4 + (j >> 1)) * 48 + ct * 4 + wf * 2 + (j & 1)) * 1024 + lane * 16);
    }
}

__device__ __forceinline__ void prepass(const Params& p, LAS char* L, __amdgpu_buffer_rsrc_t rs, int wg, int tid STAMP_ARG) {
    const int s0 = 64 * (wg >> 1), c0 = 320 * (wg & 1);
    {
        const int w = __builtin_amdgcn_readfirstlane(tid >> 6);
        const int item = w < 4 ? tid : 256 + (tid & 63);
        const int cc = item % 40, rg = item / 40;
        const int ch = c0 + 8 * cc, r0 = s0 + 8 * rg;
        v4u kk[11], vv[8], sw[4];
#pragma unroll
        for (int j = 0; j < 4; ++j) sw[j] = *(const GAS v4u*)((const char*)p.shk + (size_t)ch * 8 + j * 16);
#pragma unroll
        for (int j = 0; j < 11; ++j) {
            const int row = r0 - 3 + j;
            kk[j] = gld16<NT_KV>((const char*)p.k + (size_t)(row < 0 ? 0 : row) * (D * 2) + ch * 2);
        }
#pragma unroll
        for (int j = 0; j < 8; ++j) vv[j] = gld16<NT_KV>((const char*)p.v + (size_t)(r0 + j) * (D * 2) + ch * 2);
        if (r0 == 0) { kk[0] = (v4u){0u, 0u, 0u, 0u}; kk[1] = kk[0]; kk[2] = kk[0]; }
#pragma unroll
        for (int e = 0; e < 4; ++e) {
            if (w >= 4 && w - 4 != e) continue;
            const f32x2 W0 = {bf_lo(sw[e].x), bf_lo(sw[e].z)}, W1 = {bf_hi(sw[e].x), bf_hi(sw[e].z)};
            const f32x2 W2 = {bf_lo(sw[e].y), bf_lo(sw[e].w)}, W3 = {bf_hi(sw[e].y), bf_hi(sw[e].w)};
            f32x2 kf[11], o[8];
#pragma unroll
            for (int j = 0; j < 11; ++j) kf[j] = bf_2(kk[j][e]);
#pragma unroll
            for (int j = 0; j < 8; ++j) {
                const f32x2 ksh = kf[j] * W3 + kf[j + 1] * W2 + kf[j + 2] * W1 + kf[j + 3] * W0;
                o[j] = ksh * bf_2(vv[j][e]);
            }
            const v4u p0 = {pk_bf16(o[0].x, o[1].x), pk_bf16(o[2].x, o[3].x), pk_bf16(o[4].x, o[5].x), pk_bf16(o[6].x, o[7].x)};
            const v4u p1 = {pk_bf16(o[0].y, o[1].y), pk_bf16(o[2].y, o[3].y), pk_bf16(o[4].y, o[5].y), pk_bf16(o[6].y, o[7].y)};
            *(LAS v4u*)(L + (8 * cc + 2 * e) * 128 + ((rg ^ (cc & 7)) * 16)) = p0;
            *(LAS v4u*)(L + (8 * cc + 2 * e + 1) * 128 + ((rg ^ (cc & 7)) * 16)) = p1;
        }
    }
    STAMP(1);
    WAIT_LGKM0();
    BAR();
#pragma unroll
    for (int j = 0; j < 5; ++j) {
        const int n = tid + NTHR * j, ch = n >> 3, pc = n & 7;
        const v4u val = *(LAS v4u*)(L + ch * 128 + ((pc ^ ((ch >> 3) & 7)) * 16));
        st16_sc1(val, rs, WS_KV + (c0 + ch) * (S * 2) + s0 * 2 + pc * 16);
    }

}
struct GateIn { v4u gw[5], q[5]; };
__device__ __forceinline__ void gate_load(const Params& p, GateIn& gi, int row0, int lane) {
    const int i = lane & 15, row = row0 + (lane >> 4);
#pragma unroll
    for (int j = 0; j < 5; ++j) {
        gi.gw[j] = *(const GAS v4u*)((const char*)p.gw + (i + 16 * j) * 16);
        gi.q[j] = *(const GAS v4u*)((const char*)p.q + (size_t)row * (D * 2) + (i + 16 * j) * 16);
    }
}
__device__ __forceinline__ void gate_finish(const GateIn& gi, __amdgpu_buffer_rsrc_t rs, int row0, int lane) {
    f32x2 a2 = {0.f, 0.f};
#pragma unroll
    for (int j = 0; j < 5; ++j)
#pragma unroll
        for (int e = 0; e < 4; ++e) a2 += bf_2(gi.q[j][e]) * bf_2(gi.gw[j][e]);
    const float z = rbf(row_sum16(a2.x + a2.y));
    const float gv = rbf(__builtin_amdgcn_rcpf(1.f + __builtin_amdgcn_exp2f(z * -1.44269504f)));
    if ((lane & 15) == 0) __builtin_amdgcn_raw_buffer_store_b32(__float_as_uint(gv), rs, WS_G + (row0 + (lane >> 4)) * 4, 0, AUX_SC1);
}

__device__ __forceinline__ int y_producer(int ch) { return ch < 448 ? ch / 7 : 64 + (ch - 448) / 6; }
__device__ __forceinline__ void conv_channel(const Params& p, LAS char* Lw, __amdgpu_buffer_rsrc_t rs, ConvIn& c, int ch, int chn, int lane STAMP_ARG) {
    const int g = lane >> 4, q = (lane & 15) >> 2, pp = lane & 3, n = lane & 15;
    __builtin_amdgcn_s_setprio(2);
    if (lane < 30) *(LAS v4u*)(Lw + CV_X + lane * 16) = (v4u){0u, 0u, 0u, 0u};
    if (lane < 8) *(LAS v4u*)(Lw + (lane >> 1) * CV_COPY + (lane & 1) * 16) = (v4u){0u, 0u, 0u, 0u};
    if (lane == 0) { c.vp[0] = (v4u){0u, c.v[0].x, c.v[0].y, c.v[0].z}; c.vq[0] = 0u; }
#pragma unroll
    for (int j = 0; j < 4; ++j) {
        const v4u v = c.v[j], vp = c.vp[j];
        const unsigned a0 = __builtin_amdgcn_alignbit(v.x, vp.x, 16), a1 = __builtin_amdgcn_alignbit(v.y, v.x, 16), a2 = __builtin_amdgcn_alignbit(v.z, v.y, 16), a3 = __builtin_amdgcn_alignbit(v.w, v.z, 16);
        const unsigned am = __builtin_amdgcn_alignbit(vp.x, c.vq[j], 16);
        LAS char* d = Lw + 32 + j * 1024 + lane * 16;
        *(LAS v4u*)(d) = v;
        *(LAS v4u*)(d + CV_COPY) = (v4u){a0, a1, a2, a3};
        *(LAS v4u*)(d + 2 * CV_COPY) = vp;
        *(LAS v4u*)(d + 3 * CV_COPY) = (v4u){am, a0, a1, a2};
    }
#pragma unroll
    for (int j = 0; j < 4; ++j) *(LAS v4u*)(Lw + CV_X + 480 + j * 1024 + lane * 16) = c.x[j];
    const float Dd = bf_lo((unsigned)c.Dd);
    WAIT_LGKM0();
    __builtin_amdgcn_s_setprio(0);
    if (chn >= 0) { conv_load_h(p, c, chn, lane); conv_load_x(c, rs, chn, lane); asm volatile("" ::: "memory"); }
    STAMP(8);
    unsigned la = (unsigned)(size_t)Lw + q * CV_COPY + 32 - 16 * g + 8 * pp + 24;
    unsigned lb = (unsigned)(size_t)Lw + CV_X + 32 * n + 16 * g + 448 - 128;
    f32x4 acc[8];
#pragma unroll
    for (int t = 0; t < 8; ++t) acc[t] = (f32x4){0.f, 0.f, 0.f, 0.f};
    v2u Alo[2][8], Ahi[2][8];
    v4u B[2][8];
#define CV_RD(set, ro, k) do { \
        if ((k) % 3 == 0) A_RDTR(Alo[set][(k) / 3], la, (ro) + 512 * ((k) / 3) + 8); \
        else if ((k) % 3 == 1) A_RDTR(Ahi[set][(k) / 3], la, (ro) + 512 * ((k) / 3)); \
        else A_RD128(B[set][(k) / 3], lb, 128 - (ro) + 512 * ((k) / 3)); } while (0)
    auto step = [&](auto SET, auto RO) {
        constexpr int set = decltype(SET)::value, ro = decltype(RO)::value;
        int k = 0;
#pragma unroll
        for (int l = 0; l < 8; ++l)
#pragma unroll
            for (int i = 0; i + l < 8; ++i) {
                const v4u a = {Alo[set][i].x, Alo[set][i].y, Ahi[set][i].x, Ahi[set][i].y};
                A_MFMA(acc[i + l], a, B[set][l]);
                if (k < 24) { CV_RD(1 - set, ro, k); }
                ++k;
            }
        A_LGKM(0);
    };
#pragma unroll
    for (int k = 0; k < 24; ++k) CV_RD(0, 0, k);
    A_LGKM(0);
#pragma unroll 1
    for (int rr = 0; rr < 4; ++rr) {
        step(std::integral_constant<int, 0>{}, std::integral_constant<int, 64>{});
        step(std::integral_constant<int, 1>{}, std::integral_constant<int, 128>{});
        la += 128; lb -= 128;
    }
#undef CV_RD
    A_MFMA_DRAIN();
    STAMP(9);
    v2u xv[8];
#pragma unroll
    for (int t = 0; t < 8; ++t) xv[t] = *(LAS v2u*)(Lw + CV_X + 480 + 2 * (256 * t + 16 * n + 4 * g));
#pragma unroll
    for (int t = 0; t < 8; ++t) {
        const f32x4 y = acc[t];
        const v2u o = {pk2(bf_2(xv[t].x) * Dd + (f32x2){y[0], y[1]}), pk2(bf_2(xv[t].y) * Dd + (f32x2){y[2], y[3]})};
        *(LAS v2u*)(Lw + 512 * t + 32 * n + 8 * (g ^ (n >> 2))) = o;
    }
    WAIT_LGKM0();
    v4u val[4];
#pragma unroll
    for (int j = 0; j < 4; ++j) {
        const int nn = (lane >> 1) & 15, cc = nn >> 2;
        val[j] = *(LAS v4u*)(Lw + 1024 * j + 512 * (lane >> 5) + 32 * nn + 16 * ((lane & 1) ^ (cc >> 1)));
        if (cc & 1) val[j] = (v4u){val[j].z, val[j].w, val[j].x, val[j].y};
    }
#pragma unroll
    for (int j = 0; j < 4; ++j) st16_sc1(val[j], rs, WS_Y + ch * (S * 2) + j * 1024 + lane * 16);
    STAMP(10);
}

#define X_WPRE 20
#define X_WPOST 12
__device__ __forceinline__ void g2_stream(const Params& p, int panel, int ct, const W2Src& w2s, int sw, int lane STAMP_ARG) {
#pragma unroll 1
    for (int k = 6; k < 16; ++k) { WAIT_VM(X_WPRE); w2_issue(w2s, 2 * k + sw); }
    WAIT_VM(24);
    STAMP(5);
    BAR();
#pragma unroll 1
    for (int k = 16; k < 20; ++k) { WAIT_VM(X_WPOST); w2_issue(w2s, 2 * k + sw); }
    WAIT_VM(16);
    STAMP(9);
    BAR();
#pragma unroll 1
    for (int k = 20; k < 24; ++k) { WAIT_VM(X_WPOST); w2_issue(w2s, 2 * k + sw); }
    WAIT_VM(16);
    BAR();
#pragma unroll
    for (int i = 0; i < 8; ++i) {
        const int r = lane >> 3, c = lane & 7, row = 64 * sw + 8 * i + r;
        if ((i & 3) == 0) WAIT_VM(X_WPOST);
        glds16((unsigned)(row * (D * 2) + ((c ^ ((row >> 1) & 7)) * 16)), (const char*)p.q + (size_t)panel * 128 * D * 2 + ct * 128, G2_Q + (64 * sw + 8 * i) * 128);
    }
    WAIT_VM(8);
    BAR();
    WAIT_VM(0);
    BAR();
}
__device__ __forceinline__ void g2_helper(const Params& p, LAS char* L, __amdgpu_buffer_rsrc_t rs, unsigned tag, int panel, int ct, int hw, int lane STAMP_ARG) {
    gu32* ctl = (gu32*)(p.ws + WS_CTL);
    if (ct < 8) {
        GateIn gi[2];
#pragma unroll
        for (int t = 0; t < 2; ++t) gate_load(p, gi[t], panel * 128 + ct * 16 + 8 * hw + 4 * t, lane);
#pragma unroll
        for (int t = 0; t < 2; ++t) gate_finish(gi[t], rs, panel * 128 + ct * 16 + 8 * hw + 4 * t, lane);
    }
    if (hw == 0) poll_flags(ctl, W_H + panel * 48, 48, tag, lane);
    STAMP(5);
    BAR();
    WAIT_VM(0);
    BAR();
    if (ct < 8 && hw == 0 && lane == 0) set_flag(ctl, W_G + panel * 8 + ct, tag);
    BAR();
    BAR();
    {
        const int y0 = y_producer(64 * ct), ny = y_producer(64 * ct + 63) - y0 + 1;
        const int line = lane < ny ? W_Y + y0 + lane : (lane >= 16 && lane < 24 ? W_G + panel * 8 + lane - 16 : W_Y + y0);
        poll_flags(ctl, line - lane, 64, tag, lane);
    }
    STAMP(6);
    v4u yv[8];
#pragma unroll
    for (int i = 0; i < 8; ++i) {
        const int r = lane >> 4, c = lane & 15, d = 32 * hw + 4 * i + r;
        yv[i] = __builtin_amdgcn_raw_buffer_load_b128(rs, d * (S * 2) + c * 16, WS_Y + ct * 64 * S * 2 + panel * 256, AUX_SC1);
    }
    const unsigned gv = __builtin_amdgcn_raw_buffer_load_b32(rs, (hw * 64 + lane) * 4, WS_G + panel * 512, AUX_SC1);
#pragma unroll
    for (int i = 0; i < 8; ++i) {
        const int r = lane >> 4, c = lane & 15, d = 32 * hw + 4 * i + r;
        *(LAS v4u*)(L + G2_Y + d * 256 + ((c ^ ((d & 7) << 1)) * 16)) = yv[i];
    }
    *(LAS unsigned*)(L + G2_G + (hw * 64 + lane) * 4) = gv;
    WAIT_LGKM0();
    BAR();
}
__device__ __forceinline__ void g2_compute(const Params& p, LAS char* L, __amdgpu_buffer_rsrc_t rs, int panel, int ct, int off2, int w, int lane STAMP_ARG) {
    const int g = lane >> 4, q = (lane & 15) >> 2, pp = lane & 3, s = lane & 15;
    const unsigned hbase = WS_H + (panel * 8 + 2 * w) * (48 * 1024);
    int kld = off2;
    f32x4 acc[4][2];
#pragma unroll
    for (int a = 0; a < 4; ++a) { acc[a][0] = (f32x4){0.f, 0.f, 0.f, 0.f}; acc[a][1] = (f32x4){0.f, 0.f, 0.f, 0.f}; }
    v4u H[G2_HD][2][2];
    auto ldh1 = [&](int slot, int ks, int sx) {
        H[slot][ks][sx] = __builtin_amdgcn_raw_buffer_load_b128(rs, lane * 16 + sx * (48 * 1024) + ks * 1024,
            hbase + kld * 2048, H_AUX);
        asm volatile("" ::: "memory");
    };
    auto ldh = [&](int slot) {
#pragma unroll
        for (int ks = 0; ks < 2; ++ks)
#pragma unroll
            for (int sx = 0; sx < 2; ++sx) ldh1(slot, ks, sx);
        kld = kld + 1 == G2_NST ? 0 : kld + 1;
    };
    BAR();
    STAMP(5);
#pragma unroll
    for (int st = 0; st < G2_HD - 1; ++st) ldh(st);
#pragma unroll
    for (int a = 0; a < 4; ++a) H[G2_HD - 1][a >> 1][a & 1] = (v4u){0u, 0u, 0u, 0u};
    SCHED();
    v2u Alo[2][4], Ahi[2][4];
#pragma unroll
    for (int a = 0; a < 4; ++a) { Alo[1][a] = (v2u){0u, 0u}; Ahi[1][a] = (v2u){0u, 0u}; }
    unsigned aw[4];
    {
        const int sw = ((g & 1) << 1) | (q >> 1);
#pragma unroll
        for (int dt = 0; dt < 4; ++dt) aw[dt] = (unsigned)(size_t)L + (4 * g + q) * 128 + ((dt ^ sw) * 32) + 8 * pp;
    }
#define G2_A(set, dt) (v4u){Alo[set][dt].x, Alo[set][dt].y, Ahi[set][dt].x, Ahi[set][dt].y}
    auto half = [&](int set, int slot, int ks, const unsigned (&ad)[4], int off, bool load, int up, int lk) {
#pragma unroll
        for (int dt = 0; dt < 4; ++dt) {
            const v4u a = G2_A(set, dt);
            A_LGKM(6);
            A_MFMA(acc[dt][0], a, H[slot][ks][0]); A_RDTR(Alo[1 - set][dt], ad[dt], off);
            A_MFMA(acc[dt][1], a, H[slot][ks][1]); A_RDTR(Ahi[1 - set][dt], ad[dt], off + 2048);
            if (load && (dt & 1) == 0) ldh1(up, lk, dt >> 1);
        }
    };
    unsigned sb = 0;
    auto stage = [&](int u, bool load, bool drain) {
        const unsigned ad[4] = {aw[0] + sb, aw[1] + sb, aw[2] + sb, aw[3] + sb};
        const int up = (u + G2_HD - 1) % G2_HD;
        half(1, up, 1, ad, 0, load, up, 0);
        half(0, u, 0, ad, 4096, load, up, 1);
        if (load) kld = kld + 1 == G2_NST ? 0 : kld + 1;
        if (drain) WAIT_LGKM0();
        sb = sb + 8192 == G2_RING ? 0 : sb + 8192;
    };
    constexpr int NPASS = G2_NST / G2_HD;
#pragma unroll 1
    for (int jj = 0; jj < NPASS - 1; ++jj) {
#pragma unroll
        for (int u = 0; u < G2_HD; ++u) {
            const int j = G2_HD * jj + u;
            if (j == 4 || j == 8 || j == 12) BAR();
            if (j == 0) STAMP(14); else if (j == 8) STAMP(12); else if (j == 16) STAMP(13);
            stage(u, true, j == 3 || j == 7 || j == 11);
        }
    }
#pragma unroll
    for (int u = 0; u < G2_HD; ++u) stage(u, u == 0, false);
    WAIT_LGKM0();
#pragma unroll
    for (int dt = 0; dt < 4; ++dt) { const v4u a = G2_A(1, dt); A_MFMA(acc[dt][0], a, H[G2_HD - 1][1][0]); A_MFMA(acc[dt][1], a, H[G2_HD - 1][1][1]); }
#undef G2_A
    A_MFMA_DRAIN();
    STAMP(6);
    BAR();
    STAMP(7);
    {
        float gg[2]; v2u yv[2][4], qv[2][4];
#pragma unroll
        for (int sx = 0; sx < 2; ++sx) {
            const int row = 32 * w + 16 * sx + s;
            gg[sx] = *(LAS float*)(L + G2_G + row * 4);
#pragma unroll
            for (int dt = 0; dt < 4; ++dt) {
                const int d = 16 * dt + 4 * g + q;
                yv[sx][dt] = tr_read(L + G2_Y + d * 256 + (((2 * w + sx) ^ (d & 7)) * 32) + 8 * pp);
                qv[sx][dt] = *(LAS v2u*)(L + G2_Q + row * 128 + (((2 * dt + (g >> 1)) ^ ((row >> 1) & 7)) * 16) + 8 * (g & 1));
            }
        }
        SCHED();
#pragma unroll
        for (int sx = 0; sx < 2; ++sx) {
            const int row = 32 * w + 16 * sx + s;
            const float omg = 1.f - gg[sx];
#pragma unroll
            for (int dt = 0; dt < 4; ++dt) {
                const f32x4 a = acc[dt][sx];
                const f32x2 o01 = bf_2(qv[sx][dt].x) * bf_2(yv[sx][dt].x) * gg[sx] + (f32x2){a[0], a[1]} * omg;
                const f32x2 o23 = bf_2(qv[sx][dt].y) * bf_2(yv[sx][dt].y) * gg[sx] + (f32x2){a[2], a[3]} * omg;
                *(LAS v2u*)(L + G2_Q + row * 128 + (((2 * dt + (g >> 1)) ^ ((row >> 1) & 7)) * 16) + 8 * (g & 1)) = (v2u){pk2(o01), pk2(o23)};
            }
        }
    }
    WAIT_LGKM0();
#pragma unroll
    for (int i = 0; i < 4; ++i) {
        const int row = 32 * w + 8 * i + (lane >> 3), c = lane & 7;
        const v4u val = *(LAS v4u*)(L + G2_Q + row * 128 + c * 16);
        *(GAS v4u*)((char*)p.out + (size_t)(panel * 128 + row) * (D * 2) + ct * 128 + ((c ^ ((row >> 1) & 7)) * 16)) = val;
    }
    STAMP(11);
}

__global__ void __launch_bounds__(NTHR, 2) k_fused(Params p) {
    extern __shared__ __attribute__((aligned(16))) char smem[];
    LAS char* L = (LAS char*)smem;
    const int tid = threadIdx.x, lane = tid & 63, w = __builtin_amdgcn_readfirstlane(tid >> 6);
    const int x = blockIdx.x & 7, xr = x >> 1, xc = x & 1;
    const int idx = blockIdx.x >> 3;
    unsigned long long pc0;
    asm volatile("s_getpc_b64 %0" : "=s"(pc0));
    STAMP(0);
    const int panel = 4 * xr + idx / 6, ct = 6 * xc + idx % 6;
    const int off1 = 5 * ((idx / 6 + idx % 6) & 1);
    G1Src g1s;
    if (__builtin_expect(idx < 24 && (w >> 1) == 2, 1)) {
        g1_stream_start(g1s, p, panel, ct, off1, w - 4, lane);
    }
    if (idx < X_CODEPF_WGS && w == 6) {
        const char* c0 = (const char*)(pc0 & ~127ull);
        unsigned tl = 0;
#pragma unroll
        for (int r = 0; r < (X_CODEPF + 8191) / 8192; ++r) {
            const unsigned o = r * 8192 + lane * 128;
            asm volatile("global_load_dword %0, %1, %2" : "+v"(tl) : "v"(o < X_CODEPF ? o : 0u), "s"(c0));
        }
        asm volatile("s_waitcnt vmcnt(0)" : "+v"(tl));
    }
    gu32* ctl = (gu32*)(p.ws + WS_CTL);
    const __amdgpu_buffer_rsrc_t rs = __builtin_amdgcn_make_buffer_rsrc(p.ws, 0, WS_END, 0x00020000);
#define GET_TAG() const unsigned tag = __builtin_amdgcn_readfirstlane(__hip_atomic_load(ctl + W_EPOCH * FL, RLX_AGENT)) + 1u
    if (idx < 24) {
        const bool typeA = idx < 20;
        const int panel2 = 4 * xr + idx / 5, ct2 = 5 * xc + idx % 5;
        const int off2 = ((idx % 5) * 24 / 5 + (idx / 5) * 6) % 24;
        const int bw = x * 4 + idx - 20;
        if (typeA) {
            if (w < 4) {
                GET_TAG();
                ConvIn cin;
                g1_compute<true>(p, L, rs, panel, ct, w, lane, cin, 0, L STAMP_PASS);
                STAMP(3);
                BAR();
                WAIT_VM(0);
                if (lane == 0) set_flag(ctl, W_H + (panel * 12 + ct) * 4 + w, tag);
                STAMP(4);
                g2_compute(p, L, rs, panel2, ct2, off2, w, lane STAMP_PASS);
            } else if (w < 6) {
                __builtin_amdgcn_s_setprio(2);
                W2Src w2s;
                w2_init(w2s, p, ct2, off2, lane);
                g1_stream<true>(g1s, w2s, w - 4 STAMP_PASS);
                BAR();
                g2_stream(p, panel2, ct2, w2s, w - 4, lane STAMP_PASS);
            } else {
                GET_TAG();
#pragma unroll 1
                for (int i = 0; i < G1_NST; ++i) BAR();
                BAR();
                g2_helper(p, L, rs, tag, panel2, ct2, w - 6, lane STAMP_PASS);
            }
        } else {
            constexpr int B_DONE = 32704;
            const int ch = 448 + 6 * bw + (w >= 6 ? w - 6 : 2 + (w & 3));
            ConvIn cin;
            if (w < 4) {
                GET_TAG();
                if (w == 0 && lane < 4) *(LAS unsigned*)(L + B_DONE + 4 * lane) = 0u;
                WAIT_LGKM0();
                g1_compute<false>(p, L, rs, panel, ct, w, lane, cin, ch, L + B_DONE STAMP_PASS);
                STAMP(3);
                conv_load_h(p, cin, ch, lane);
                WAIT_VM(12);
                if (lane == 0) set_flag(ctl, W_H + (panel * 12 + ct) * 4 + w, tag);
                __builtin_amdgcn_s_setprio(0);
                STAMP(4);
                poll_flags(ctl, W_KV, 64, tag, lane);
                conv_load_x(cin, rs, ch, lane);
#pragma unroll 1
                for (unsigned spins = 0; spins < (1u << 22); ++spins) {
                    const v4u d = *(volatile LAS v4u*)(L + B_DONE);
                    if (__builtin_amdgcn_readfirstlane(d.x & d.y & d.z & d.w) != 0u) break;
                }
                asm volatile("" ::: "memory");
                conv_channel(p, L + 75264 + w * CV_PAIR, rs, cin, ch, -1, lane STAMP_PASS);
                WAIT_VM(0);
                BAR();
                if (tid == 0) set_flag(ctl, W_Y + 64 + bw, tag);
                STAMP(16);
            } else if (w < 6) {
                __builtin_amdgcn_s_setprio(2);
                W2Src w2s;
                g1_stream<false>(g1s, w2s, w - 4 STAMP_PASS);
                BAR();
            } else {
                GET_TAG();
                conv_load_h(p, cin, ch, lane);
#pragma unroll 1
                for (int i = 0; i < G1_NST; ++i) BAR();
                poll_flags(ctl, W_KV, 64, tag, lane);
                conv_load_x(cin, rs, ch, lane);
                conv_channel(p, L + 32768 + (w - 6) * CV_PAIR, rs, cin, ch, -1, lane STAMP_PASS);
                WAIT_VM(0);
                BAR();
            }
        }
    } else {
        const int cw = x * 8 + (idx - 24);
        constexpr int nch = 7;
        const int ch = 7 * cw + (w < nch ? w : 0);
        ConvIn cin;
        unsigned epoch_v = __hip_atomic_load(ctl + W_EPOCH * FL, RLX_AGENT);
        prepass(p, L, rs, cw, tid STAMP_PASS);
        STAMP(2);
        WAIT_VM(0);
        asm volatile("" : "+v"(epoch_v));
        const unsigned tag = __builtin_amdgcn_readfirstlane(epoch_v) + 1u;
        BAR();
        if (tid == 0) set_flag(ctl, W_KV + cw, tag);
        STAMP(3);
        if (w < nch) conv_load_h(p, cin, ch, lane);
        if (w == 7) poll_flags(ctl, W_KV, 64, tag, lane);
        BAR();
        STAMP(4);
        if (w == 7 && x == 7 && idx == 31) {
#pragma unroll 1
            for (unsigned spins = 0; spins < kSpinCap; ++spins) {
                unsigned ok = 1;
#pragma unroll
                for (int r = 0; r < 3; ++r) ok &= __hip_atomic_load(ctl + (W_H + (r * 64 + lane) * 4) * FL, RLX_AGENT) == tag;
                if (__all(ok)) break;
                __builtin_amdgcn_s_sleep(16);
            }
            if (lane == 0) set_flag(ctl, W_EPOCH, tag);
        }
        if (w < nch) {
            conv_load_x(cin, rs, ch, lane);
            conv_channel(p, L + w * CV_PAIR, rs, cin, ch, -1, lane STAMP_PASS);
        }
        WAIT_VM(0);
        BAR();
        if (tid == 0) set_flag(ctl, W_Y + cw, tag);
        STAMP(16);
    }
    STAMP_FLUSH();
}

extern "C" void kernel_launch(void* const* d_in, const int* in_sizes, int n_in, void* d_out, int out_size, void* d_ws, size_t ws_size, hipStream_t stream) {
    Params p;
    p.q = (const u16*)d_in[0]; p.k = (const u16*)d_in[1]; p.v = (const u16*)d_in[2]; p.shk = (const u16*)d_in[3];
    p.ssm = (const u16*)d_in[4]; p.Dv = (const u16*)d_in[5]; p.w1 = (const u16*)d_in[7]; p.w2 = (const u16*)d_in[8]; p.gw = (const u16*)d_in[9];
    p.out = (u16*)d_out; p.ws = (char*)d_ws;
    (void)hipFuncSetAttribute((const void*)k_fused, hipFuncAttributeMaxDynamicSharedMemorySize, LDS_BYTES);
    k_fused<<<256, NTHR, LDS_BYTES, stream>>>(p);
}
```
